```python
import torch

def _softmax(x, axis=-1):
    return torch.softmax(x, dim=axis)
def _gelu(x):
    return torch.nn.functional.gelu(x, approximate="tanh")
def _silu(x):
    return torch.nn.functional.silu(x)
def _sigmoid(x):
    return torch.sigmoid(x)
def _layernorm(x, axis=-1, eps=1e-5):
    m = x.mean(dim=axis, keepdim=True)
    v = x.var(dim=axis, keepdim=True, unbiased=False)
    return (x - m) / torch.sqrt(v + eps)
def _scatter_add(tgt, idx, src, axis=0):
    return tgt.clone().index_add_(axis, idx, src)
def _segment_sum(src, seg, n):
    out = torch.zeros((n,) + src.shape[1:], dtype=src.dtype)
    return out.index_add_(0, seg, src)
def _pad(x, pw):
    flat = tuple(v for pair in reversed(pw) for v in pair)
    return torch.nn.functional.pad(x, flat)

kernel_name = 'gated_mix__linear_attn_block__linear_attn_block__prompt'

def setup_inputs():

    _w = (lambda a: (lambda t: t.to(torch.bfloat16) if t.is_floating_point() else t)(torch.as_tensor(a)))
    def _A_setup():
        B, S, D = 1, 2048, 512
        return {
            "x": _w((lambda *s: torch.randn(*s, dtype=torch.bfloat16) * 0.05)(B, S, D)),
            "wqkv": _w((lambda *s: torch.randn(*s, dtype=torch.bfloat16) * 0.8)(D, 3 * D)),
            "wo": _w((lambda *s: torch.randn(*s, dtype=torch.bfloat16) * 0.05)(D, D)),
        }
    _A_in = _A_setup()

    def _B_setup():
        B, S, D = 1, 2048, 512
        return {
            "x": _w((lambda *s: torch.randn(*s, dtype=torch.bfloat16) * 0.05)(B, S, D)),
            "wqkv": _w((lambda *s: torch.randn(*s, dtype=torch.bfloat16) * 0.8)(D, 3 * D)),
            "wo": _w((lambda *s: torch.randn(*s, dtype=torch.bfloat16) * 0.05)(D, D)),
        }
    _B_in = _B_setup()

    return {
        "A_x": _A_in["x"],
        "A_wqkv": _A_in["wqkv"],
        "A_wo": _A_in["wo"],
        "B_x": _B_in["x"],
        "B_wqkv": _B_in["wqkv"],
        "B_wo": _B_in["wo"],
        "gate_w": _w((lambda *s: torch.randn(*s, dtype=torch.bfloat16) * 0.05)(512) * 16.0),
    }

def reference(A_x, A_wqkv, A_wo, B_x, B_wqkv, B_wo, gate_w):
    def _A_ref(x, wqkv, wo):
        B, S, D = x.shape
        H = 32; Dh = D // H
        qkv = (lambda x, *d: x.permute(*d))((x @ wqkv).reshape(B, S, 3, H, Dh), 2, 0, 3, 1, 4)
        q, k, v = qkv[0], qkv[1], qkv[2]
        phi = lambda t: torch.where(t > 0, t + 1, torch.exp(t)) + 1e-6
        pq, pk = phi(q), phi(k)
        kv = torch.einsum("bhsd,bhse->bhde", pk, v)
        z = torch.einsum("bhsd->bhd", pk)
        num = torch.einsum("bhqd,bhde->bhqe", pq, kv)
        den = torch.einsum("bhqd,bhd->bhq", pq, z)[..., None] + 1e-6
        o = num / den
        return (lambda x, *d: x.permute(*d))(o, 0, 2, 1, 3).reshape(B, S, D) @ wo

    def _B_ref(x, wqkv, wo):
        B, S, D = x.shape
        H = 32; Dh = D // H
        qkv = (lambda x, *d: x.permute(*d))((x @ wqkv).reshape(B, S, 3, H, Dh), 2, 0, 3, 1, 4)
        q, k, v = qkv[0], qkv[1], qkv[2]
        phi = lambda t: torch.where(t > 0, t + 1, torch.exp(t)) + 1e-6
        pq, pk = phi(q), phi(k)
        kv = torch.einsum("bhsd,bhse->bhde", pk, v)
        z = torch.einsum("bhsd->bhd", pk)
        num = torch.einsum("bhqd,bhde->bhqe", pq, kv)
        den = torch.einsum("bhqd,bhd->bhq", pq, z)[..., None] + 1e-6
        o = num / den
        return (lambda x, *d: x.permute(*d))(o, 0, 2, 1, 3).reshape(B, S, D) @ wo

    x = A_x
    g = _sigmoid((x * gate_w).sum(-1, keepdims=True))
    a = _A_ref(x, A_wqkv, A_wo)
    b = _B_ref(x, B_wqkv, B_wo)
    return g * a + (1 - g) * b
```

```cpp
#include <hip/hip_runtime.h>
#include <hip/hip_bf16.h>
typedef __hip_bfloat16 bf16;
constexpr int S=2048, D=512, H=32, DH=16;
__device__ inline float b2f(bf16 v){ return __bfloat162float(v); }
__device__ inline float rb(float v){ return __bfloat162float(__float2bfloat16(v)); }
__global__ void k_qkv(const bf16* x, const bf16* wa, const bf16* wb, float* qkv){
  int br = blockIdx.z; const bf16* w = br? wb: wa;
  int n = blockIdx.x*64 + (threadIdx.x&63);
  int s = blockIdx.y*4 + (threadIdx.x>>6);
  float acc=0;
  for(int k=0;k<D;k++) acc += b2f(x[s*D+k])*b2f(w[k*1536+n]);
  acc = rb(acc);
  if(n<1024){ acc = rb(rb(acc>0? acc+1.f : expf(acc)) + 1e-6f); }
  qkv[((size_t)br*S+s)*1536+n]=acc;
}
__global__ void k_kv(const float* qkv, float* kv, float* z){
  int br = blockIdx.y, h = blockIdx.x; int d = threadIdx.x>>4, e = threadIdx.x&15;
  float acc=0, zz=0;
  for(int s=0;s<S;s++){ const float* r = qkv+((size_t)br*S+s)*1536; float pk=r[512+h*16+d]; acc += pk*r[1024+h*16+e]; zz+=pk; }
  kv[((br*H+h)*16+d)*16+e]=rb(acc);
  if(e==0) z[(br*H+h)*16+d]=rb(zz);
}
__global__ void k_o(const float* qkv, const float* kv, const float* z, float* o){
  int br = blockIdx.y; int s = blockIdx.x; int c = threadIdx.x; int h=c>>4, e=c&15;
  const float* r = qkv+((size_t)br*S+s)*1536;
  float num=0, den=0;
  for(int d=0;d<16;d++){ float pq=r[h*16+d]; num+=pq*kv[((br*H+h)*16+d)*16+e]; den+=pq*z[(br*H+h)*16+d]; }
  num=rb(num); den=rb(rb(den)+1e-6f);
  o[((size_t)br*S+s)*D+c]=rb(num/den);
}
__global__ void k_out(const float* o, const bf16* x, const bf16* woa, const bf16* wob, const bf16* gw, bf16* out){
  int s=blockIdx.x; int n=threadIdx.x;
  __shared__ float red[512];
  red[n]=rb(b2f(x[s*D+n])*b2f(gw[n])); __syncthreads();
  for(int st=256;st>0;st>>=1){ if(n<st) red[n]+=red[n+st]; __syncthreads(); }
  float gs=rb(red[0]); float g=rb(1.f/(1.f+expf(-gs)));
  float a=0,b=0;
  for(int k=0;k<D;k++){ a+=o[(size_t)s*D+k]*b2f(woa[k*D+n]); b+=o[((size_t)S+s)*D+k]*b2f(wob[k*D+n]); }
  a=rb(a); b=rb(b);
  out[s*D+n]=__float2bfloat16(rb(g*a)+rb(rb(1.f-g)*b));
}
extern "C" void kernel_launch(void* const* d_in, const int* in_sizes, int n_in, void* d_out, int out_size, void* d_ws, size_t ws_size, hipStream_t stream){
  const bf16* x=(const bf16*)d_in[0]; const bf16* wa=(const bf16*)d_in[1]; const bf16* woa=(const bf16*)d_in[2];
  const bf16* wb=(const bf16*)d_in[4]; const bf16* wob=(const bf16*)d_in[5]; const bf16* gw=(const bf16*)d_in[6];
  float* qkv=(float*)d_ws; float* kv=qkv+(size_t)2*S*1536; float* z=kv+2*H*256; float* o=z+2*H*16;
  k_qkv<<<dim3(24,S/4,2),256,0,stream>>>(x,wa,wb,qkv);
  k_kv<<<dim3(H,2),256,0,stream>>>(qkv,kv,z);
  k_o<<<dim3(S,2),512,0,stream>>>(qkv,kv,z,o);
  k_out<<<S,512,0,stream>>>(o,x,woa,wob,gw,(bf16*)d_out);
}
```

```cpp
#include <hip/hip_runtime.h>
#include <hip/hip_bf16.h>
#include <stdint.h>

#define LAS __attribute__((address_space(3)))
#define GAS __attribute__((address_space(1)))
#define DI __device__ __forceinline__

typedef unsigned short u16;
typedef __bf16 bf16x8 __attribute__((ext_vector_type(8)));
typedef __bf16 bf16x4 __attribute__((ext_vector_type(4)));
typedef __bf16 bf16x2 __attribute__((ext_vector_type(2)));
typedef float f32x16 __attribute__((ext_vector_type(16)));
typedef float f32x4 __attribute__((ext_vector_type(4)));
typedef float f32x2 __attribute__((ext_vector_type(2)));
typedef unsigned u32x4 __attribute__((ext_vector_type(4)));
typedef unsigned u32x2 __attribute__((ext_vector_type(2)));
typedef __amdgpu_buffer_rsrc_t rsrc_t;

#define ROT 0

constexpr int S_ = 2048, D_ = 512;
constexpr unsigned XR = 0;
constexpr unsigned WR = 49152;
constexpr unsigned WQ = 98304;
constexpr unsigned WOA = 0;
constexpr unsigned WOB = 98304;
constexpr unsigned SCR = 81920;
constexpr unsigned AFR = SCR + 12288;
constexpr unsigned EXC = SCR;
constexpr unsigned LDS_BYTES = 163840;
constexpr unsigned WS_EPOCH = 0;
constexpr unsigned WS_FLAGA = 4096;
constexpr unsigned WS_FLAGB = WS_FLAGA + 1024 * 128;
constexpr unsigned WS_GATE = WS_FLAGB + 1024 * 128;
constexpr unsigned WS_PART = WS_GATE + 8192;
constexpr unsigned PART_STRIDE = 1152;
constexpr unsigned WS_O = WS_PART + 1024 * PART_STRIDE;
constexpr unsigned WS_END = WS_O + 64 * 64 * 1024;
constexpr unsigned SPIN_CAP = 1u << 18;

#define ST(i) do {} while (0)
#define STR(i) do {} while (0)
#define STFLUSH() do {} while (0)

DI void glds(unsigned voff, rsrc_t rs, unsigned soff, unsigned ldsaddr) {
  asm volatile("s_mov_b32 m0, %0\n\ts_nop 0\n\tbuffer_load_dwordx4 %1, %2, %3 offen lds"
               :: "s"(ldsaddr), "v"(voff), "s"(rs), "s"(soff) : "memory");
}
#define WAITV(n) asm volatile("s_waitcnt vmcnt(" #n ")" ::: "memory")
#define WAITL0() asm volatile("s_waitcnt lgkmcnt(0)" ::: "memory")
#define BAR() asm volatile("s_barrier" ::: "memory")

DI f32x16 mfma(bf16x8 a, bf16x8 b, f32x16 c) { return __builtin_amdgcn_mfma_f32_32x32x16_bf16(a, b, c, 0, 0, 0); }
DI bf16x8 ldsr128(LAS char* L, unsigned a) { return *(LAS bf16x8*)(L + a); }
DI bf16x8 ldstr(LAS char* L, unsigned a0, unsigned a1) {
  bf16x4 lo = __builtin_amdgcn_ds_read_tr16_b64_v4bf16((LAS bf16x4*)(L + a0));
  bf16x4 hi = __builtin_amdgcn_ds_read_tr16_b64_v4bf16((LAS bf16x4*)(L + a1));
  return __builtin_shufflevector(lo, hi, 0, 1, 2, 3, 4, 5, 6, 7);
}
DI bf16x2 cvt2(float a, float b) { f32x2 p = {a, b}; return __builtin_convertvector(p, bf16x2); }
DI bf16x8 pack8(float a0, float a1, float a2, float a3, float a4, float a5, float a6, float a7) {
  bf16x2 p0 = cvt2(a0, a1), p1 = cvt2(a2, a3), p2 = cvt2(a4, a5), p3 = cvt2(a6, a7);
  bf16x4 lo = __builtin_shufflevector(p0, p1, 0, 1, 2, 3), hi = __builtin_shufflevector(p2, p3, 0, 1, 2, 3);
  return __builtin_shufflevector(lo, hi, 0, 1, 2, 3, 4, 5, 6, 7);
}
DI float phi(float t) {
  float e = __builtin_amdgcn_exp2f(t * 1.44269504088896340736f);
  e = __builtin_amdgcn_fmed3f(e, 0.0f, 1.0f);
  return (fmaxf(t, 0.0f) + e) + 1e-6f;
}
DI bf16x8 phi8(const f32x16& x, int s) {
  return pack8(phi(x[8 * s]), phi(x[8 * s + 1]), phi(x[8 * s + 2]), phi(x[8 * s + 3]),
               phi(x[8 * s + 4]), phi(x[8 * s + 5]), phi(x[8 * s + 6]), phi(x[8 * s + 7]));
}
DI bf16x8 cvt8(const f32x16& x, int s) {
  return pack8(x[8 * s], x[8 * s + 1], x[8 * s + 2], x[8 * s + 3], x[8 * s + 4], x[8 * s + 5], x[8 * s + 6], x[8 * s + 7]);
}
DI float sum8(bf16x8 v, float acc) {
  const bf16x2 ones = {(__bf16)1.0f, (__bf16)1.0f};
  acc = __builtin_amdgcn_fdot2_f32_bf16(__builtin_shufflevector(v, v, 0, 1), ones, acc, false);
  acc = __builtin_amdgcn_fdot2_f32_bf16(__builtin_shufflevector(v, v, 2, 3), ones, acc, false);
  acc = __builtin_amdgcn_fdot2_f32_bf16(__builtin_shufflevector(v, v, 4, 5), ones, acc, false);
  acc = __builtin_amdgcn_fdot2_f32_bf16(__builtin_shufflevector(v, v, 6, 7), ones, acc, false);
  return acc;
}
DI unsigned long long ld_flag(GAS char* ws, unsigned off) {
  return __hip_atomic_load((GAS unsigned long long*)(ws + off), __ATOMIC_RELAXED, __HIP_MEMORY_SCOPE_AGENT);
}
DI void st_flag(GAS char* ws, unsigned off, unsigned long long v) {
  __hip_atomic_store((GAS unsigned long long*)(ws + off), v, __ATOMIC_RELAXED, __HIP_MEMORY_SCOPE_AGENT);
}
DI unsigned long long load_epoch(const char* wsp) {
  unsigned long long e;
  asm volatile("s_load_dwordx2 %0, %1, 0x0\n\ts_waitcnt lgkmcnt(0)" : "=s"(e) : "s"(wsp) : "memory");
  return e;
}
DI void wait16(GAS char* ws, unsigned off0, unsigned long long tag, int lane) {
  const unsigned off = off0 + (unsigned)(lane & 15) * 128u;
  for (unsigned spins = 0; spins < SPIN_CAP; ++spins) {
    const unsigned long long v = ld_flag(ws, off);
    if (__all(v == tag)) break;
    __builtin_amdgcn_s_sleep(1);
  }
  asm volatile("" ::: "memory");
}

__global__ __launch_bounds__(512, 2) void gated_mix_fused(const u16* __restrict__ x, const u16* __restrict__ wA, const u16* __restrict__ wB,
                                                          const u16* __restrict__ woA, const u16* __restrict__ woB, const u16* __restrict__ gw,
                                                          u16* __restrict__ out, char* __restrict__ wsp
                                                          ) {
  extern __shared__ __attribute__((aligned(16))) char lds[];
  LAS char* L = (LAS char*)lds;
  const int tid = threadIdx.x;
  const int wave = __builtin_amdgcn_readfirstlane(tid >> 6), lane = tid & 63;
  const int b = blockIdx.x, xcd = b & 7, r = b >> 3;
  const int ti = (xcd & 1) * 8 + (r & 7), g = (xcd >> 1) * 4 + (r >> 3);
  const int gg = g & 7;
  const int rt = 4 * xcd + (r & 3), ct = r >> 2;
  GAS char* ws = (GAS char*)wsp;
  STR(0); ST(1);

  if (wave >= 4) {
    const int hw = wave - 4;
    int ro = 0;
    if (ROT == 1) ro = (ti + 2 * g) & 7;
    if (ROT == 2) ro = 2 * ((ti + g) & 1);
    if (ROT == 3) ro = 4 * ((ti + g) & 1);
    if (ROT == 5) ro = ((ti + g) % 3) * 2;
    const u16* w = (g >= 8) ? wB : wA;
    const rsrc_t rsX = __builtin_amdgcn_make_buffer_rsrc((void*)x, 0, S_ * D_ * 2, 0x00020000);
    const rsrc_t rsW = __builtin_amdgcn_make_buffer_rsrc((void*)w, 0, D_ * 3 * D_ * 2, 0x00020000);
    const int rin = lane >> 3, ch = lane & 7;
    const unsigned vx = rin * 1024 + ((ch ^ (4 * (hw & 1) + (rin >> 1))) << 4);
    const unsigned csw = (ch ^ (((rin >> 1) & 1) << 2)) << 4;
    const unsigned vw = rin * 3072 + csw;
    const unsigned sx0 = (ti * 128 + 8 * hw) * 1024;
    const unsigned sw0 = 8 * hw * 3072 + gg * 128;
    auto stage = [&](int s) {
      const unsigned ks = (s + ro) & 7, slot = (s % 3) * 16384;
#pragma unroll
      for (int i = 0; i < 4; ++i) glds(vx, rsX, sx0 + ks * 128 + i * 32768, XR + slot + (hw + 4 * i) * 1024);
#pragma unroll
      for (int i = 0; i < 2; ++i) glds(vw, rsW, sw0 + ks * 196608 + i * 98304 + 1024, WR + slot + (hw + 4 * i) * 1024);
#pragma unroll
      for (int i = 0; i < 2; ++i) glds(vw, rsW, sw0 + ks * 196608 + i * 98304 + 2048, WR + slot + 8192 + (hw + 4 * i) * 1024);
    };
    auto stageq = [&](int s) {
      const unsigned ks = (s + ro) & 7;
#pragma unroll
      for (int i = 0; i < 2; ++i) glds(vw, rsW, sw0 + ks * 196608 + i * 98304, WQ + s * 8192 + (hw + 4 * i) * 1024);
    };
    stage(0); stage(1);
    ST(2);
    const unsigned long long tag = load_epoch(wsp) + 1ull;
    WAITV(8);
    BAR();
    ST(3);
#pragma unroll
    for (int s = 1; s < 8; ++s) {
      if (s + 1 < 8) { stage(s + 1); WAITV(8); }
      else { stageq(0); stageq(1); stageq(2); stageq(3); WAITV(8); }
      BAR();
    }
    ST(4);
    stageq(4); stageq(5); stageq(6); stageq(7);
    {
      const rsrc_t rsG = __builtin_amdgcn_make_buffer_rsrc((void*)gw, 0, D_ * 2, 0x00020000);
      const unsigned row = ti * 128 + 8 * g + 2 * hw;
      u32x4 x0, x1, gv;
      asm volatile("buffer_load_dwordx4 %0, %3, %4, %6 offen\n\tbuffer_load_dwordx4 %1, %3, %4, %6 offen offset:1024\n\t"
                   "buffer_load_dwordx4 %2, %3, %5, 0 offen\n\ts_waitcnt vmcnt(0)"
                   : "=&v"(x0), "=&v"(x1), "=&v"(gv) : "v"(lane * 16), "s"(rsX), "s"(rsG), "s"(row * 1024) : "memory");
      float a0 = 0.f, a1 = 0.f;
#pragma unroll
      for (int i = 0; i < 4; ++i) {
        const unsigned ug = gv[i], u0 = x0[i], u1 = x1[i];
        const bf16x2 gp = __builtin_bit_cast(bf16x2, ug);
        a0 = __builtin_amdgcn_fdot2_f32_bf16(__builtin_bit_cast(bf16x2, u0), gp, a0, false);
        a1 = __builtin_amdgcn_fdot2_f32_bf16(__builtin_bit_cast(bf16x2, u1), gp, a1, false);
      }
#pragma unroll
      for (int m = 32; m >= 1; m >>= 1) { a0 += __shfl_xor(a0, m); a1 += __shfl_xor(a1, m); }
      const float lg = (lane & 1) ? a1 : a0;
      const float gval = __builtin_amdgcn_rcpf(1.0f + __builtin_amdgcn_exp2f(-1.44269504088896340736f * lg));
      if (lane < 2) __hip_atomic_store((GAS float*)(ws + WS_GATE) + row + lane, gval, __ATOMIC_RELAXED, __HIP_MEMORY_SCOPE_AGENT);
    }
    ST(5);
    BAR();
    ST(6);
    const rsrc_t rsWS = __builtin_amdgcn_make_buffer_rsrc((void*)wsp, 0, WS_END, 0x00020000);
    const unsigned hid = g * 4 + hw;
    {
      const f32x4 p0 = *(LAS f32x4*)(L + SCR + hw * 1024 + lane * 16), p1 = *(LAS f32x4*)(L + SCR + (4 + hw) * 1024 + lane * 16);
      const f32x4 z0 = *(LAS f32x4*)(L + SCR + (8 + hw) * 1024 + (lane & 3) * 16), z1 = *(LAS f32x4*)(L + SCR + (8 + hw) * 1024 + 64 + (lane & 3) * 16);
      const unsigned po = WS_PART + (hid * 16 + ti) * PART_STRIDE;
      __builtin_amdgcn_raw_buffer_store_b128(__builtin_bit_cast(u32x4, p0 + p1), rsWS, lane * 16, po, 16);
      if (lane < 4) __builtin_amdgcn_raw_buffer_store_b128(__builtin_bit_cast(u32x4, z0 + z1), rsWS, 1024 + lane * 16, po, 16);
      WAITV(0);
      if (lane == 0) st_flag(ws, WS_FLAGA + (hid * 16 + ti) * 128, tag);
    }
    ST(7);
    const rsrc_t rsOA = __builtin_amdgcn_make_buffer_rsrc((void*)woA, 0, D_ * D_ * 2, 0x00020000);
    const rsrc_t rsOB = __builtin_amdgcn_make_buffer_rsrc((void*)woB, 0, D_ * D_ * 2, 0x00020000);
    const unsigned vo = rin * 1024 + csw;
#pragma unroll
    for (int i = 0; i < 16; ++i) glds(vo, rsOA, (hw + 4 * i) * 8192 + ct * 128, WOA + (hw + 4 * i) * 1024);
    ST(8);
    wait16(ws, WS_FLAGA + hid * 16 * 128, tag, lane);
    ST(9);
    {
      const unsigned pb = WS_PART + hid * 16 * PART_STRIDE;
      u32x4 pv[16];
#pragma unroll
      for (int p = 0; p < 16; ++p) pv[p] = __builtin_amdgcn_raw_buffer_load_b128(rsWS, lane * 16, pb + p * PART_STRIDE, 16);
      const u32x4 zv = __builtin_amdgcn_raw_buffer_load_b128(rsWS, (lane >> 2) * PART_STRIDE + 1024 + (lane & 3) * 16, pb, 16);
      f32x4 a = __builtin_bit_cast(f32x4, pv[0]);
#pragma unroll
      for (int p = 1; p < 16; ++p) a += __builtin_bit_cast(f32x4, pv[p]);
      f32x4 z = __builtin_bit_cast(f32x4, zv);
#pragma unroll
      for (int m = 4; m <= 32; m <<= 1) {
        z[0] += __shfl_xor(z[0], m); z[1] += __shfl_xor(z[1], m); z[2] += __shfl_xor(z[2], m); z[3] += __shfl_xor(z[3], m);
      }
      const int slot = lane & 31, jh = lane >> 5;
      u32x2 ka = {__builtin_bit_cast(unsigned, cvt2(a[0], a[1])), __builtin_bit_cast(unsigned, cvt2(a[2], a[3]))};
      *(LAS u32x2*)(L + AFR + hw * 1024 + ((slot >> 4) * 32 + (slot & 15)) * 16 + jh * 8) = ka;
      const int q4 = lane & 3;
      u32x2 kz = {__builtin_bit_cast(unsigned, cvt2(z[0], z[1])), __builtin_bit_cast(unsigned, cvt2(z[2], z[3]))};
      *(LAS u32x2*)(L + AFR + hw * 1024 + ((q4 & 1) * 32 + 16 + (lane >> 2)) * 16 + (q4 >> 1) * 8) = kz;
    }
    WAITL0();
    ST(10);
    BAR();
    ST(11);
#pragma unroll
    for (int i = 0; i < 16; ++i) glds(vo, rsOB, (hw + 4 * i) * 8192 + ct * 128, WOB + (hw + 4 * i) * 1024);
    WAITV(0);
    ST(12);
    BAR();
    STR(16); STFLUSH();
    return;
  }

  const int wr = wave >> 1, wc = wave & 1;
  const int r32 = lane & 31, h = lane >> 5;
  const int g1 = (lane >> 4) & 1, q = (lane & 15) >> 2, p = lane & 3;
  __builtin_amdgcn_s_setprio(1);
  const int own = 2 * wr + wc, oth = 2 * wr + 1 - wc;
  const unsigned fx = (h ^ ((r32 >> 1) & 7)) << 4;
  const unsigned xo0 = XR + (own * 32 + r32) * 128 + fx, xt0 = XR + (oth * 32 + r32) * 128 + fx;
  const unsigned sw = ((2 * g1 + (p >> 1)) ^ (((q >> 1) & 1) << 2)) << 4;
  const unsigned wn0 = (8 * h + q) * 128 + sw + (p & 1) * 8;
  const unsigned wkv0 = WR + (wn0 ^ (wc << 6));

  bf16x8 xr[32];
  f32x16 kO = {}, kT = {}, vO = {}, vT = {};
  const unsigned long long tag = load_epoch(wsp) + 1ull;
  {
    BAR();
    ST(3);
    bf16x8 xt = ldsr128(L, xt0), wk = ldstr(L, wkv0, wkv0 + 512), wv = ldstr(L, wkv0 + 8192, wkv0 + 8192 + 512);
    xr[0] = ldsr128(L, xo0);
#pragma unroll
    for (int t = 0; t < 32; ++t) {
      bf16x8 nxt, nwk, nwv;
      if (t + 1 < 32) {
        const int s1 = (t + 1) >> 2, k1 = (t + 1) & 3;
        const unsigned slot = (s1 % 3) * 16384;
        if (k1 == 0) { WAITL0(); BAR(); }
        xr[t + 1] = ldsr128(L, (xo0 ^ (k1 << 5)) + slot);
        nxt = ldsr128(L, (xt0 ^ (k1 << 5)) + slot);
        nwk = ldstr(L, wkv0 + slot + k1 * 2048, wkv0 + slot + k1 * 2048 + 512);
        nwv = ldstr(L, wkv0 + slot + 8192 + k1 * 2048, wkv0 + slot + 8192 + k1 * 2048 + 512);
      }
      __builtin_amdgcn_sched_barrier(0);
      kO = mfma(xr[t], wk, kO);
      kT = mfma(xt, wk, kT);
      vO = mfma(xr[t], wv, vO);
      vT = mfma(xt, wv, vT);
      __builtin_amdgcn_sched_barrier(0);
      if (t + 1 < 32) { xt = nxt; wk = nwk; wv = nwv; }
    }
  }
  ST(4);
  {
    f32x16 kv = {};
    float zs = 0.f;
#pragma unroll
    for (int s = 0; s < 2; ++s) {
      const bf16x8 pa = phi8(kO, s), pb = phi8(kT, s);
      kv = mfma(pa, cvt8(vO, s), kv);
      kv = mfma(pb, cvt8(vT, s), kv);
      zs = sum8(pa, zs); zs = sum8(pb, zs);
    }
    zs += __shfl_xor(zs, 32);
    f32x4 lo, hi;
#pragma unroll
    for (int j = 0; j < 4; ++j) { lo[j] = g1 ? kv[8 + j] : kv[j]; hi[j] = g1 ? kv[12 + j] : kv[4 + j]; }
    const int hh = 2 * wc + g1;
    const unsigned a = SCR + (4 * wr + hh) * 1024 + (h * 16 + (lane & 15)) * 16;
    *(LAS f32x4*)(L + a) = lo;
    *(LAS f32x4*)(L + a + 512) = hi;
    if (h == 0) *(LAS float*)(L + SCR + (8 + hh) * 1024 + wr * 64 + (lane & 15) * 4) = zs;
  }
  WAITL0();
  ST(5);
  BAR();
  ST(6);
  f32x16 qa = {}, qb = {};
  {
    const unsigned wq0 = WQ + wn0, wq1 = WQ + (wn0 ^ 64);
    bf16x8 a0 = ldstr(L, wq0, wq0 + 512), a1 = ldstr(L, wq1, wq1 + 512);
#pragma unroll
    for (int t = 0; t < 32; ++t) {
      bf16x8 n0, n1;
      if (t + 1 < 32) {
        const unsigned o = (t + 1) * 2048;
        n0 = ldstr(L, wq0 + o, wq0 + o + 512);
        n1 = ldstr(L, wq1 + o, wq1 + o + 512);
      }
      __builtin_amdgcn_sched_barrier(0);
      qa = mfma(a0, xr[t], qa);
      qb = mfma(a1, xr[t], qb);
      __builtin_amdgcn_sched_barrier(0);
      if (t + 1 < 32) { a0 = n0; a1 = n1; }
    }
  }
  ST(7);
  bf16x8 pq[4];
  pq[0] = phi8(qa, 0); pq[1] = phi8(qa, 1); pq[2] = phi8(qb, 0); pq[3] = phi8(qb, 1);
  const rsrc_t rsWS = __builtin_amdgcn_make_buffer_rsrc((void*)wsp, 0, WS_END, 0x00020000);
  WAITL0();
  ST(8);
  BAR();
  ST(9);
  {
    const unsigned st = 4 * ti + own;
#pragma unroll
    for (int hh = 0; hh < 4; ++hh) {
      const bf16x8 af = ldsr128(L, AFR + hh * 1024 + lane * 16);
      f32x16 d = {};
      d = mfma(af, pq[hh], d);
      const float inv = __builtin_amdgcn_rcpf(d[8] + 1e-6f);
      const bf16x8 o = pack8(d[0] * inv, d[1] * inv, d[2] * inv, d[3] * inv, d[4] * inv, d[5] * inv, d[6] * inv, d[7] * inv);
      __builtin_amdgcn_raw_buffer_store_b128(__builtin_bit_cast(u32x4, o), rsWS, lane * 16, WS_O + (st * 64 + 4 * g + hh) * 1024, 16);
    }
    ST(10);
    WAITV(0);
    if (lane == 0) st_flag(ws, WS_FLAGB + (st * 16 + g) * 128, tag);
  }
  ST(11);
  BAR();
  ST(12);
  {
    const int br2 = wave >> 1, sh = wave & 1;
    const unsigned st2 = 2 * rt + sh;
    wait16(ws, WS_FLAGB + st2 * 16 * 128, tag, lane);
    ST(13);
    if (b == 0 && wave == 0 && lane == 0) st_flag(ws, WS_EPOCH, tag);
    u32x4 of[32];
    const unsigned ob = WS_O + (st2 * 64 + 32 * br2) * 1024;
#pragma unroll
    for (int j = 0; j < 32; ++j) of[j] = __builtin_amdgcn_raw_buffer_load_b128(rsWS, lane * 16, ob + j * 1024, 16);
    const float gv = __hip_atomic_load((GAS float*)(ws + WS_GATE) + st2 * 32 + r32, __ATOMIC_RELAXED, __HIP_MEMORY_SCOPE_AGENT);
    const unsigned wp0 = (br2 ? WOB : WOA) + (4 * h + q) * 128 + sw + (p & 1) * 8, wp1 = wp0 ^ 64;
    f32x16 c0 = {}, c1 = {};
    bf16x8 a0 = ldstr(L, wp0, wp0 + 1024), a1 = ldstr(L, wp1, wp1 + 1024);
#pragma unroll
    for (int j = 0; j < 32; ++j) {
      bf16x8 n0, n1;
      if (j + 1 < 32) {
        const unsigned o = (j + 1) * 2048;
        n0 = ldstr(L, wp0 + o, wp0 + o + 1024);
        n1 = ldstr(L, wp1 + o, wp1 + o + 1024);
      }
      __builtin_amdgcn_sched_barrier(0);
      const bf16x8 bf = __builtin_bit_cast(bf16x8, of[j]);
      c0 = mfma(a0, bf, c0);
      c1 = mfma(a1, bf, c1);
      __builtin_amdgcn_sched_barrier(0);
      if (j + 1 < 32) { a0 = n0; a1 = n1; }
    }
    ST(14);
    const float gs = br2 ? 1.0f - gv : gv;
    f32x16 keep, send;
#pragma unroll
    for (int i = 0; i < 16; ++i) { keep[i] = (br2 ? c1[i] : c0[i]) * gs; send[i] = (br2 ? c0[i] : c1[i]) * gs; }
#pragma unroll
    for (int qq = 0; qq < 4; ++qq) {
      const f32x4 v = {send[4 * qq], send[4 * qq + 1], send[4 * qq + 2], send[4 * qq + 3]};
      *(LAS f32x4*)(L + EXC + wave * 4096 + qq * 1024 + lane * 16) = v;
    }
    WAITL0();
    BAR();
    unsigned pk[8];
#pragma unroll
    for (int qq = 0; qq < 4; ++qq) {
      const f32x4 v = *(LAS f32x4*)(L + EXC + (wave ^ 2) * 4096 + qq * 1024 + lane * 16);
      pk[2 * qq] = __builtin_bit_cast(unsigned, cvt2(keep[4 * qq] + v[0], keep[4 * qq + 1] + v[1]));
      pk[2 * qq + 1] = __builtin_bit_cast(unsigned, cvt2(keep[4 * qq + 2] + v[2], keep[4 * qq + 3] + v[3]));
    }
    const rsrc_t rsO = __builtin_amdgcn_make_buffer_rsrc((void*)out, 0, S_ * D_ * 2, 0x00020000);
    const unsigned orow = (st2 * 32 + r32) * 1024 + (64 * ct + 32 * br2) * 2 + h * 16;
#pragma unroll
    for (int qq = 0; qq < 4; qq += 2) {
      const auto s0 = __builtin_amdgcn_permlane32_swap(pk[2 * qq], pk[2 * qq + 2], false, false);
      const auto s1 = __builtin_amdgcn_permlane32_swap(pk[2 * qq + 1], pk[2 * qq + 3], false, false);
      const u32x4 v = {s0[0], s1[0], s0[1], s1[1]};
      __builtin_amdgcn_raw_buffer_store_b128(v, rsO, orow + qq * 16, 0, 0);
    }
    ST(15); STR(16); STFLUSH();
  }
}

extern "C" void kernel_launch(void* const* d_in, const int* in_sizes, int n_in, void* d_out, int out_size, void* d_ws, size_t ws_size,
                              hipStream_t stream) {
  (void)in_sizes; (void)n_in; (void)out_size; (void)ws_size;
  const u16* x = (const u16*)d_in[0];
  const u16* wA = (const u16*)d_in[1];
  const u16* woA = (const u16*)d_in[2];
  const u16* wB = (const u16*)d_in[4];
  const u16* woB = (const u16*)d_in[5];
  const u16* gw = (const u16*)d_in[6];
  (void)hipFuncSetAttribute((const void*)gated_mix_fused, hipFuncAttributeMaxDynamicSharedMemorySize, LDS_BYTES);
  gated_mix_fused<<<256, 512, LDS_BYTES, stream>>>(x, wA, wB, woA, woB, gw, (u16*)d_out, (char*)d_ws);
}
```

```cpp
#include <hip/hip_runtime.h>
#include <hip/hip_bf16.h>
#include <stdint.h>

#define LAS __attribute__((address_space(3)))
#define GAS __attribute__((address_space(1)))
#define DI __device__ __forceinline__

typedef unsigned short u16;
typedef __bf16 bf16x8 __attribute__((ext_vector_type(8)));
typedef __bf16 bf16x4 __attribute__((ext_vector_type(4)));
typedef __bf16 bf16x2 __attribute__((ext_vector_type(2)));
typedef float f32x16 __attribute__((ext_vector_type(16)));
typedef float f32x4 __attribute__((ext_vector_type(4)));
typedef float f32x2 __attribute__((ext_vector_type(2)));
typedef unsigned u32x4 __attribute__((ext_vector_type(4)));
typedef unsigned u32x2 __attribute__((ext_vector_type(2)));
typedef __amdgpu_buffer_rsrc_t rsrc_t;

#define ROT 0

constexpr int S_ = 2048, D_ = 512;
constexpr unsigned XR = 0;
constexpr unsigned WR = 49152;
constexpr unsigned WQ = 98304;
constexpr unsigned WOA = 0;
constexpr unsigned WOB = 98304;
constexpr unsigned SCR = 81920;
constexpr unsigned AFR = SCR + 12288;
constexpr unsigned EXC = SCR;
constexpr unsigned LDS_BYTES = 163840;
constexpr unsigned WS_EPOCH = 0;
constexpr unsigned WS_FLAGA = 4096;
constexpr unsigned WS_FLAGB = WS_FLAGA + 1024 * 128;
constexpr unsigned WS_GATE = WS_FLAGB + 1024 * 128;
constexpr unsigned WS_PART = WS_GATE + 8192;
constexpr unsigned PART_STRIDE = 1152;
constexpr unsigned WS_O = WS_PART + 1024 * PART_STRIDE;
constexpr unsigned WS_END = WS_O + 64 * 64 * 1024;
constexpr unsigned SPIN_CAP = 1u << 18;

#define ST(i) do {} while (0)
#define STR(i) do {} while (0)
#define STFLUSH() do {} while (0)

DI void glds(unsigned voff, rsrc_t rs, unsigned soff, unsigned ldsaddr) {
  asm volatile("s_mov_b32 m0, %0\n\ts_nop 0\n\tbuffer_load_dwordx4 %1, %2, %3 offen lds"
               :: "s"(ldsaddr), "v"(voff), "s"(rs), "s"(soff) : "memory");
}
#define WAITV(n) asm volatile("s_waitcnt vmcnt(" #n ")" ::: "memory")
#define WAITL0() asm volatile("s_waitcnt lgkmcnt(0)" ::: "memory")
#define BAR() asm volatile("s_barrier" ::: "memory")

DI f32x16 mfma(bf16x8 a, bf16x8 b, f32x16 c) { return __builtin_amdgcn_mfma_f32_32x32x16_bf16(a, b, c, 0, 0, 0); }
DI bf16x8 ldsr128(LAS char* L, unsigned a) { return *(LAS bf16x8*)(L + a); }
DI bf16x8 ldstr(LAS char* L, unsigned a0, unsigned a1) {
  bf16x4 lo = __builtin_amdgcn_ds_read_tr16_b64_v4bf16((LAS bf16x4*)(L + a0));
  bf16x4 hi = __builtin_amdgcn_ds_read_tr16_b64_v4bf16((LAS bf16x4*)(L + a1));
  return __builtin_shufflevector(lo, hi, 0, 1, 2, 3, 4, 5, 6, 7);
}
DI bf16x2 cvt2(float a, float b) { f32x2 p = {a, b}; return __builtin_convertvector(p, bf16x2); }
DI bf16x8 pack8(float a0, float a1, float a2, float a3, float a4, float a5, float a6, float a7) {
  bf16x2 p0 = cvt2(a0, a1), p1 = cvt2(a2, a3), p2 = cvt2(a4, a5), p3 = cvt2(a6, a7);
  bf16x4 lo = __builtin_shufflevector(p0, p1, 0, 1, 2, 3), hi = __builtin_shufflevector(p2, p3, 0, 1, 2, 3);
  return __builtin_shufflevector(lo, hi, 0, 1, 2, 3, 4, 5, 6, 7);
}
DI float phi(float t) {
  float e = __builtin_amdgcn_exp2f(t * 1.44269504088896340736f);
  e = __builtin_amdgcn_fmed3f(e, 0.0f, 1.0f);
  return (fmaxf(t, 0.0f) + e) + 1e-6f;
}
DI bf16x8 phi8(const f32x16& x, int s) {
  return pack8(phi(x[8 * s]), phi(x[8 * s + 1]), phi(x[8 * s + 2]), phi(x[8 * s + 3]),
               phi(x[8 * s + 4]), phi(x[8 * s + 5]), phi(x[8 * s + 6]), phi(x[8 * s + 7]));
}
DI bf16x8 cvt8(const f32x16& x, int s) {
  return pack8(x[8 * s], x[8 * s + 1], x[8 * s + 2], x[8 * s + 3], x[8 * s + 4], x[8 * s + 5], x[8 * s + 6], x[8 * s + 7]);
}
DI float sum8(bf16x8 v, float acc) {
  const bf16x2 ones = {(__bf16)1.0f, (__bf16)1.0f};
  acc = __builtin_amdgcn_fdot2_f32_bf16(__builtin_shufflevector(v, v, 0, 1), ones, acc, false);
  acc = __builtin_amdgcn_fdot2_f32_bf16(__builtin_shufflevector(v, v, 2, 3), ones, acc, false);
  acc = __builtin_amdgcn_fdot2_f32_bf16(__builtin_shufflevector(v, v, 4, 5), ones, acc, false);
  acc = __builtin_amdgcn_fdot2_f32_bf16(__builtin_shufflevector(v, v, 6, 7), ones, acc, false);
  return acc;
}
DI unsigned long long ld_flag(GAS char* ws, unsigned off) {
  return __hip_atomic_load((GAS unsigned long long*)(ws + off), __ATOMIC_RELAXED, __HIP_MEMORY_SCOPE_AGENT);
}
DI void st_flag(GAS char* ws, unsigned off, unsigned long long v) {
  __hip_atomic_store((GAS unsigned long long*)(ws + off), v, __ATOMIC_RELAXED, __HIP_MEMORY_SCOPE_AGENT);
}
DI unsigned long long load_epoch(const char* wsp) {
  unsigned long long e;
  asm volatile("s_load_dwordx2 %0, %1, 0x0\n\ts_waitcnt lgkmcnt(0)" : "=s"(e) : "s"(wsp) : "memory");
  return e;
}
DI void wait16(GAS char* ws, unsigned off0, unsigned long long tag, int lane) {
  const unsigned off = off0 + (unsigned)(lane & 15) * 128u;
  for (unsigned spins = 0; spins < SPIN_CAP; ++spins) {
    const unsigned long long v = ld_flag(ws, off);
    if (__all(v == tag)) break;
    __builtin_amdgcn_s_sleep(1);
  }
  asm volatile("" ::: "memory");
}

__global__ __launch_bounds__(512, 2) void gated_mix_fused(const u16* __restrict__ x, const u16* __restrict__ wA, const u16* __restrict__ wB,
                                                          const u16* __restrict__ woA, const u16* __restrict__ woB, const u16* __restrict__ gw,
                                                          u16* __restrict__ out, char* __restrict__ wsp
                                                          ) {
  extern __shared__ __attribute__((aligned(16))) char lds[];
  LAS char* L = (LAS char*)lds;
  const int tid = threadIdx.x;
  const int wave = __builtin_amdgcn_readfirstlane(tid >> 6), lane = tid & 63;
  const int b = blockIdx.x, xcd = b & 7, r = b >> 3;
  const int ti = (xcd & 1) * 8 + (r & 7), g = (xcd >> 1) * 4 + (r >> 3);
  const int gg = g & 7;
  const int rt = 4 * xcd + (r & 3), ct = r >> 2;
  GAS char* ws = (GAS char*)wsp;
  const unsigned long long rt0 = __builtin_amdgcn_s_memrealtime();
  if (wave == 0 && lane == 0) __hip_atomic_store((GAS unsigned long long*)(ws + (8u << 20)) + b, rt0, __ATOMIC_RELAXED, __HIP_MEMORY_SCOPE_AGENT);

  if (wave >= 4) {
    const int hw = wave - 4;
    int ro = 0;
    if (ROT == 1) ro = (ti + 2 * g) & 7;
    if (ROT == 2) ro = 2 * ((ti + g) & 1);
    if (ROT == 3) ro = 4 * ((ti + g) & 1);
    if (ROT == 5) ro = ((ti + g) % 3) * 2;
    const u16* w = (g >= 8) ? wB : wA;
    const rsrc_t rsX = __builtin_amdgcn_make_buffer_rsrc((void*)x, 0, S_ * D_ * 2, 0x00020000);
    const rsrc_t rsW = __builtin_amdgcn_make_buffer_rsrc((void*)w, 0, D_ * 3 * D_ * 2, 0x00020000);
    const int rin = lane >> 3, ch = lane & 7;
    const unsigned vx = rin * 1024 + ((ch ^ (4 * (hw & 1) + (rin >> 1))) << 4);
    const unsigned csw = (ch ^ (((rin >> 1) & 1) << 2)) << 4;
    const unsigned vw = rin * 3072 + csw;
    const unsigned sx0 = (ti * 128 + 8 * hw) * 1024;
    const unsigned sw0 = 8 * hw * 3072 + gg * 128;
    auto stage = [&](int s) {
      const unsigned ks = (s + ro) & 7, slot = (s % 3) * 16384;
#pragma unroll
      for (int i = 0; i < 4; ++i) glds(vx, rsX, sx0 + ks * 128 + i * 32768, XR + slot + (hw + 4 * i) * 1024);
#pragma unroll
      for (int i = 0; i < 2; ++i) glds(vw, rsW, sw0 + ks * 196608 + i * 98304 + 1024, WR + slot + (hw + 4 * i) * 1024);
#pragma unroll
      for (int i = 0; i < 2; ++i) glds(vw, rsW, sw0 + ks * 196608 + i * 98304 + 2048, WR + slot + 8192 + (hw + 4 * i) * 1024);
    };
    auto stageq = [&](int s) {
      const unsigned ks = (s + ro) & 7;
#pragma unroll
      for (int i = 0; i < 2; ++i) glds(vw, rsW, sw0 + ks * 196608 + i * 98304, WQ + s * 8192 + (hw + 4 * i) * 1024);
    };
    stage(0); stage(1);
    ST(2);
    const unsigned long long tag = load_epoch(wsp) + 1ull;
    WAITV(8);
    BAR();
    ST(3);
#pragma unroll
    for (int s = 1; s < 8; ++s) {
      if (s + 1 < 8) { stage(s + 1); WAITV(8); }
      else { stageq(0); stageq(1); stageq(2); stageq(3); WAITV(8); }
      BAR();
    }
    ST(4);
    stageq(4); stageq(5); stageq(6); stageq(7);
    {
      const rsrc_t rsG = __builtin_amdgcn_make_buffer_rsrc((void*)gw, 0, D_ * 2, 0x00020000);
      const unsigned row = ti * 128 + 8 * g + 2 * hw;
      u32x4 x0, x1, gv;
      asm volatile("buffer_load_dwordx4 %0, %3, %4, %6 offen\n\tbuffer_load_dwordx4 %1, %3, %4, %6 offen offset:1024\n\t"
                   "buffer_load_dwordx4 %2, %3, %5, 0 offen\n\ts_waitcnt vmcnt(0)"
                   : "=&v"(x0), "=&v"(x1), "=&v"(gv) : "v"(lane * 16), "s"(rsX), "s"(rsG), "s"(row * 1024) : "memory");
      float a0 = 0.f, a1 = 0.f;
#pragma unroll
      for (int i = 0; i < 4; ++i) {
        const unsigned ug = gv[i], u0 = x0[i], u1 = x1[i];
        const bf16x2 gp = __builtin_bit_cast(bf16x2, ug);
        a0 = __builtin_amdgcn_fdot2_f32_bf16(__builtin_bit_cast(bf16x2, u0), gp, a0, false);
        a1 = __builtin_amdgcn_fdot2_f32_bf16(__builtin_bit_cast(bf16x2, u1), gp, a1, false);
      }
#pragma unroll
      for (int m = 32; m >= 1; m >>= 1) { a0 += __shfl_xor(a0, m); a1 += __shfl_xor(a1, m); }
      const float lg = (lane & 1) ? a1 : a0;
      const float gval = __builtin_amdgcn_rcpf(1.0f + __builtin_amdgcn_exp2f(-1.44269504088896340736f * lg));
      if (lane < 2) __hip_atomic_store((GAS float*)(ws + WS_GATE) + row + lane, gval, __ATOMIC_RELAXED, __HIP_MEMORY_SCOPE_AGENT);
    }
    ST(5);
    BAR();
    ST(6);
    const rsrc_t rsWS = __builtin_amdgcn_make_buffer_rsrc((void*)wsp, 0, WS_END, 0x00020000);
    const unsigned hid = g * 4 + hw;
    {
      const f32x4 p0 = *(LAS f32x4*)(L + SCR + hw * 1024 + lane * 16), p1 = *(LAS f32x4*)(L + SCR + (4 + hw) * 1024 + lane * 16);
      const f32x4 z0 = *(LAS f32x4*)(L + SCR + (8 + hw) * 1024 + (lane & 3) * 16), z1 = *(LAS f32x4*)(L + SCR + (8 + hw) * 1024 + 64 + (lane & 3) * 16);
      const unsigned po = WS_PART + (hid * 16 + ti) * PART_STRIDE;
      __builtin_amdgcn_raw_buffer_store_b128(__builtin_bit_cast(u32x4, p0 + p1), rsWS, lane * 16, po, 16);
      if (lane < 4) __builtin_amdgcn_raw_buffer_store_b128(__builtin_bit_cast(u32x4, z0 + z1), rsWS, 1024 + lane * 16, po, 16);
      WAITV(0);
      if (lane == 0) st_flag(ws, WS_FLAGA + (hid * 16 + ti) * 128, tag);
    }
    ST(7);
    const rsrc_t rsOA = __builtin_amdgcn_make_buffer_rsrc((void*)woA, 0, D_ * D_ * 2, 0x00020000);
    const rsrc_t rsOB = __builtin_amdgcn_make_buffer_rsrc((void*)woB, 0, D_ * D_ * 2, 0x00020000);
    const unsigned vo = rin * 1024 + csw;
#pragma unroll
    for (int i = 0; i < 16; ++i) glds(vo, rsOA, (hw + 4 * i) * 8192 + ct * 128, WOA + (hw + 4 * i) * 1024);
    ST(8);
    wait16(ws, WS_FLAGA + hid * 16 * 128, tag, lane);
    ST(9);
    {
      const unsigned pb = WS_PART + hid * 16 * PART_STRIDE;
      u32x4 pv[16];
#pragma unroll
      for (int p = 0; p < 16; ++p) pv[p] = __builtin_amdgcn_raw_buffer_load_b128(rsWS, lane * 16, pb + p * PART_STRIDE, 16);
      const u32x4 zv = __builtin_amdgcn_raw_buffer_load_b128(rsWS, (lane >> 2) * PART_STRIDE + 1024 + (lane & 3) * 16, pb, 16);
      f32x4 a = __builtin_bit_cast(f32x4, pv[0]);
#pragma unroll
      for (int p = 1; p < 16; ++p) a += __builtin_bit_cast(f32x4, pv[p]);
      f32x4 z = __builtin_bit_cast(f32x4, zv);
#pragma unroll
      for (int m = 4; m <= 32; m <<= 1) {
        z[0] += __shfl_xor(z[0], m); z[1] += __shfl_xor(z[1], m); z[2] += __shfl_xor(z[2], m); z[3] += __shfl_xor(z[3], m);
      }
      const int slot = lane & 31, jh = lane >> 5;
      u32x2 ka = {__builtin_bit_cast(unsigned, cvt2(a[0], a[1])), __builtin_bit_cast(unsigned, cvt2(a[2], a[3]))};
      *(LAS u32x2*)(L + AFR + hw * 1024 + ((slot >> 4) * 32 + (slot & 15)) * 16 + jh * 8) = ka;
      const int q4 = lane & 3;
      u32x2 kz = {__builtin_bit_cast(unsigned, cvt2(z[0], z[1])), __builtin_bit_cast(unsigned, cvt2(z[2], z[3]))};
      *(LAS u32x2*)(L + AFR + hw * 1024 + ((q4 & 1) * 32 + 16 + (lane >> 2)) * 16 + (q4 >> 1) * 8) = kz;
    }
    WAITL0();
    ST(10);
    BAR();
    ST(11);
#pragma unroll
    for (int i = 0; i < 16; ++i) glds(vo, rsOB, (hw + 4 * i) * 8192 + ct * 128, WOB + (hw + 4 * i) * 1024);
    WAITV(0);
    ST(12);
    BAR();
    STR(16); STFLUSH();
    return;
  }

  const int wr = wave >> 1, wc = wave & 1;
  const int r32 = lane & 31, h = lane >> 5;
  const int g1 = (lane >> 4) & 1, q = (lane & 15) >> 2, p = lane & 3;
  __builtin_amdgcn_s_setprio(1);
  const int own = 2 * wr + wc, oth = 2 * wr + 1 - wc;
  const unsigned fx = (h ^ ((r32 >> 1) & 7)) << 4;
  const unsigned xo0 = XR + (own * 32 + r32) * 128 + fx, xt0 = XR + (oth * 32 + r32) * 128 + fx;
  const unsigned sw = ((2 * g1 + (p >> 1)) ^ (((q >> 1) & 1) << 2)) << 4;
  const unsigned wn0 = (8 * h + q) * 128 + sw + (p & 1) * 8;
  const unsigned wkv0 = WR + (wn0 ^ (wc << 6));

  bf16x8 xr[32];
  f32x16 kO = {}, kT = {}, vO = {}, vT = {};
  const unsigned long long tag = load_epoch(wsp) + 1ull;
  {
    BAR();
    ST(3);
    bf16x8 xt = ldsr128(L, xt0), wk = ldstr(L, wkv0, wkv0 + 512), wv = ldstr(L, wkv0 + 8192, wkv0 + 8192 + 512);
    xr[0] = ldsr128(L, xo0);
#pragma unroll
    for (int t = 0; t < 32; ++t) {
      bf16x8 nxt, nwk, nwv;
      if (t + 1 < 32) {
        const int s1 = (t + 1) >> 2, k1 = (t + 1) & 3;
        const unsigned slot = (s1 % 3) * 16384;
        if (k1 == 0) { WAITL0(); BAR(); }
        xr[t + 1] = ldsr128(L, (xo0 ^ (k1 << 5)) + slot);
        nxt = ldsr128(L, (xt0 ^ (k1 << 5)) + slot);
        nwk = ldstr(L, wkv0 + slot + k1 * 2048, wkv0 + slot + k1 * 2048 + 512);
        nwv = ldstr(L, wkv0 + slot + 8192 + k1 * 2048, wkv0 + slot + 8192 + k1 * 2048 + 512);
      }
      __builtin_amdgcn_sched_barrier(0);
      kO = mfma(xr[t], wk, kO);
      kT = mfma(xt, wk, kT);
      vO = mfma(xr[t], wv, vO);
      vT = mfma(xt, wv, vT);
      __builtin_amdgcn_sched_barrier(0);
      if (t + 1 < 32) { xt = nxt; wk = nwk; wv = nwv; }
    }
  }
  ST(4);
  {
    f32x16 kv = {};
    float zs = 0.f;
#pragma unroll
    for (int s = 0; s < 2; ++s) {
      const bf16x8 pa = phi8(kO, s), pb = phi8(kT, s);
      kv = mfma(pa, cvt8(vO, s), kv);
      kv = mfma(pb, cvt8(vT, s), kv);
      zs = sum8(pa, zs); zs = sum8(pb, zs);
    }
    zs += __shfl_xor(zs, 32);
    f32x4 lo, hi;
#pragma unroll
    for (int j = 0; j < 4; ++j) { lo[j] = g1 ? kv[8 + j] : kv[j]; hi[j] = g1 ? kv[12 + j] : kv[4 + j]; }
    const int hh = 2 * wc + g1;
    const unsigned a = SCR + (4 * wr + hh) * 1024 + (h * 16 + (lane & 15)) * 16;
    *(LAS f32x4*)(L + a) = lo;
    *(LAS f32x4*)(L + a + 512) = hi;
    if (h == 0) *(LAS float*)(L + SCR + (8 + hh) * 1024 + wr * 64 + (lane & 15) * 4) = zs;
  }
  WAITL0();
  ST(5);
  BAR();
  ST(6);
  f32x16 qa = {}, qb = {};
  {
    const unsigned wq0 = WQ + wn0, wq1 = WQ + (wn0 ^ 64);
    bf16x8 a0 = ldstr(L, wq0, wq0 + 512), a1 = ldstr(L, wq1, wq1 + 512);
#pragma unroll
    for (int t = 0; t < 32; ++t) {
      bf16x8 n0, n1;
      if (t + 1 < 32) {
        const unsigned o = (t + 1) * 2048;
        n0 = ldstr(L, wq0 + o, wq0 + o + 512);
        n1 = ldstr(L, wq1 + o, wq1 + o + 512);
      }
      __builtin_amdgcn_sched_barrier(0);
      qa = mfma(a0, xr[t], qa);
      qb = mfma(a1, xr[t], qb);
      __builtin_amdgcn_sched_barrier(0);
      if (t + 1 < 32) { a0 = n0; a1 = n1; }
    }
  }
  ST(7);
  bf16x8 pq[4];
  pq[0] = phi8(qa, 0); pq[1] = phi8(qa, 1); pq[2] = phi8(qb, 0); pq[3] = phi8(qb, 1);
  const rsrc_t rsWS = __builtin_amdgcn_make_buffer_rsrc((void*)wsp, 0, WS_END, 0x00020000);
  WAITL0();
  ST(8);
  BAR();
  ST(9);
  {
    const unsigned st = 4 * ti + own;
#pragma unroll
    for (int hh = 0; hh < 4; ++hh) {
      const bf16x8 af = ldsr128(L, AFR + hh * 1024 + lane * 16);
      f32x16 d = {};
      d = mfma(af, pq[hh], d);
      const float inv = __builtin_amdgcn_rcpf(d[8] + 1e-6f);
      const bf16x8 o = pack8(d[0] * inv, d[1] * inv, d[2] * inv, d[3] * inv, d[4] * inv, d[5] * inv, d[6] * inv, d[7] * inv);
      __builtin_amdgcn_raw_buffer_store_b128(__builtin_bit_cast(u32x4, o), rsWS, lane * 16, WS_O + (st * 64 + 4 * g + hh) * 1024, 16);
    }
    ST(10);
    WAITV(0);
    if (lane == 0) st_flag(ws, WS_FLAGB + (st * 16 + g) * 128, tag);
  }
  ST(11);
  BAR();
  ST(12);
  {
    const int br2 = wave >> 1, sh = wave & 1;
    const unsigned st2 = 2 * rt + sh;
    wait16(ws, WS_FLAGB + st2 * 16 * 128, tag, lane);
    ST(13);
    if (b == 0 && wave == 0 && lane == 0) st_flag(ws, WS_EPOCH, tag);
    u32x4 of[32];
    const unsigned ob = WS_O + (st2 * 64 + 32 * br2) * 1024;
#pragma unroll
    for (int j = 0; j < 32; ++j) of[j] = __builtin_amdgcn_raw_buffer_load_b128(rsWS, lane * 16, ob + j * 1024, 16);
    const float gv = __hip_atomic_load((GAS float*)(ws + WS_GATE) + st2 * 32 + r32, __ATOMIC_RELAXED, __HIP_MEMORY_SCOPE_AGENT);
    const unsigned wp0 = (br2 ? WOB : WOA) + (4 * h + q) * 128 + sw + (p & 1) * 8, wp1 = wp0 ^ 64;
    f32x16 c0 = {}, c1 = {};
    bf16x8 a0 = ldstr(L, wp0, wp0 + 1024), a1 = ldstr(L, wp1, wp1 + 1024);
#pragma unroll
    for (int j = 0; j < 32; ++j) {
      bf16x8 n0, n1;
      if (j + 1 < 32) {
        const unsigned o = (j + 1) * 2048;
        n0 = ldstr(L, wp0 + o, wp0 + o + 1024);
        n1 = ldstr(L, wp1 + o, wp1 + o + 1024);
      }
      __builtin_amdgcn_sched_barrier(0);
      const bf16x8 bf = __builtin_bit_cast(bf16x8, of[j]);
      c0 = mfma(a0, bf, c0);
      c1 = mfma(a1, bf, c1);
      __builtin_amdgcn_sched_barrier(0);
      if (j + 1 < 32) { a0 = n0; a1 = n1; }
    }
    ST(14);
    const float gs = br2 ? 1.0f - gv : gv;
    f32x16 keep, send;
#pragma unroll
    for (int i = 0; i < 16; ++i) { keep[i] = (br2 ? c1[i] : c0[i]) * gs; send[i] = (br2 ? c0[i] : c1[i]) * gs; }
#pragma unroll
    for (int qq = 0; qq < 4; ++qq) {
      const f32x4 v = {send[4 * qq], send[4 * qq + 1], send[4 * qq + 2], send[4 * qq + 3]};
      *(LAS f32x4*)(L + EXC + wave * 4096 + qq * 1024 + lane * 16) = v;
    }
    WAITL0();
    BAR();
    unsigned pk[8];
#pragma unroll
    for (int qq = 0; qq < 4; ++qq) {
      const f32x4 v = *(LAS f32x4*)(L + EXC + (wave ^ 2) * 4096 + qq * 1024 + lane * 16);
      pk[2 * qq] = __builtin_bit_cast(unsigned, cvt2(keep[4 * qq] + v[0], keep[4 * qq + 1] + v[1]));
      pk[2 * qq + 1] = __builtin_bit_cast(unsigned, cvt2(keep[4 * qq + 2] + v[2], keep[4 * qq + 3] + v[3]));
    }
    const rsrc_t rsO = __builtin_amdgcn_make_buffer_rsrc((void*)out, 0, S_ * D_ * 2, 0x00020000);
    const unsigned orow = (st2 * 32 + r32) * 1024 + (64 * ct + 32 * br2) * 2 + h * 16;
#pragma unroll
    for (int qq = 0; qq < 4; qq += 2) {
      const auto s0 = __builtin_amdgcn_permlane32_swap(pk[2 * qq], pk[2 * qq + 2], false, false);
      const auto s1 = __builtin_amdgcn_permlane32_swap(pk[2 * qq + 1], pk[2 * qq + 3], false, false);
      const u32x4 v = {s0[0], s1[0], s0[1], s1[1]};
      __builtin_amdgcn_raw_buffer_store_b128(v, rsO, orow + qq * 16, 0, 0);
    }
    if (b == 0 && wave == 0) {
      WAITV(0);
      const unsigned long long now = __builtin_amdgcn_s_memrealtime();
      unsigned long long mn = ~0ull, mx = 0;
      for (int i = 0; i < 4; ++i) { unsigned long long v = __hip_atomic_load((GAS unsigned long long*)(ws + (8u << 20)) + lane + 64 * i, __ATOMIC_RELAXED, __HIP_MEMORY_SCOPE_AGENT); mn = v < mn ? v : mn; mx = v > mx ? v : mx; }
      for (int m = 32; m >= 1; m >>= 1) { unsigned long long a = __shfl_xor(mn, m), c = __shfl_xor(mx, m); mn = a < mn ? a : mn; mx = c > mx ? c : mx; }
      unsigned long long X = (rt0 - mn);
      if (X > 5000) X = 5000;
      const unsigned long long until = now + X * 4;
      while (__builtin_amdgcn_s_memrealtime() < until) __builtin_amdgcn_s_sleep(1);
    }
  }
}

extern "C" void kernel_launch(void* const* d_in, const int* in_sizes, int n_in, void* d_out, int out_size, void* d_ws, size_t ws_size,
                              hipStream_t stream) {
  (void)in_sizes; (void)n_in; (void)out_size; (void)ws_size;
  const u16* x = (const u16*)d_in[0];
  const u16* wA = (const u16*)d_in[1];
  const u16* woA = (const u16*)d_in[2];
  const u16* wB = (const u16*)d_in[4];
  const u16* woB = (const u16*)d_in[5];
  const u16* gw = (const u16*)d_in[6];
  (void)hipFuncSetAttribute((const void*)gated_mix_fused, hipFuncAttributeMaxDynamicSharedMemorySize, LDS_BYTES);
  gated_mix_fused<<<256, 512, LDS_BYTES, stream>>>(x, wA, wB, woA, woB, gw, (u16*)d_out, (char*)d_ws);
}
```

```cpp
#include <hip/hip_runtime.h>
#include <hip/hip_bf16.h>
#include <stdint.h>

#define LAS __attribute__((address_space(3)))
#define GAS __attribute__((address_space(1)))
#define DI __device__ __forceinline__

typedef unsigned short u16;
typedef __bf16 bf16x8 __attribute__((ext_vector_type(8)));
typedef __bf16 bf16x4 __attribute__((ext_vector_type(4)));
typedef __bf16 bf16x2 __attribute__((ext_vector_type(2)));
typedef float f32x16 __attribute__((ext_vector_type(16)));
typedef float f32x4 __attribute__((ext_vector_type(4)));
typedef float f32x2 __attribute__((ext_vector_type(2)));
typedef unsigned u32x4 __attribute__((ext_vector_type(4)));
typedef unsigned u32x2 __attribute__((ext_vector_type(2)));
typedef __amdgpu_buffer_rsrc_t rsrc_t;

#define ROT 0

constexpr int S_ = 2048, D_ = 512;
constexpr unsigned XR = 0;
constexpr unsigned WR = 49152;
constexpr unsigned WQ = 98304;
constexpr unsigned WOA = 0;
constexpr unsigned WOB = 98304;
constexpr unsigned SCR = 81920;
constexpr unsigned AFR = SCR + 12288;
constexpr unsigned EXC = SCR;
constexpr unsigned LDS_BYTES = 163840;
constexpr unsigned WS_EPOCH = 0;
constexpr unsigned WS_FLAGA = 4096;
constexpr unsigned WS_FLAGB = WS_FLAGA + 1024 * 128;
constexpr unsigned WS_GATE = WS_FLAGB + 1024 * 128;
constexpr unsigned WS_PART = WS_GATE + 8192;
constexpr unsigned PART_STRIDE = 1152;
constexpr unsigned WS_O = WS_PART + 1024 * PART_STRIDE;
constexpr unsigned WS_END = WS_O + 64 * 64 * 1024;
constexpr unsigned SPIN_CAP = 1u << 18;

#define ST(i) do {} while (0)
#define STR(i) do {} while (0)
#define STFLUSH() do {} while (0)

DI void glds(unsigned voff, rsrc_t rs, unsigned soff, unsigned ldsaddr) {
  asm volatile("s_mov_b32 m0, %0\n\ts_nop 0\n\tbuffer_load_dwordx4 %1, %2, %3 offen lds"
               :: "s"(ldsaddr), "v"(voff), "s"(rs), "s"(soff) : "memory");
}
#define WAITV(n) asm volatile("s_waitcnt vmcnt(" #n ")" ::: "memory")
#define WAITL0() asm volatile("s_waitcnt lgkmcnt(0)" ::: "memory")
#define BAR() asm volatile("s_barrier" ::: "memory")

DI f32x16 mfma(bf16x8 a, bf16x8 b, f32x16 c) { return __builtin_amdgcn_mfma_f32_32x32x16_bf16(a, b, c, 0, 0, 0); }
DI bf16x8 ldsr128(LAS char* L, unsigned a) { return *(LAS bf16x8*)(L + a); }
DI bf16x8 ldstr(LAS char* L, unsigned a0, unsigned a1) {
  bf16x4 lo = __builtin_amdgcn_ds_read_tr16_b64_v4bf16((LAS bf16x4*)(L + a0));
  bf16x4 hi = __builtin_amdgcn_ds_read_tr16_b64_v4bf16((LAS bf16x4*)(L + a1));
  return __builtin_shufflevector(lo, hi, 0, 1, 2, 3, 4, 5, 6, 7);
}
DI bf16x2 cvt2(float a, float b) { f32x2 p = {a, b}; return __builtin_convertvector(p, bf16x2); }
DI bf16x8 pack8(float a0, float a1, float a2, float a3, float a4, float a5, float a6, float a7) {
  bf16x2 p0 = cvt2(a0, a1), p1 = cvt2(a2, a3), p2 = cvt2(a4, a5), p3 = cvt2(a6, a7);
  bf16x4 lo = __builtin_shufflevector(p0, p1, 0, 1, 2, 3), hi = __builtin_shufflevector(p2, p3, 0, 1, 2, 3);
  return __builtin_shufflevector(lo, hi, 0, 1, 2, 3, 4, 5, 6, 7);
}
DI float phi(float t) {
  float e = __builtin_amdgcn_exp2f(t * 1.44269504088896340736f);
  e = __builtin_amdgcn_fmed3f(e, 0.0f, 1.0f);
  return (fmaxf(t, 0.0f) + e) + 1e-6f;
}
DI bf16x8 phi8(const f32x16& x, int s) {
  return pack8(phi(x[8 * s]), phi(x[8 * s + 1]), phi(x[8 * s + 2]), phi(x[8 * s + 3]),
               phi(x[8 * s + 4]), phi(x[8 * s + 5]), phi(x[8 * s + 6]), phi(x[8 * s + 7]));
}
DI bf16x8 cvt8(const f32x16& x, int s) {
  return pack8(x[8 * s], x[8 * s + 1], x[8 * s + 2], x[8 * s + 3], x[8 * s + 4], x[8 * s + 5], x[8 * s + 6], x[8 * s + 7]);
}
DI float sum8(bf16x8 v, float acc) {
  const bf16x2 ones = {(__bf16)1.0f, (__bf16)1.0f};
  acc = __builtin_amdgcn_fdot2_f32_bf16(__builtin_shufflevector(v, v, 0, 1), ones, acc, false);
  acc = __builtin_amdgcn_fdot2_f32_bf16(__builtin_shufflevector(v, v, 2, 3), ones, acc, false);
  acc = __builtin_amdgcn_fdot2_f32_bf16(__builtin_shufflevector(v, v, 4, 5), ones, acc, false);
  acc = __builtin_amdgcn_fdot2_f32_bf16(__builtin_shufflevector(v, v, 6, 7), ones, acc, false);
  return acc;
}
DI unsigned long long ld_flag(GAS char* ws, unsigned off) {
  return __hip_atomic_load((GAS unsigned long long*)(ws + off), __ATOMIC_RELAXED, __HIP_MEMORY_SCOPE_AGENT);
}
DI void st_flag(GAS char* ws, unsigned off, unsigned long long v) {
  __hip_atomic_store((GAS unsigned long long*)(ws + off), v, __ATOMIC_RELAXED, __HIP_MEMORY_SCOPE_AGENT);
}
DI unsigned long long load_epoch(const char* wsp) {
  unsigned long long e;
  asm volatile("s_load_dwordx2 %0, %1, 0x0\n\ts_waitcnt lgkmcnt(0)" : "=s"(e) : "s"(wsp) : "memory");
  return e;
}
DI void wait16(GAS char* ws, unsigned off0, unsigned long long tag, int lane) {
  const unsigned off = off0 + (unsigned)(lane & 15) * 128u;
  for (unsigned spins = 0; spins < SPIN_CAP; ++spins) {
    const unsigned long long v = ld_flag(ws, off);
    if (__all(v == tag)) break;
    __builtin_amdgcn_s_sleep(1);
  }
  asm volatile("" ::: "memory");
}

__global__ __launch_bounds__(512, 2) void gated_mix_fused(const u16* __restrict__ x, const u16* __restrict__ wA, const u16* __restrict__ wB,
                                                          const u16* __restrict__ woA, const u16* __restrict__ woB, const u16* __restrict__ gw,
                                                          u16* __restrict__ out, char* __restrict__ wsp
                                                          ) {
  extern __shared__ __attribute__((aligned(16))) char lds[];
  unsigned long long rt0, rt1, dummy;
  asm volatile("s_memrealtime %0\n\ts_waitcnt lgkmcnt(0)\n\ts_load_dwordx2 %1, %3, 0x38\n\ts_waitcnt lgkmcnt(0)\n\ts_memrealtime %2\n\ts_waitcnt lgkmcnt(0)"
     : "=&s"(rt0), "=&s"(dummy), "=&s"(rt1) : "s"(__builtin_amdgcn_kernarg_segment_ptr()) : "memory");
  LAS char* L = (LAS char*)lds;
  const int tid = threadIdx.x;
  const int wave = __builtin_amdgcn_readfirstlane(tid >> 6), lane = tid & 63;
  const int b = blockIdx.x, xcd = b & 7, r = b >> 3;
  const int ti = (xcd & 1) * 8 + (r & 7), g = (xcd >> 1) * 4 + (r >> 3);
  const int gg = g & 7;
  const int rt = 4 * xcd + (r & 3), ct = r >> 2;
  GAS char* ws = (GAS char*)wsp;
  if (wave == 0 && lane == 0) __hip_atomic_store((GAS unsigned long long*)(ws + (8u << 20)) + b, rt0, __ATOMIC_RELAXED, __HIP_MEMORY_SCOPE_AGENT);

  if (wave >= 4) {
    const int hw = wave - 4;
    int ro = 0;
    if (ROT == 1) ro = (ti + 2 * g) & 7;
    if (ROT == 2) ro = 2 * ((ti + g) & 1);
    if (ROT == 3) ro = 4 * ((ti + g) & 1);
    if (ROT == 5) ro = ((ti + g) % 3) * 2;
    const u16* w = (g >= 8) ? wB : wA;
    const rsrc_t rsX = __builtin_amdgcn_make_buffer_rsrc((void*)x, 0, S_ * D_ * 2, 0x00020000);
    const rsrc_t rsW = __builtin_amdgcn_make_buffer_rsrc((void*)w, 0, D_ * 3 * D_ * 2, 0x00020000);
    const int rin = lane >> 3, ch = lane & 7;
    const unsigned vx = rin * 1024 + ((ch ^ (4 * (hw & 1) + (rin >> 1))) << 4);
    const unsigned csw = (ch ^ (((rin >> 1) & 1) << 2)) << 4;
    const unsigned vw = rin * 3072 + csw;
    const unsigned sx0 = (ti * 128 + 8 * hw) * 1024;
    const unsigned sw0 = 8 * hw * 3072 + gg * 128;
    auto stage = [&](int s) {
      const unsigned ks = (s + ro) & 7, slot = (s % 3) * 16384;
#pragma unroll
      for (int i = 0; i < 4; ++i) glds(vx, rsX, sx0 + ks * 128 + i * 32768, XR + slot + (hw + 4 * i) * 1024);
#pragma unroll
      for (int i = 0; i < 2; ++i) glds(vw, rsW, sw0 + ks * 196608 + i * 98304 + 1024, WR + slot + (hw + 4 * i) * 1024);
#pragma unroll
      for (int i = 0; i < 2; ++i) glds(vw, rsW, sw0 + ks * 196608 + i * 98304 + 2048, WR + slot + 8192 + (hw + 4 * i) * 1024);
    };
    auto stageq = [&](int s) {
      const unsigned ks = (s + ro) & 7;
#pragma unroll
      for (int i = 0; i < 2; ++i) glds(vw, rsW, sw0 + ks * 196608 + i * 98304, WQ + s * 8192 + (hw + 4 * i) * 1024);
    };
    stage(0); stage(1);
    ST(2);
    const unsigned long long tag = load_epoch(wsp) + 1ull;
    WAITV(8);
    BAR();
    ST(3);
#pragma unroll
    for (int s = 1; s < 8; ++s) {
      if (s + 1 < 8) { stage(s + 1); WAITV(8); }
      else { stageq(0); stageq(1); stageq(2); stageq(3); WAITV(8); }
      BAR();
    }
    ST(4);
    stageq(4); stageq(5); stageq(6); stageq(7);
    {
      const rsrc_t rsG = __builtin_amdgcn_make_buffer_rsrc((void*)gw, 0, D_ * 2, 0x00020000);
      const unsigned row = ti * 128 + 8 * g + 2 * hw;
      u32x4 x0, x1, gv;
      asm volatile("buffer_load_dwordx4 %0, %3, %4, %6 offen\n\tbuffer_load_dwordx4 %1, %3, %4, %6 offen offset:1024\n\t"
                   "buffer_load_dwordx4 %2, %3, %5, 0 offen\n\ts_waitcnt vmcnt(0)"
                   : "=&v"(x0), "=&v"(x1), "=&v"(gv) : "v"(lane * 16), "s"(rsX), "s"(rsG), "s"(row * 1024) : "memory");
      float a0 = 0.f, a1 = 0.f;
#pragma unroll
      for (int i = 0; i < 4; ++i) {
        const unsigned ug = gv[i], u0 = x0[i], u1 = x1[i];
        const bf16x2 gp = __builtin_bit_cast(bf16x2, ug);
        a0 = __builtin_amdgcn_fdot2_f32_bf16(__builtin_bit_cast(bf16x2, u0), gp, a0, false);
        a1 = __builtin_amdgcn_fdot2_f32_bf16(__builtin_bit_cast(bf16x2, u1), gp, a1, false);
      }
#pragma unroll
      for (int m = 32; m >= 1; m >>= 1) { a0 += __shfl_xor(a0, m); a1 += __shfl_xor(a1, m); }
      const float lg = (lane & 1) ? a1 : a0;
      const float gval = __builtin_amdgcn_rcpf(1.0f + __builtin_amdgcn_exp2f(-1.44269504088896340736f * lg));
      if (lane < 2) __hip_atomic_store((GAS float*)(ws + WS_GATE) + row + lane, gval, __ATOMIC_RELAXED, __HIP_MEMORY_SCOPE_AGENT);
    }
    ST(5);
    BAR();
    ST(6);
    const rsrc_t rsWS = __builtin_amdgcn_make_buffer_rsrc((void*)wsp, 0, WS_END, 0x00020000);
    const unsigned hid = g * 4 + hw;
    {
      const f32x4 p0 = *(LAS f32x4*)(L + SCR + hw * 1024 + lane * 16), p1 = *(LAS f32x4*)(L + SCR + (4 + hw) * 1024 + lane * 16);
      const f32x4 z0 = *(LAS f32x4*)(L + SCR + (8 + hw) * 1024 + (lane & 3) * 16), z1 = *(LAS f32x4*)(L + SCR + (8 + hw) * 1024 + 64 + (lane & 3) * 16);
      const unsigned po = WS_PART + (hid * 16 + ti) * PART_STRIDE;
      __builtin_amdgcn_raw_buffer_store_b128(__builtin_bit_cast(u32x4, p0 + p1), rsWS, lane * 16, po, 16);
      if (lane < 4) __builtin_amdgcn_raw_buffer_store_b128(__builtin_bit_cast(u32x4, z0 + z1), rsWS, 1024 + lane * 16, po, 16);
      WAITV(0);
      if (lane == 0) st_flag(ws, WS_FLAGA + (hid * 16 + ti) * 128, tag);
    }
    ST(7);
    const rsrc_t rsOA = __builtin_amdgcn_make_buffer_rsrc((void*)woA, 0, D_ * D_ * 2, 0x00020000);
    const rsrc_t rsOB = __builtin_amdgcn_make_buffer_rsrc((void*)woB, 0, D_ * D_ * 2, 0x00020000);
    const unsigned vo = rin * 1024 + csw;
#pragma unroll
    for (int i = 0; i < 16; ++i) glds(vo, rsOA, (hw + 4 * i) * 8192 + ct * 128, WOA + (hw + 4 * i) * 1024);
    ST(8);
    wait16(ws, WS_FLAGA + hid * 16 * 128, tag, lane);
    ST(9);
    {
      const unsigned pb = WS_PART + hid * 16 * PART_STRIDE;
      u32x4 pv[16];
#pragma unroll
      for (int p = 0; p < 16; ++p) pv[p] = __builtin_amdgcn_raw_buffer_load_b128(rsWS, lane * 16, pb + p * PART_STRIDE, 16);
      const u32x4 zv = __builtin_amdgcn_raw_buffer_load_b128(rsWS, (lane >> 2) * PART_STRIDE + 1024 + (lane & 3) * 16, pb, 16);
      f32x4 a = __builtin_bit_cast(f32x4, pv[0]);
#pragma unroll
      for (int p = 1; p < 16; ++p) a += __builtin_bit_cast(f32x4, pv[p]);
      f32x4 z = __builtin_bit_cast(f32x4, zv);
#pragma unroll
      for (int m = 4; m <= 32; m <<= 1) {
        z[0] += __shfl_xor(z[0], m); z[1] += __shfl_xor(z[1], m); z[2] += __shfl_xor(z[2], m); z[3] += __shfl_xor(z[3], m);
      }
      const int slot = lane & 31, jh = lane >> 5;
      u32x2 ka = {__builtin_bit_cast(unsigned, cvt2(a[0], a[1])), __builtin_bit_cast(unsigned, cvt2(a[2], a[3]))};
      *(LAS u32x2*)(L + AFR + hw * 1024 + ((slot >> 4) * 32 + (slot & 15)) * 16 + jh * 8) = ka;
      const int q4 = lane & 3;
      u32x2 kz = {__builtin_bit_cast(unsigned, cvt2(z[0], z[1])), __builtin_bit_cast(unsigned, cvt2(z[2], z[3]))};
      *(LAS u32x2*)(L + AFR + hw * 1024 + ((q4 & 1) * 32 + 16 + (lane >> 2)) * 16 + (q4 >> 1) * 8) = kz;
    }
    WAITL0();
    ST(10);
    BAR();
    ST(11);
#pragma unroll
    for (int i = 0; i < 16; ++i) glds(vo, rsOB, (hw + 4 * i) * 8192 + ct * 128, WOB + (hw + 4 * i) * 1024);
    WAITV(0);
    ST(12);
    BAR();
    STR(16); STFLUSH();
    return;
  }

  const int wr = wave >> 1, wc = wave & 1;
  const int r32 = lane & 31, h = lane >> 5;
  const int g1 = (lane >> 4) & 1, q = (lane & 15) >> 2, p = lane & 3;
  __builtin_amdgcn_s_setprio(1);
  const int own = 2 * wr + wc, oth = 2 * wr + 1 - wc;
  const unsigned fx = (h ^ ((r32 >> 1) & 7)) << 4;
  const unsigned xo0 = XR + (own * 32 + r32) * 128 + fx, xt0 = XR + (oth * 32 + r32) * 128 + fx;
  const unsigned sw = ((2 * g1 + (p >> 1)) ^ (((q >> 1) & 1) << 2)) << 4;
  const unsigned wn0 = (8 * h + q) * 128 + sw + (p & 1) * 8;
  const unsigned wkv0 = WR + (wn0 ^ (wc << 6));

  bf16x8 xr[32];
  f32x16 kO = {}, kT = {}, vO = {}, vT = {};
  const unsigned long long tag = load_epoch(wsp) + 1ull;
  {
    BAR();
    ST(3);
    bf16x8 xt = ldsr128(L, xt0), wk = ldstr(L, wkv0, wkv0 + 512), wv = ldstr(L, wkv0 + 8192, wkv0 + 8192 + 512);
    xr[0] = ldsr128(L, xo0);
#pragma unroll
    for (int t = 0; t < 32; ++t) {
      bf16x8 nxt, nwk, nwv;
      if (t + 1 < 32) {
        const int s1 = (t + 1) >> 2, k1 = (t + 1) & 3;
        const unsigned slot = (s1 % 3) * 16384;
        if (k1 == 0) { WAITL0(); BAR(); }
        xr[t + 1] = ldsr128(L, (xo0 ^ (k1 << 5)) + slot);
        nxt = ldsr128(L, (xt0 ^ (k1 << 5)) + slot);
        nwk = ldstr(L, wkv0 + slot + k1 * 2048, wkv0 + slot + k1 * 2048 + 512);
        nwv = ldstr(L, wkv0 + slot + 8192 + k1 * 2048, wkv0 + slot + 8192 + k1 * 2048 + 512);
      }
      __builtin_amdgcn_sched_barrier(0);
      kO = mfma(xr[t], wk, kO);
      kT = mfma(xt, wk, kT);
      vO = mfma(xr[t], wv, vO);
      vT = mfma(xt, wv, vT);
      __builtin_amdgcn_sched_barrier(0);
      if (t + 1 < 32) { xt = nxt; wk = nwk; wv = nwv; }
    }
  }
  ST(4);
  {
    f32x16 kv = {};
    float zs = 0.f;
#pragma unroll
    for (int s = 0; s < 2; ++s) {
      const bf16x8 pa = phi8(kO, s), pb = phi8(kT, s);
      kv = mfma(pa, cvt8(vO, s), kv);
      kv = mfma(pb, cvt8(vT, s), kv);
      zs = sum8(pa, zs); zs = sum8(pb, zs);
    }
    zs += __shfl_xor(zs, 32);
    f32x4 lo, hi;
#pragma unroll
    for (int j = 0; j < 4; ++j) { lo[j] = g1 ? kv[8 + j] : kv[j]; hi[j] = g1 ? kv[12 + j] : kv[4 + j]; }
    const int hh = 2 * wc + g1;
    const unsigned a = SCR + (4 * wr + hh) * 1024 + (h * 16 + (lane & 15)) * 16;
    *(LAS f32x4*)(L + a) = lo;
    *(LAS f32x4*)(L + a + 512) = hi;
    if (h == 0) *(LAS float*)(L + SCR + (8 + hh) * 1024 + wr * 64 + (lane & 15) * 4) = zs;
  }
  WAITL0();
  ST(5);
  BAR();
  ST(6);
  f32x16 qa = {}, qb = {};
  {
    const unsigned wq0 = WQ + wn0, wq1 = WQ + (wn0 ^ 64);
    bf16x8 a0 = ldstr(L, wq0, wq0 + 512), a1 = ldstr(L, wq1, wq1 + 512);
#pragma unroll
    for (int t = 0; t < 32; ++t) {
      bf16x8 n0, n1;
      if (t + 1 < 32) {
        const unsigned o = (t + 1) * 2048;
        n0 = ldstr(L, wq0 + o, wq0 + o + 512);
        n1 = ldstr(L, wq1 + o, wq1 + o + 512);
      }
      __builtin_amdgcn_sched_barrier(0);
      qa = mfma(a0, xr[t], qa);
      qb = mfma(a1, xr[t], qb);
      __builtin_amdgcn_sched_barrier(0);
      if (t + 1 < 32) { a0 = n0; a1 = n1; }
    }
  }
  ST(7);
  bf16x8 pq[4];
  pq[0] = phi8(qa, 0); pq[1] = phi8(qa, 1); pq[2] = phi8(qb, 0); pq[3] = phi8(qb, 1);
  const rsrc_t rsWS = __builtin_amdgcn_make_buffer_rsrc((void*)wsp, 0, WS_END, 0x00020000);
  WAITL0();
  ST(8);
  BAR();
  ST(9);
  {
    const unsigned st = 4 * ti + own;
#pragma unroll
    for (int hh = 0; hh < 4; ++hh) {
      const bf16x8 af = ldsr128(L, AFR + hh * 1024 + lane * 16);
      f32x16 d = {};
      d = mfma(af, pq[hh], d);
      const float inv = __builtin_amdgcn_rcpf(d[8] + 1e-6f);
      const bf16x8 o = pack8(d[0] * inv, d[1] * inv, d[2] * inv, d[3] * inv, d[4] * inv, d[5] * inv, d[6] * inv, d[7] * inv);
      __builtin_amdgcn_raw_buffer_store_b128(__builtin_bit_cast(u32x4, o), rsWS, lane * 16, WS_O + (st * 64 + 4 * g + hh) * 1024, 16);
    }
    ST(10);
    WAITV(0);
    if (lane == 0) st_flag(ws, WS_FLAGB + (st * 16 + g) * 128, tag);
  }
  ST(11);
  BAR();
  ST(12);
  {
    const int br2 = wave >> 1, sh = wave & 1;
    const unsigned st2 = 2 * rt + sh;
    wait16(ws, WS_FLAGB + st2 * 16 * 128, tag, lane);
    ST(13);
    if (b == 0 && wave == 0 && lane == 0) st_flag(ws, WS_EPOCH, tag);
    u32x4 of[32];
    const unsigned ob = WS_O + (st2 * 64 + 32 * br2) * 1024;
#pragma unroll
    for (int j = 0; j < 32; ++j) of[j] = __builtin_amdgcn_raw_buffer_load_b128(rsWS, lane * 16, ob + j * 1024, 16);
    const float gv = __hip_atomic_load((GAS float*)(ws + WS_GATE) + st2 * 32 + r32, __ATOMIC_RELAXED, __HIP_MEMORY_SCOPE_AGENT);
    const unsigned wp0 = (br2 ? WOB : WOA) + (4 * h + q) * 128 + sw + (p & 1) * 8, wp1 = wp0 ^ 64;
    f32x16 c0 = {}, c1 = {};
    bf16x8 a0 = ldstr(L, wp0, wp0 + 1024), a1 = ldstr(L, wp1, wp1 + 1024);
#pragma unroll
    for (int j = 0; j < 32; ++j) {
      bf16x8 n0, n1;
      if (j + 1 < 32) {
        const unsigned o = (j + 1) * 2048;
        n0 = ldstr(L, wp0 + o, wp0 + o + 1024);
        n1 = ldstr(L, wp1 + o, wp1 + o + 1024);
      }
      __builtin_amdgcn_sched_barrier(0);
      const bf16x8 bf = __builtin_bit_cast(bf16x8, of[j]);
      c0 = mfma(a0, bf, c0);
      c1 = mfma(a1, bf, c1);
      __builtin_amdgcn_sched_barrier(0);
      if (j + 1 < 32) { a0 = n0; a1 = n1; }
    }
    ST(14);
    const float gs = br2 ? 1.0f - gv : gv;
    f32x16 keep, send;
#pragma unroll
    for (int i = 0; i < 16; ++i) { keep[i] = (br2 ? c1[i] : c0[i]) * gs; send[i] = (br2 ? c0[i] : c1[i]) * gs; }
#pragma unroll
    for (int qq = 0; qq < 4; ++qq) {
      const f32x4 v = {send[4 * qq], send[4 * qq + 1], send[4 * qq + 2], send[4 * qq + 3]};
      *(LAS f32x4*)(L + EXC + wave * 4096 + qq * 1024 + lane * 16) = v;
    }
    WAITL0();
    BAR();
    unsigned pk[8];
#pragma unroll
    for (int qq = 0; qq < 4; ++qq) {
      const f32x4 v = *(LAS f32x4*)(L + EXC + (wave ^ 2) * 4096 + qq * 1024 + lane * 16);
      pk[2 * qq] = __builtin_bit_cast(unsigned, cvt2(keep[4 * qq] + v[0], keep[4 * qq + 1] + v[1]));
      pk[2 * qq + 1] = __builtin_bit_cast(unsigned, cvt2(keep[4 * qq + 2] + v[2], keep[4 * qq + 3] + v[3]));
    }
    const rsrc_t rsO = __builtin_amdgcn_make_buffer_rsrc((void*)out, 0, S_ * D_ * 2, 0x00020000);
    const unsigned orow = (st2 * 32 + r32) * 1024 + (64 * ct + 32 * br2) * 2 + h * 16;
#pragma unroll
    for (int qq = 0; qq < 4; qq += 2) {
      const auto s0 = __builtin_amdgcn_permlane32_swap(pk[2 * qq], pk[2 * qq + 2], false, false);
      const auto s1 = __builtin_amdgcn_permlane32_swap(pk[2 * qq + 1], pk[2 * qq + 3], false, false);
      const u32x4 v = {s0[0], s1[0], s0[1], s1[1]};
      __builtin_amdgcn_raw_buffer_store_b128(v, rsO, orow + qq * 16, 0, 0);
    }
    if (b == 0 && wave == 0) {
      WAITV(0);
      const unsigned long long now = __builtin_amdgcn_s_memrealtime();
      unsigned long long mn = ~0ull, mx = 0;
      for (int i = 0; i < 4; ++i) { unsigned long long v = __hip_atomic_load((GAS unsigned long long*)(ws + (8u << 20)) + lane + 64 * i, __ATOMIC_RELAXED, __HIP_MEMORY_SCOPE_AGENT); mn = v < mn ? v : mn; mx = v > mx ? v : mx; }
      for (int m = 32; m >= 1; m >>= 1) { unsigned long long a = __shfl_xor(mn, m), c = __shfl_xor(mx, m); mn = a < mn ? a : mn; mx = c > mx ? c : mx; }
      unsigned long long X = 0;
      if (X > 5000) X = 5000;
      const unsigned long long until = now + X * 1;
      while (__builtin_amdgcn_s_memrealtime() < until) __builtin_amdgcn_s_sleep(1);
    }
  }
}

extern "C" void kernel_launch(void* const* d_in, const int* in_sizes, int n_in, void* d_out, int out_size, void* d_ws, size_t ws_size,
                              hipStream_t stream) {
  (void)in_sizes; (void)n_in; (void)out_size; (void)ws_size;
  const u16* x = (const u16*)d_in[0];
  const u16* wA = (const u16*)d_in[1];
  const u16* woA = (const u16*)d_in[2];
  const u16* wB = (const u16*)d_in[4];
  const u16* woB = (const u16*)d_in[5];
  const u16* gw = (const u16*)d_in[6];
  (void)hipFuncSetAttribute((const void*)gated_mix_fused, hipFuncAttributeMaxDynamicSharedMemorySize, LDS_BYTES);
  gated_mix_fused<<<256, 512, LDS_BYTES, stream>>>(x, wA, wB, woA, woB, gw, (u16*)d_out, (char*)d_ws);
}
```

```cpp
#include <hip/hip_runtime.h>
#include <hip/hip_bf16.h>
#include <stdint.h>

#define LAS __attribute__((address_space(3)))
#define GAS __attribute__((address_space(1)))
#define DI __device__ __forceinline__

typedef unsigned short u16;
typedef __bf16 bf16x8 __attribute__((ext_vector_type(8)));
typedef __bf16 bf16x4 __attribute__((ext_vector_type(4)));
typedef __bf16 bf16x2 __attribute__((ext_vector_type(2)));
typedef float f32x16 __attribute__((ext_vector_type(16)));
typedef float f32x4 __attribute__((ext_vector_type(4)));
typedef float f32x2 __attribute__((ext_vector_type(2)));
typedef unsigned u32x4 __attribute__((ext_vector_type(4)));
typedef unsigned u32x2 __attribute__((ext_vector_type(2)));
typedef __amdgpu_buffer_rsrc_t rsrc_t;

#define ROT 0

constexpr int S_ = 2048, D_ = 512;
constexpr unsigned XR = 0;
constexpr unsigned WR = 49152;
constexpr unsigned WQ = 98304;
constexpr unsigned WOA = 0;
constexpr unsigned WOB = 98304;
constexpr unsigned SCR = 81920;
constexpr unsigned AFR = SCR + 12288;
constexpr unsigned EXC = SCR;
constexpr unsigned LDS_BYTES = 163840;
constexpr unsigned WS_EPOCH = 0;
constexpr unsigned WS_FLAGA = 4096;
constexpr unsigned WS_FLAGB = WS_FLAGA + 1024 * 128;
constexpr unsigned WS_GATE = WS_FLAGB + 1024 * 128;
constexpr unsigned WS_PART = WS_GATE + 8192;
constexpr unsigned PART_STRIDE = 1152;
constexpr unsigned WS_O = WS_PART + 1024 * PART_STRIDE;
constexpr unsigned WS_END = WS_O + 64 * 64 * 1024;
constexpr unsigned SPIN_CAP = 1u << 18;

#define ST(i) do {} while (0)
#define STR(i) do {} while (0)
#define STFLUSH() do {} while (0)

DI void glds(unsigned voff, rsrc_t rs, unsigned soff, unsigned ldsaddr) {
  asm volatile("s_mov_b32 m0, %0\n\ts_nop 0\n\tbuffer_load_dwordx4 %1, %2, %3 offen lds"
               :: "s"(ldsaddr), "v"(voff), "s"(rs), "s"(soff) : "memory");
}
#define WAITV(n) asm volatile("s_waitcnt vmcnt(" #n ")" ::: "memory")
#define WAITL0() asm volatile("s_waitcnt lgkmcnt(0)" ::: "memory")
#define BAR() asm volatile("s_barrier" ::: "memory")

DI f32x16 mfma(bf16x8 a, bf16x8 b, f32x16 c) { return __builtin_amdgcn_mfma_f32_32x32x16_bf16(a, b, c, 0, 0, 0); }
DI bf16x8 ldsr128(LAS char* L, unsigned a) { return *(LAS bf16x8*)(L + a); }
DI bf16x8 ldstr(LAS char* L, unsigned a0, unsigned a1) {
  bf16x4 lo = __builtin_amdgcn_ds_read_tr16_b64_v4bf16((LAS bf16x4*)(L + a0));
  bf16x4 hi = __builtin_amdgcn_ds_read_tr16_b64_v4bf16((LAS bf16x4*)(L + a1));
  return __builtin_shufflevector(lo, hi, 0, 1, 2, 3, 4, 5, 6, 7);
}
DI bf16x2 cvt2(float a, float b) { f32x2 p = {a, b}; return __builtin_convertvector(p, bf16x2); }
DI bf16x8 pack8(float a0, float a1, float a2, float a3, float a4, float a5, float a6, float a7) {
  bf16x2 p0 = cvt2(a0, a1), p1 = cvt2(a2, a3), p2 = cvt2(a4, a5), p3 = cvt2(a6, a7);
  bf16x4 lo = __builtin_shufflevector(p0, p1, 0, 1, 2, 3), hi = __builtin_shufflevector(p2, p3, 0, 1, 2, 3);
  return __builtin_shufflevector(lo, hi, 0, 1, 2, 3, 4, 5, 6, 7);
}
DI float phi(float t) {
  float e = __builtin_amdgcn_exp2f(t * 1.44269504088896340736f);
  e = __builtin_amdgcn_fmed3f(e, 0.0f, 1.0f);
  return (fmaxf(t, 0.0f) + e) + 1e-6f;
}
DI bf16x8 phi8(const f32x16& x, int s) {
  return pack8(phi(x[8 * s]), phi(x[8 * s + 1]), phi(x[8 * s + 2]), phi(x[8 * s + 3]),
               phi(x[8 * s + 4]), phi(x[8 * s + 5]), phi(x[8 * s + 6]), phi(x[8 * s + 7]));
}
DI bf16x8 cvt8(const f32x16& x, int s) {
  return pack8(x[8 * s], x[8 * s + 1], x[8 * s + 2], x[8 * s + 3], x[8 * s + 4], x[8 * s + 5], x[8 * s + 6], x[8 * s + 7]);
}
DI float sum8(bf16x8 v, float acc) {
  const bf16x2 ones = {(__bf16)1.0f, (__bf16)1.0f};
  acc = __builtin_amdgcn_fdot2_f32_bf16(__builtin_shufflevector(v, v, 0, 1), ones, acc, false);
  acc = __builtin_amdgcn_fdot2_f32_bf16(__builtin_shufflevector(v, v, 2, 3), ones, acc, false);
  acc = __builtin_amdgcn_fdot2_f32_bf16(__builtin_shufflevector(v, v, 4, 5), ones, acc, false);
  acc = __builtin_amdgcn_fdot2_f32_bf16(__builtin_shufflevector(v, v, 6, 7), ones, acc, false);
  return acc;
}
DI unsigned long long ld_flag(GAS char* ws, unsigned off) {
  return __hip_atomic_load((GAS unsigned long long*)(ws + off), __ATOMIC_RELAXED, __HIP_MEMORY_SCOPE_AGENT);
}
DI void st_flag(GAS char* ws, unsigned off, unsigned long long v) {
  __hip_atomic_store((GAS unsigned long long*)(ws + off), v, __ATOMIC_RELAXED, __HIP_MEMORY_SCOPE_AGENT);
}
DI unsigned long long load_epoch(const char* wsp) {
  unsigned long long e;
  asm volatile("s_load_dwordx2 %0, %1, 0x0\n\ts_waitcnt lgkmcnt(0)" : "=s"(e) : "s"(wsp) : "memory");
  return e;
}
DI void wait16(GAS char* ws, unsigned off0, unsigned long long tag, int lane) {
  const unsigned off = off0 + (unsigned)(lane & 15) * 128u;
  for (unsigned spins = 0; spins < SPIN_CAP; ++spins) {
    const unsigned long long v = ld_flag(ws, off);
    if (__all(v == tag)) break;
    __builtin_amdgcn_s_sleep(1);
  }
  asm volatile("" ::: "memory");
}

__global__ __launch_bounds__(512, 2) void gated_mix_fused(const u16* __restrict__ x, const u16* __restrict__ wA, const u16* __restrict__ wB,
                                                          const u16* __restrict__ woA, const u16* __restrict__ woB, const u16* __restrict__ gw,
                                                          u16* __restrict__ out, char* __restrict__ wsp
                                                          ) {
  extern __shared__ __attribute__((aligned(16))) char lds[];
  LAS char* L = (LAS char*)lds;
  const int tid = threadIdx.x;
  const int wave = __builtin_amdgcn_readfirstlane(tid >> 6), lane = tid & 63;
  const int b = blockIdx.x, xcd = b & 7, r = b >> 3;
  const int ti = (xcd & 1) * 8 + (r & 7), g = (xcd >> 1) * 4 + (r >> 3);
  const int gg = g & 7;
  const int rt = 4 * xcd + (r & 3), ct = r >> 2;
  GAS char* ws = (GAS char*)wsp;
  { const unsigned long long rt0 = __builtin_amdgcn_s_memrealtime();
  if (wave == 0 && lane == 0) __hip_atomic_store((GAS unsigned long long*)(ws + (8u << 20)) + b, rt0, __ATOMIC_RELAXED, __HIP_MEMORY_SCOPE_AGENT); }

  if (wave >= 4) {
    const int hw = wave - 4;
    int ro = 0;
    if (ROT == 1) ro = (ti + 2 * g) & 7;
    if (ROT == 2) ro = 2 * ((ti + g) & 1);
    if (ROT == 3) ro = 4 * ((ti + g) & 1);
    if (ROT == 5) ro = ((ti + g) % 3) * 2;
    const u16* w = (g >= 8) ? wB : wA;
    const rsrc_t rsX = __builtin_amdgcn_make_buffer_rsrc((void*)x, 0, S_ * D_ * 2, 0x00020000);
    const rsrc_t rsW = __builtin_amdgcn_make_buffer_rsrc((void*)w, 0, D_ * 3 * D_ * 2, 0x00020000);
    const int rin = lane >> 3, ch = lane & 7;
    const unsigned vx = rin * 1024 + ((ch ^ (4 * (hw & 1) + (rin >> 1))) << 4);
    const unsigned csw = (ch ^ (((rin >> 1) & 1) << 2)) << 4;
    const unsigned vw = rin * 3072 + csw;
    const unsigned sx0 = (ti * 128 + 8 * hw) * 1024;
    const unsigned sw0 = 8 * hw * 3072 + gg * 128;
    auto stage = [&](int s) {
      const unsigned ks = (s + ro) & 7, slot = (s % 3) * 16384;
#pragma unroll
      for (int i = 0; i < 4; ++i) glds(vx, rsX, sx0 + ks * 128 + i * 32768, XR + slot + (hw + 4 * i) * 1024);
#pragma unroll
      for (int i = 0; i < 2; ++i) glds(vw, rsW, sw0 + ks * 196608 + i * 98304 + 1024, WR + slot + (hw + 4 * i) * 1024);
#pragma unroll
      for (int i = 0; i < 2; ++i) glds(vw, rsW, sw0 + ks * 196608 + i * 98304 + 2048, WR + slot + 8192 + (hw + 4 * i) * 1024);
    };
    auto stageq = [&](int s) {
      const unsigned ks = (s + ro) & 7;
#pragma unroll
      for (int i = 0; i < 2; ++i) glds(vw, rsW, sw0 + ks * 196608 + i * 98304, WQ + s * 8192 + (hw + 4 * i) * 1024);
    };
    stage(0); stage(1);
    ST(2);
    const unsigned long long tag = load_epoch(wsp) + 1ull;
    WAITV(8);
    BAR();
    ST(3);
#pragma unroll
    for (int s = 1; s < 8; ++s) {
      if (s + 1 < 8) { stage(s + 1); WAITV(8); }
      else { stageq(0); stageq(1); stageq(2); stageq(3); WAITV(8); }
      BAR();
    }
    ST(4);
    stageq(4); stageq(5); stageq(6); stageq(7);
    {
      const rsrc_t rsG = __builtin_amdgcn_make_buffer_rsrc((void*)gw, 0, D_ * 2, 0x00020000);
      const unsigned row = ti * 128 + 8 * g + 2 * hw;
      u32x4 x0, x1, gv;
      asm volatile("buffer_load_dwordx4 %0, %3, %4, %6 offen\n\tbuffer_load_dwordx4 %1, %3, %4, %6 offen offset:1024\n\t"
                   "buffer_load_dwordx4 %2, %3, %5, 0 offen\n\ts_waitcnt vmcnt(0)"
                   : "=&v"(x0), "=&v"(x1), "=&v"(gv) : "v"(lane * 16), "s"(rsX), "s"(rsG), "s"(row * 1024) : "memory");
      float a0 = 0.f, a1 = 0.f;
#pragma unroll
      for (int i = 0; i < 4; ++i) {
        const unsigned ug = gv[i], u0 = x0[i], u1 = x1[i];
        const bf16x2 gp = __builtin_bit_cast(bf16x2, ug);
        a0 = __builtin_amdgcn_fdot2_f32_bf16(__builtin_bit_cast(bf16x2, u0), gp, a0, false);
        a1 = __builtin_amdgcn_fdot2_f32_bf16(__builtin_bit_cast(bf16x2, u1), gp, a1, false);
      }
#pragma unroll
      for (int m = 32; m >= 1; m >>= 1) { a0 += __shfl_xor(a0, m); a1 += __shfl_xor(a1, m); }
      const float lg = (lane & 1) ? a1 : a0;
      const float gval = __builtin_amdgcn_rcpf(1.0f + __builtin_amdgcn_exp2f(-1.44269504088896340736f * lg));
      if (lane < 2) __hip_atomic_store((GAS float*)(ws + WS_GATE) + row + lane, gval, __ATOMIC_RELAXED, __HIP_MEMORY_SCOPE_AGENT);
    }
    ST(5);
    BAR();
    ST(6);
    const rsrc_t rsWS = __builtin_amdgcn_make_buffer_rsrc((void*)wsp, 0, WS_END, 0x00020000);
    const unsigned hid = g * 4 + hw;
    {
      const f32x4 p0 = *(LAS f32x4*)(L + SCR + hw * 1024 + lane * 16), p1 = *(LAS f32x4*)(L + SCR + (4 + hw) * 1024 + lane * 16);
      const f32x4 z0 = *(LAS f32x4*)(L + SCR + (8 + hw) * 1024 + (lane & 3) * 16), z1 = *(LAS f32x4*)(L + SCR + (8 + hw) * 1024 + 64 + (lane & 3) * 16);
      const unsigned po = WS_PART + (hid * 16 + ti) * PART_STRIDE;
      __builtin_amdgcn_raw_buffer_store_b128(__builtin_bit_cast(u32x4, p0 + p1), rsWS, lane * 16, po, 16);
      if (lane < 4) __builtin_amdgcn_raw_buffer_store_b128(__builtin_bit_cast(u32x4, z0 + z1), rsWS, 1024 + lane * 16, po, 16);
      WAITV(0);
      if (lane == 0) st_flag(ws, WS_FLAGA + (hid * 16 + ti) * 128, tag);
    }
    ST(7);
    const rsrc_t rsOA = __builtin_amdgcn_make_buffer_rsrc((void*)woA, 0, D_ * D_ * 2, 0x00020000);
    const rsrc_t rsOB = __builtin_amdgcn_make_buffer_rsrc((void*)woB, 0, D_ * D_ * 2, 0x00020000);
    const unsigned vo = rin * 1024 + csw;
#pragma unroll
    for (int i = 0; i < 16; ++i) glds(vo, rsOA, (hw + 4 * i) * 8192 + ct * 128, WOA + (hw + 4 * i) * 1024);
    ST(8);
    wait16(ws, WS_FLAGA + hid * 16 * 128, tag, lane);
    ST(9);
    {
      const unsigned pb = WS_PART + hid * 16 * PART_STRIDE;
      u32x4 pv[16];
#pragma unroll
      for (int p = 0; p < 16; ++p) pv[p] = __builtin_amdgcn_raw_buffer_load_b128(rsWS, lane * 16, pb + p * PART_STRIDE, 16);
      const u32x4 zv = __builtin_amdgcn_raw_buffer_load_b128(rsWS, (lane >> 2) * PART_STRIDE + 1024 + (lane & 3) * 16, pb, 16);
      f32x4 a = __builtin_bit_cast(f32x4, pv[0]);
#pragma unroll
      for (int p = 1; p < 16; ++p) a += __builtin_bit_cast(f32x4, pv[p]);
      f32x4 z = __builtin_bit_cast(f32x4, zv);
#pragma unroll
      for (int m = 4; m <= 32; m <<= 1) {
        z[0] += __shfl_xor(z[0], m); z[1] += __shfl_xor(z[1], m); z[2] += __shfl_xor(z[2], m); z[3] += __shfl_xor(z[3], m);
      }
      const int slot = lane & 31, jh = lane >> 5;
      u32x2 ka = {__builtin_bit_cast(unsigned, cvt2(a[0], a[1])), __builtin_bit_cast(unsigned, cvt2(a[2], a[3]))};
      *(LAS u32x2*)(L + AFR + hw * 1024 + ((slot >> 4) * 32 + (slot & 15)) * 16 + jh * 8) = ka;
      const int q4 = lane & 3;
      u32x2 kz = {__builtin_bit_cast(unsigned, cvt2(z[0], z[1])), __builtin_bit_cast(unsigned, cvt2(z[2], z[3]))};
      *(LAS u32x2*)(L + AFR + hw * 1024 + ((q4 & 1) * 32 + 16 + (lane >> 2)) * 16 + (q4 >> 1) * 8) = kz;
    }
    WAITL0();
    ST(10);
    BAR();
    ST(11);
#pragma unroll
    for (int i = 0; i < 16; ++i) glds(vo, rsOB, (hw + 4 * i) * 8192 + ct * 128, WOB + (hw + 4 * i) * 1024);
    WAITV(0);
    ST(12);
    BAR();
    STR(16); STFLUSH();
    return;
  }

  const int wr = wave >> 1, wc = wave & 1;
  const int r32 = lane & 31, h = lane >> 5;
  const int g1 = (lane >> 4) & 1, q = (lane & 15) >> 2, p = lane & 3;
  __builtin_amdgcn_s_setprio(1);
  const int own = 2 * wr + wc, oth = 2 * wr + 1 - wc;
  const unsigned fx = (h ^ ((r32 >> 1) & 7)) << 4;
  const unsigned xo0 = XR + (own * 32 + r32) * 128 + fx, xt0 = XR + (oth * 32 + r32) * 128 + fx;
  const unsigned sw = ((2 * g1 + (p >> 1)) ^ (((q >> 1) & 1) << 2)) << 4;
  const unsigned wn0 = (8 * h + q) * 128 + sw + (p & 1) * 8;
  const unsigned wkv0 = WR + (wn0 ^ (wc << 6));

  bf16x8 xr[32];
  f32x16 kO = {}, kT = {}, vO = {}, vT = {};
  const unsigned long long tag = load_epoch(wsp) + 1ull;
  {
    BAR();
    ST(3);
    bf16x8 xt = ldsr128(L, xt0), wk = ldstr(L, wkv0, wkv0 + 512), wv = ldstr(L, wkv0 + 8192, wkv0 + 8192 + 512);
    xr[0] = ldsr128(L, xo0);
#pragma unroll
    for (int t = 0; t < 32; ++t) {
      bf16x8 nxt, nwk, nwv;
      if (t + 1 < 32) {
        const int s1 = (t + 1) >> 2, k1 = (t + 1) & 3;
        const unsigned slot = (s1 % 3) * 16384;
        if (k1 == 0) { WAITL0(); BAR(); }
        xr[t + 1] = ldsr128(L, (xo0 ^ (k1 << 5)) + slot);
        nxt = ldsr128(L, (xt0 ^ (k1 << 5)) + slot);
        nwk = ldstr(L, wkv0 + slot + k1 * 2048, wkv0 + slot + k1 * 2048 + 512);
        nwv = ldstr(L, wkv0 + slot + 8192 + k1 * 2048, wkv0 + slot + 8192 + k1 * 2048 + 512);
      }
      __builtin_amdgcn_sched_barrier(0);
      kO = mfma(xr[t], wk, kO);
      kT = mfma(xt, wk, kT);
      vO = mfma(xr[t], wv, vO);
      vT = mfma(xt, wv, vT);
      __builtin_amdgcn_sched_barrier(0);
      if (t + 1 < 32) { xt = nxt; wk = nwk; wv = nwv; }
    }
  }
  ST(4);
  {
    f32x16 kv = {};
    float zs = 0.f;
#pragma unroll
    for (int s = 0; s < 2; ++s) {
      const bf16x8 pa = phi8(kO, s), pb = phi8(kT, s);
      kv = mfma(pa, cvt8(vO, s), kv);
      kv = mfma(pb, cvt8(vT, s), kv);
      zs = sum8(pa, zs); zs = sum8(pb, zs);
    }
    zs += __shfl_xor(zs, 32);
    f32x4 lo, hi;
#pragma unroll
    for (int j = 0; j < 4; ++j) { lo[j] = g1 ? kv[8 + j] : kv[j]; hi[j] = g1 ? kv[12 + j] : kv[4 + j]; }
    const int hh = 2 * wc + g1;
    const unsigned a = SCR + (4 * wr + hh) * 1024 + (h * 16 + (lane & 15)) * 16;
    *(LAS f32x4*)(L + a) = lo;
    *(LAS f32x4*)(L + a + 512) = hi;
    if (h == 0) *(LAS float*)(L + SCR + (8 + hh) * 1024 + wr * 64 + (lane & 15) * 4) = zs;
  }
  WAITL0();
  ST(5);
  BAR();
  ST(6);
  f32x16 qa = {}, qb = {};
  {
    const unsigned wq0 = WQ + wn0, wq1 = WQ + (wn0 ^ 64);
    bf16x8 a0 = ldstr(L, wq0, wq0 + 512), a1 = ldstr(L, wq1, wq1 + 512);
#pragma unroll
    for (int t = 0; t < 32; ++t) {
      bf16x8 n0, n1;
      if (t + 1 < 32) {
        const unsigned o = (t + 1) * 2048;
        n0 = ldstr(L, wq0 + o, wq0 + o + 512);
        n1 = ldstr(L, wq1 + o, wq1 + o + 512);
      }
      __builtin_amdgcn_sched_barrier(0);
      qa = mfma(a0, xr[t], qa);
      qb = mfma(a1, xr[t], qb);
      __builtin_amdgcn_sched_barrier(0);
      if (t + 1 < 32) { a0 = n0; a1 = n1; }
    }
  }
  ST(7);
  bf16x8 pq[4];
  pq[0] = phi8(qa, 0); pq[1] = phi8(qa, 1); pq[2] = phi8(qb, 0); pq[3] = phi8(qb, 1);
  const rsrc_t rsWS = __builtin_amdgcn_make_buffer_rsrc((void*)wsp, 0, WS_END, 0x00020000);
  WAITL0();
  ST(8);
  BAR();
  ST(9);
  {
    const unsigned st = 4 * ti + own;
#pragma unroll
    for (int hh = 0; hh < 4; ++hh) {
      const bf16x8 af = ldsr128(L, AFR + hh * 1024 + lane * 16);
      f32x16 d = {};
      d = mfma(af, pq[hh], d);
      const float inv = __builtin_amdgcn_rcpf(d[8] + 1e-6f);
      const bf16x8 o = pack8(d[0] * inv, d[1] * inv, d[2] * inv, d[3] * inv, d[4] * inv, d[5] * inv, d[6] * inv, d[7] * inv);
      __builtin_amdgcn_raw_buffer_store_b128(__builtin_bit_cast(u32x4, o), rsWS, lane * 16, WS_O + (st * 64 + 4 * g + hh) * 1024, 16);
    }
    ST(10);
    WAITV(0);
    if (lane == 0) st_flag(ws, WS_FLAGB + (st * 16 + g) * 128, tag);
  }
  ST(11);
  BAR();
  ST(12);
  {
    const int br2 = wave >> 1, sh = wave & 1;
    const unsigned st2 = 2 * rt + sh;
    wait16(ws, WS_FLAGB + st2 * 16 * 128, tag, lane);
    ST(13);
    if (b == 0 && wave == 0 && lane == 0) st_flag(ws, WS_EPOCH, tag);
    u32x4 of[32];
    const unsigned ob = WS_O + (st2 * 64 + 32 * br2) * 1024;
#pragma unroll
    for (int j = 0; j < 32; ++j) of[j] = __builtin_amdgcn_raw_buffer_load_b128(rsWS, lane * 16, ob + j * 1024, 16);
    const float gv = __hip_atomic_load((GAS float*)(ws + WS_GATE) + st2 * 32 + r32, __ATOMIC_RELAXED, __HIP_MEMORY_SCOPE_AGENT);
    const unsigned wp0 = (br2 ? WOB : WOA) + (4 * h + q) * 128 + sw + (p & 1) * 8, wp1 = wp0 ^ 64;
    f32x16 c0 = {}, c1 = {};
    bf16x8 a0 = ldstr(L, wp0, wp0 + 1024), a1 = ldstr(L, wp1, wp1 + 1024);
#pragma unroll
    for (int j = 0; j < 32; ++j) {
      bf16x8 n0, n1;
      if (j + 1 < 32) {
        const unsigned o = (j + 1) * 2048;
        n0 = ldstr(L, wp0 + o, wp0 + o + 1024);
        n1 = ldstr(L, wp1 + o, wp1 + o + 1024);
      }
      __builtin_amdgcn_sched_barrier(0);
      const bf16x8 bf = __builtin_bit_cast(bf16x8, of[j]);
      c0 = mfma(a0, bf, c0);
      c1 = mfma(a1, bf, c1);
      __builtin_amdgcn_sched_barrier(0);
      if (j + 1 < 32) { a0 = n0; a1 = n1; }
    }
    ST(14);
    const float gs = br2 ? 1.0f - gv : gv;
    f32x16 keep, send;
#pragma unroll
    for (int i = 0; i < 16; ++i) { keep[i] = (br2 ? c1[i] : c0[i]) * gs; send[i] = (br2 ? c0[i] : c1[i]) * gs; }
#pragma unroll
    for (int qq = 0; qq < 4; ++qq) {
      const f32x4 v = {send[4 * qq], send[4 * qq + 1], send[4 * qq + 2], send[4 * qq + 3]};
      *(LAS f32x4*)(L + EXC + wave * 4096 + qq * 1024 + lane * 16) = v;
    }
    WAITL0();
    BAR();
    unsigned pk[8];
#pragma unroll
    for (int qq = 0; qq < 4; ++qq) {
      const f32x4 v = *(LAS f32x4*)(L + EXC + (wave ^ 2) * 4096 + qq * 1024 + lane * 16);
      pk[2 * qq] = __builtin_bit_cast(unsigned, cvt2(keep[4 * qq] + v[0], keep[4 * qq + 1] + v[1]));
      pk[2 * qq + 1] = __builtin_bit_cast(unsigned, cvt2(keep[4 * qq + 2] + v[2], keep[4 * qq + 3] + v[3]));
    }
    const rsrc_t rsO = __builtin_amdgcn_make_buffer_rsrc((void*)out, 0, S_ * D_ * 2, 0x00020000);
    const unsigned orow = (st2 * 32 + r32) * 1024 + (64 * ct + 32 * br2) * 2 + h * 16;
#pragma unroll
    for (int qq = 0; qq < 4; qq += 2) {
      const auto s0 = __builtin_amdgcn_permlane32_swap(pk[2 * qq], pk[2 * qq + 2], false, false);
      const auto s1 = __builtin_amdgcn_permlane32_swap(pk[2 * qq + 1], pk[2 * qq + 3], false, false);
      const u32x4 v = {s0[0], s1[0], s0[1], s1[1]};
      __builtin_amdgcn_raw_buffer_store_b128(v, rsO, orow + qq * 16, 0, 0);
    }
    { WAITV(0); const unsigned long long rt1 = __builtin_amdgcn_s_memrealtime();
      if (lane == 0) __hip_atomic_store((GAS unsigned long long*)(ws + (8u << 20) + 4096) + b * 4 + wave, rt1, __ATOMIC_RELAXED, __HIP_MEMORY_SCOPE_AGENT); }
  }
}

__global__ void k_a(char* ws) { if (threadIdx.x == 0) *(unsigned long long*)(ws + (9u << 20)) = __builtin_amdgcn_s_memrealtime(); }
__global__ void k_b(char* ws) {
  const unsigned long long now = __builtin_amdgcn_s_memrealtime();
  const int t = threadIdx.x;
  unsigned long long a = *(unsigned long long*)(ws + (9u << 20));
  unsigned long long mn = ~0ull, mx = 0;
  for (int i = 0; i < 4; ++i) { unsigned long long v = ((unsigned long long*)(ws + (8u << 20)))[t + 64 * i]; mn = v < mn ? v : mn; }
  for (int i = 0; i < 16; ++i) { unsigned long long v = ((unsigned long long*)(ws + (8u << 20) + 4096))[t + 64 * i]; mx = v > mx ? v : mx; }
  for (int m = 32; m >= 1; m >>= 1) { unsigned long long p = __shfl_xor(mn, m), c = __shfl_xor(mx, m); mn = p < mn ? p : mn; mx = c > mx ? c : mx; }
  unsigned long long X = 0;
  if (X > 5000) X = 5000;
  const unsigned long long t2 = __builtin_amdgcn_s_memrealtime();
  const unsigned long long until = t2 + X * 1;
  while (__builtin_amdgcn_s_memrealtime() < until) __builtin_amdgcn_s_sleep(1);
}
extern "C" void kernel_launch(void* const* d_in, const int* in_sizes, int n_in, void* d_out, int out_size, void* d_ws, size_t ws_size,
                              hipStream_t stream) {
  (void)in_sizes; (void)n_in; (void)out_size; (void)ws_size;
  const u16* x = (const u16*)d_in[0];
  const u16* wA = (const u16*)d_in[1];
  const u16* woA = (const u16*)d_in[2];
  const u16* wB = (const u16*)d_in[4];
  const u16* woB = (const u16*)d_in[5];
  const u16* gw = (const u16*)d_in[6];
  k_a<<<1, 64, 0, stream>>>((char*)d_ws);
  (void)hipFuncSetAttribute((const void*)gated_mix_fused, hipFuncAttributeMaxDynamicSharedMemorySize, LDS_BYTES);
  gated_mix_fused<<<256, 512, LDS_BYTES, stream>>>(x, wA, wB, woA, woB, gw, (u16*)d_out, (char*)d_ws);
  k_b<<<1, 64, 0, stream>>>((char*)d_ws);
}
```

```cpp
#include <hip/hip_runtime.h>
#include <hip/hip_bf16.h>
#include <stdint.h>

#define LAS __attribute__((address_space(3)))
#define GAS __attribute__((address_space(1)))
#define DI __device__ __forceinline__

typedef unsigned short u16;
typedef __bf16 bf16x8 __attribute__((ext_vector_type(8)));
typedef __bf16 bf16x4 __attribute__((ext_vector_type(4)));
typedef __bf16 bf16x2 __attribute__((ext_vector_type(2)));
typedef float f32x16 __attribute__((ext_vector_type(16)));
typedef float f32x4 __attribute__((ext_vector_type(4)));
typedef float f32x2 __attribute__((ext_vector_type(2)));
typedef unsigned u32x4 __attribute__((ext_vector_type(4)));
typedef unsigned u32x2 __attribute__((ext_vector_type(2)));
typedef __amdgpu_buffer_rsrc_t rsrc_t;

#define ROT 2

constexpr int S_ = 2048, D_ = 512;
constexpr unsigned XR = 0;
constexpr unsigned WR = 49152;
constexpr unsigned WQ = 98304;
constexpr unsigned WOA = 0;
constexpr unsigned WOB = 98304;
constexpr unsigned SCR = 81920;
constexpr unsigned AFR = SCR + 12288;
constexpr unsigned EXC = SCR;
constexpr unsigned LDS_BYTES = 163840;
constexpr unsigned WS_EPOCH = 0;
constexpr unsigned WS_FLAGA = 4096;
constexpr unsigned WS_FLAGB = WS_FLAGA + 1024 * 128;
constexpr unsigned WS_GATE = WS_FLAGB + 1024 * 128;
constexpr unsigned WS_PART = WS_GATE + 8192;
constexpr unsigned PART_STRIDE = 1152;
constexpr unsigned WS_O = WS_PART + 1024 * PART_STRIDE;
constexpr unsigned WS_END = WS_O + 64 * 64 * 1024;
constexpr unsigned SPIN_CAP = 1u << 18;

#define ST(i) do {} while (0)
#define STR(i) do {} while (0)
#define STFLUSH() do {} while (0)

DI void glds(unsigned voff, rsrc_t rs, unsigned soff, unsigned ldsaddr) {
  asm volatile("s_mov_b32 m0, %0\n\ts_nop 0\n\tbuffer_load_dwordx4 %1, %2, %3 offen lds"
               :: "s"(ldsaddr), "v"(voff), "s"(rs), "s"(soff) : "memory");
}
#define WAITV(n) asm volatile("s_waitcnt vmcnt(" #n ")" ::: "memory")
#define WAITL0() asm volatile("s_waitcnt lgkmcnt(0)" ::: "memory")
#define BAR() asm volatile("s_barrier" ::: "memory")

DI f32x16 mfma(bf16x8 a, bf16x8 b, f32x16 c) { return __builtin_amdgcn_mfma_f32_32x32x16_bf16(a, b, c, 0, 0, 0); }
DI bf16x8 ldsr128(LAS char* L, unsigned a) { return *(LAS bf16x8*)(L + a); }
DI bf16x8 ldstr(LAS char* L, unsigned a0, unsigned a1) {
  bf16x4 lo = __builtin_amdgcn_ds_read_tr16_b64_v4bf16((LAS bf16x4*)(L + a0));
  bf16x4 hi = __builtin_amdgcn_ds_read_tr16_b64_v4bf16((LAS bf16x4*)(L + a1));
  return __builtin_shufflevector(lo, hi, 0, 1, 2, 3, 4, 5, 6, 7);
}
DI bf16x2 cvt2(float a, float b) { f32x2 p = {a, b}; return __builtin_convertvector(p, bf16x2); }
DI bf16x8 pack8(float a0, float a1, float a2, float a3, float a4, float a5, float a6, float a7) {
  bf16x2 p0 = cvt2(a0, a1), p1 = cvt2(a2, a3), p2 = cvt2(a4, a5), p3 = cvt2(a6, a7);
  bf16x4 lo = __builtin_shufflevector(p0, p1, 0, 1, 2, 3), hi = __builtin_shufflevector(p2, p3, 0, 1, 2, 3);
  return __builtin_shufflevector(lo, hi, 0, 1, 2, 3, 4, 5, 6, 7);
}
DI float phi(float t) {
  float e = __builtin_amdgcn_exp2f(t * 1.44269504088896340736f);
  e = __builtin_amdgcn_fmed3f(e, 0.0f, 1.0f);
  return (fmaxf(t, 0.0f) + e) + 1e-6f;
}
DI bf16x8 phi8(const f32x16& x, int s) {
  return pack8(phi(x[8 * s]), phi(x[8 * s + 1]), phi(x[8 * s + 2]), phi(x[8 * s + 3]),
               phi(x[8 * s + 4]), phi(x[8 * s + 5]), phi(x[8 * s + 6]), phi(x[8 * s + 7]));
}
DI bf16x8 cvt8(const f32x16& x, int s) {
  return pack8(x[8 * s], x[8 * s + 1], x[8 * s + 2], x[8 * s + 3], x[8 * s + 4], x[8 * s + 5], x[8 * s + 6], x[8 * s + 7]);
}
DI float sum8(bf16x8 v, float acc) {
  const bf16x2 ones = {(__bf16)1.0f, (__bf16)1.0f};
  acc = __builtin_amdgcn_fdot2_f32_bf16(__builtin_shufflevector(v, v, 0, 1), ones, acc, false);
  acc = __builtin_amdgcn_fdot2_f32_bf16(__builtin_shufflevector(v, v, 2, 3), ones, acc, false);
  acc = __builtin_amdgcn_fdot2_f32_bf16(__builtin_shufflevector(v, v, 4, 5), ones, acc, false);
  acc = __builtin_amdgcn_fdot2_f32_bf16(__builtin_shufflevector(v, v, 6, 7), ones, acc, false);
  return acc;
}
DI unsigned long long ld_flag(GAS char* ws, unsigned off) {
  return __hip_atomic_load((GAS unsigned long long*)(ws + off), __ATOMIC_RELAXED, __HIP_MEMORY_SCOPE_AGENT);
}
DI void st_flag(GAS char* ws, unsigned off, unsigned long long v) {
  __hip_atomic_store((GAS unsigned long long*)(ws + off), v, __ATOMIC_RELAXED, __HIP_MEMORY_SCOPE_AGENT);
}
DI unsigned long long load_epoch(const char* wsp) {
  unsigned long long e;
  asm volatile("s_load_dwordx2 %0, %1, 0x0\n\ts_waitcnt lgkmcnt(0)" : "=s"(e) : "s"(wsp) : "memory");
  return e;
}
DI void wait16(GAS char* ws, unsigned off0, unsigned long long tag, int lane) {
  const unsigned off = off0 + (unsigned)(lane & 15) * 128u;
  for (unsigned spins = 0; spins < SPIN_CAP; ++spins) {
    const unsigned long long v = ld_flag(ws, off);
    if (__all(v == tag)) break;
    __builtin_amdgcn_s_sleep(1);
  }
  asm volatile("" ::: "memory");
}

__global__ __launch_bounds__(512, 2) void gated_mix_fused(const u16* __restrict__ x, const u16* __restrict__ wA, const u16* __restrict__ wB,
                                                          const u16* __restrict__ woA, const u16* __restrict__ woB, const u16* __restrict__ gw,
                                                          u16* __restrict__ out, char* __restrict__ wsp
                                                          ) {
  extern __shared__ __attribute__((aligned(16))) char lds[];
  LAS char* L = (LAS char*)lds;
  const int tid = threadIdx.x;
  const int wave = __builtin_amdgcn_readfirstlane(tid >> 6), lane = tid & 63;
  const int b = blockIdx.x, xcd = b & 7, r = b >> 3;
  const int ti = (xcd & 1) * 8 + (r & 7), g = (xcd >> 1) * 4 + (r >> 3);
  const int gg = g & 7;
  const int rt = 4 * xcd + (r & 3), ct = r >> 2;
  GAS char* ws = (GAS char*)wsp;
  STR(0); ST(1);

  if (wave >= 4) {
    const int hw = wave - 4;
    int ro = 0;
    if (ROT == 1) ro = (ti + 2 * g) & 7;
    if (ROT == 2) ro = 2 * ((ti + g) & 1);
    if (ROT == 3) ro = 4 * ((ti + g) & 1);
    if (ROT == 5) ro = ((ti + g) % 3) * 2;
    const u16* w = (g >= 8) ? wB : wA;
    const rsrc_t rsX = __builtin_amdgcn_make_buffer_rsrc((void*)x, 0, S_ * D_ * 2, 0x00020000);
    const rsrc_t rsW = __builtin_amdgcn_make_buffer_rsrc((void*)w, 0, D_ * 3 * D_ * 2, 0x00020000);
    const int rin = lane >> 3, ch = lane & 7;
    const unsigned vx = rin * 1024 + ((ch ^ (4 * (hw & 1) + (rin >> 1))) << 4);
    const unsigned csw = (ch ^ (((rin >> 1) & 1) << 2)) << 4;
    const unsigned vw = rin * 3072 + csw;
    const unsigned sx0 = (ti * 128 + 8 * hw) * 1024;
    const unsigned sw0 = 8 * hw * 3072 + gg * 128;
    auto stage = [&](int s) {
      const unsigned ks = (s + ro) & 7, slot = (s % 3) * 16384;
#pragma unroll
      for (int i = 0; i < 4; ++i) glds(vx, rsX, sx0 + ks * 128 + i * 32768, XR + slot + (hw + 4 * i) * 1024);
#pragma unroll
      for (int i = 0; i < 2; ++i) glds(vw, rsW, sw0 + ks * 196608 + i * 98304 + 1024, WR + slot + (hw + 4 * i) * 1024);
#pragma unroll
      for (int i = 0; i < 2; ++i) glds(vw, rsW, sw0 + ks * 196608 + i * 98304 + 2048, WR + slot + 8192 + (hw + 4 * i) * 1024);
    };
    auto stageq = [&](int s) {
      const unsigned ks = (s + ro) & 7;
#pragma unroll
      for (int i = 0; i < 2; ++i) glds(vw, rsW, sw0 + ks * 196608 + i * 98304, WQ + s * 8192 + (hw + 4 * i) * 1024);
    };
    stage(0); stage(1);
    ST(2);
    const unsigned long long tag = load_epoch(wsp) + 1ull;
    WAITV(8);
    BAR();
    ST(3);
#pragma unroll
    for (int s = 1; s < 8; ++s) {
      if (s + 1 < 8) { stage(s + 1); WAITV(8); }
      else { stageq(0); stageq(1); stageq(2); stageq(3); WAITV(8); }
      BAR();
    }
    ST(4);
    stageq(4); stageq(5); stageq(6); stageq(7);
    {
      const rsrc_t rsG = __builtin_amdgcn_make_buffer_rsrc((void*)gw, 0, D_ * 2, 0x00020000);
      const unsigned row = ti * 128 + 8 * g + 2 * hw;
      u32x4 x0, x1, gv;
      asm volatile("buffer_load_dwordx4 %0, %3, %4, %6 offen\n\tbuffer_load_dwordx4 %1, %3, %4, %6 offen offset:1024\n\t"
                   "buffer_load_dwordx4 %2, %3, %5, 0 offen\n\ts_waitcnt vmcnt(0)"
                   : "=&v"(x0), "=&v"(x1), "=&v"(gv) : "v"(lane * 16), "s"(rsX), "s"(rsG), "s"(row * 1024) : "memory");
      float a0 = 0.f, a1 = 0.f;
#pragma unroll
      for (int i = 0; i < 4; ++i) {
        const unsigned ug = gv[i], u0 = x0[i], u1 = x1[i];
        const bf16x2 gp = __builtin_bit_cast(bf16x2, ug);
        a0 = __builtin_amdgcn_fdot2_f32_bf16(__builtin_bit_cast(bf16x2, u0), gp, a0, false);
        a1 = __builtin_amdgcn_fdot2_f32_bf16(__builtin_bit_cast(bf16x2, u1), gp, a1, false);
      }
#pragma unroll
      for (int m = 32; m >= 1; m >>= 1) { a0 += __shfl_xor(a0, m); a1 += __shfl_xor(a1, m); }
      const float lg = (lane & 1) ? a1 : a0;
      const float gval = __builtin_amdgcn_rcpf(1.0f + __builtin_amdgcn_exp2f(-1.44269504088896340736f * lg));
      if (lane < 2) __hip_atomic_store((GAS float*)(ws + WS_GATE) + row + lane, gval, __ATOMIC_RELAXED, __HIP_MEMORY_SCOPE_AGENT);
    }
    ST(5);
    BAR();
    ST(6);
    const rsrc_t rsWS = __builtin_amdgcn_make_buffer_rsrc((void*)wsp, 0, WS_END, 0x00020000);
    const unsigned hid = g * 4 + hw;
    {
      const f32x4 p0 = *(LAS f32x4*)(L + SCR + hw * 1024 + lane * 16), p1 = *(LAS f32x4*)(L + SCR + (4 + hw) * 1024 + lane * 16);
      const f32x4 z0 = *(LAS f32x4*)(L + SCR + (8 + hw) * 1024 + (lane & 3) * 16), z1 = *(LAS f32x4*)(L + SCR + (8 + hw) * 1024 + 64 + (lane & 3) * 16);
      const unsigned po = WS_PART + (hid * 16 + ti) * PART_STRIDE;
      __builtin_amdgcn_raw_buffer_store_b128(__builtin_bit_cast(u32x4, p0 + p1), rsWS, lane * 16, po, 16);
      if (lane < 4) __builtin_amdgcn_raw_buffer_store_b128(__builtin_bit_cast(u32x4, z0 + z1), rsWS, 1024 + lane * 16, po, 16);
      WAITV(0);
      if (lane == 0) st_flag(ws, WS_FLAGA + (hid * 16 + ti) * 128, tag);
    }
    ST(7);
    const rsrc_t rsOA = __builtin_amdgcn_make_buffer_rsrc((void*)woA, 0, D_ * D_ * 2, 0x00020000);
    const rsrc_t rsOB = __builtin_amdgcn_make_buffer_rsrc((void*)woB, 0, D_ * D_ * 2, 0x00020000);
    const unsigned vo = rin * 1024 + csw;
#pragma unroll
    for (int i = 0; i < 16; ++i) glds(vo, rsOA, (hw + 4 * i) * 8192 + ct * 128, WOA + (hw + 4 * i) * 1024);
    ST(8);
    wait16(ws, WS_FLAGA + hid * 16 * 128, tag, lane);
    ST(9);
    {
      const unsigned pb = WS_PART + hid * 16 * PART_STRIDE;
      u32x4 pv[16];
#pragma unroll
      for (int p = 0; p < 16; ++p) pv[p] = __builtin_amdgcn_raw_buffer_load_b128(rsWS, lane * 16, pb + p * PART_STRIDE, 16);
      const u32x4 zv = __builtin_amdgcn_raw_buffer_load_b128(rsWS, (lane >> 2) * PART_STRIDE + 1024 + (lane & 3) * 16, pb, 16);
      f32x4 a = __builtin_bit_cast(f32x4, pv[0]);
#pragma unroll
      for (int p = 1; p < 16; ++p) a += __builtin_bit_cast(f32x4, pv[p]);
      f32x4 z = __builtin_bit_cast(f32x4, zv);
#pragma unroll
      for (int m = 4; m <= 32; m <<= 1) {
        z[0] += __shfl_xor(z[0], m); z[1] += __shfl_xor(z[1], m); z[2] += __shfl_xor(z[2], m); z[3] += __shfl_xor(z[3], m);
      }
      const int slot = lane & 31, jh = lane >> 5;
      u32x2 ka = {__builtin_bit_cast(unsigned, cvt2(a[0], a[1])), __builtin_bit_cast(unsigned, cvt2(a[2], a[3]))};
      *(LAS u32x2*)(L + AFR + hw * 1024 + ((slot >> 4) * 32 + (slot & 15)) * 16 + jh * 8) = ka;
      const int q4 = lane & 3;
      u32x2 kz = {__builtin_bit_cast(unsigned, cvt2(z[0], z[1])), __builtin_bit_cast(unsigned, cvt2(z[2], z[3]))};
      *(LAS u32x2*)(L + AFR + hw * 1024 + ((q4 & 1) * 32 + 16 + (lane >> 2)) * 16 + (q4 >> 1) * 8) = kz;
    }
    WAITL0();
    ST(10);
    BAR();
    ST(11);
#pragma unroll
    for (int i = 0; i < 16; ++i) glds(vo, rsOB, (hw + 4 * i) * 8192 + ct * 128, WOB + (hw + 4 * i) * 1024);
    WAITV(0);
    ST(12);
    BAR();
    STR(16); STFLUSH();
    return;
  }

  const int wr = wave >> 1, wc = wave & 1;
  const int r32 = lane & 31, h = lane >> 5;
  const int g1 = (lane >> 4) & 1, q = (lane & 15) >> 2, p = lane & 3;
  __builtin_amdgcn_s_setprio(1);
  const int own = 2 * wr + wc, oth = 2 * wr + 1 - wc;
  const unsigned fx = (h ^ ((r32 >> 1) & 7)) << 4;
  const unsigned xo0 = XR + (own * 32 + r32) * 128 + fx, xt0 = XR + (oth * 32 + r32) * 128 + fx;
  const unsigned sw = ((2 * g1 + (p >> 1)) ^ (((q >> 1) & 1) << 2)) << 4;
  const unsigned wn0 = (8 * h + q) * 128 + sw + (p & 1) * 8;
  const unsigned wkv0 = WR + (wn0 ^ (wc << 6));

  bf16x8 xr[32];
  f32x16 kO = {}, kT = {}, vO = {}, vT = {};
  const unsigned long long tag = load_epoch(wsp) + 1ull;
  {
    BAR();
    ST(3);
    bf16x8 xt = ldsr128(L, xt0), wk = ldstr(L, wkv0, wkv0 + 512), wv = ldstr(L, wkv0 + 8192, wkv0 + 8192 + 512);
    xr[0] = ldsr128(L, xo0);
#pragma unroll
    for (int t = 0; t < 32; ++t) {
      bf16x8 nxt, nwk, nwv;
      if (t + 1 < 32) {
        const int s1 = (t + 1) >> 2, k1 = (t + 1) & 3;
        const unsigned slot = (s1 % 3) * 16384;
        if (k1 == 0) { WAITL0(); BAR(); }
        xr[t + 1] = ldsr128(L, (xo0 ^ (k1 << 5)) + slot);
        nxt = ldsr128(L, (xt0 ^ (k1 << 5)) + slot);
        nwk = ldstr(L, wkv0 + slot + k1 * 2048, wkv0 + slot + k1 * 2048 + 512);
        nwv = ldstr(L, wkv0 + slot + 8192 + k1 * 2048, wkv0 + slot + 8192 + k1 * 2048 + 512);
      }
      __builtin_amdgcn_sched_barrier(0);
      kO = mfma(xr[t], wk, kO);
      kT = mfma(xt, wk, kT);
      vO = mfma(xr[t], wv, vO);
      vT = mfma(xt, wv, vT);
      __builtin_amdgcn_sched_barrier(0);
      if (t + 1 < 32) { xt = nxt; wk = nwk; wv = nwv; }
    }
  }
  ST(4);
  {
    f32x16 kv = {};
    float zs = 0.f;
#pragma unroll
    for (int s = 0; s < 2; ++s) {
      const bf16x8 pa = phi8(kO, s), pb = phi8(kT, s);
      kv = mfma(pa, cvt8(vO, s), kv);
      kv = mfma(pb, cvt8(vT, s), kv);
      zs = sum8(pa, zs); zs = sum8(pb, zs);
    }
    zs += __shfl_xor(zs, 32);
    f32x4 lo, hi;
#pragma unroll
    for (int j = 0; j < 4; ++j) { lo[j] = g1 ? kv[8 + j] : kv[j]; hi[j] = g1 ? kv[12 + j] : kv[4 + j]; }
    const int hh = 2 * wc + g1;
    const unsigned a = SCR + (4 * wr + hh) * 1024 + (h * 16 + (lane & 15)) * 16;
    *(LAS f32x4*)(L + a) = lo;
    *(LAS f32x4*)(L + a + 512) = hi;
    if (h == 0) *(LAS float*)(L + SCR + (8 + hh) * 1024 + wr * 64 + (lane & 15) * 4) = zs;
  }
  WAITL0();
  ST(5);
  BAR();
  ST(6);
  f32x16 qa = {}, qb = {};
  {
    const unsigned wq0 = WQ + wn0, wq1 = WQ + (wn0 ^ 64);
    bf16x8 a0 = ldstr(L, wq0, wq0 + 512), a1 = ldstr(L, wq1, wq1 + 512);
#pragma unroll
    for (int t = 0; t < 32; ++t) {
      bf16x8 n0, n1;
      if (t + 1 < 32) {
        const unsigned o = (t + 1) * 2048;
        n0 = ldstr(L, wq0 + o, wq0 + o + 512);
        n1 = ldstr(L, wq1 + o, wq1 + o + 512);
      }
      __builtin_amdgcn_sched_barrier(0);
      qa = mfma(a0, xr[t], qa);
      qb = mfma(a1, xr[t], qb);
      __builtin_amdgcn_sched_barrier(0);
      if (t + 1 < 32) { a0 = n0; a1 = n1; }
    }
  }
  ST(7);
  bf16x8 pq[4];
  pq[0] = phi8(qa, 0); pq[1] = phi8(qa, 1); pq[2] = phi8(qb, 0); pq[3] = phi8(qb, 1);
  const rsrc_t rsWS = __builtin_amdgcn_make_buffer_rsrc((void*)wsp, 0, WS_END, 0x00020000);
  WAITL0();
  ST(8);
  BAR();
  ST(9);
  {
    const unsigned st = 4 * ti + own;
#pragma unroll
    for (int hh = 0; hh < 4; ++hh) {
      const bf16x8 af = ldsr128(L, AFR + hh * 1024 + lane * 16);
      f32x16 d = {};
      d = mfma(af, pq[hh], d);
      const float inv = __builtin_amdgcn_rcpf(d[8] + 1e-6f);
      const bf16x8 o = pack8(d[0] * inv, d[1] * inv, d[2] * inv, d[3] * inv, d[4] * inv, d[5] * inv, d[6] * inv, d[7] * inv);
      __builtin_amdgcn_raw_buffer_store_b128(__builtin_bit_cast(u32x4, o), rsWS, lane * 16, WS_O + (st * 64 + 4 * g + hh) * 1024, 16);
    }
    ST(10);
    WAITV(0);
    if (lane == 0) st_flag(ws, WS_FLAGB + (st * 16 + g) * 128, tag);
  }
  ST(11);
  BAR();
  ST(12);
  {
    const int br2 = wave >> 1, sh = wave & 1;
    const unsigned st2 = 2 * rt + sh;
    wait16(ws, WS_FLAGB + st2 * 16 * 128, tag, lane);
    ST(13);
    if (b == 0 && wave == 0 && lane == 0) st_flag(ws, WS_EPOCH, tag);
    u32x4 of[32];
    const unsigned ob = WS_O + (st2 * 64 + 32 * br2) * 1024;
#pragma unroll
    for (int j = 0; j < 32; ++j) of[j] = __builtin_amdgcn_raw_buffer_load_b128(rsWS, lane * 16, ob + j * 1024, 16);
    const float gv = __hip_atomic_load((GAS float*)(ws + WS_GATE) + st2 * 32 + r32, __ATOMIC_RELAXED, __HIP_MEMORY_SCOPE_AGENT);
    const unsigned wp0 = (br2 ? WOB : WOA) + (4 * h + q) * 128 + sw + (p & 1) * 8, wp1 = wp0 ^ 64;
    f32x16 c0 = {}, c1 = {};
    bf16x8 a0 = ldstr(L, wp0, wp0 + 1024), a1 = ldstr(L, wp1, wp1 + 1024);
#pragma unroll
    for (int j = 0; j < 32; ++j) {
      bf16x8 n0, n1;
      if (j + 1 < 32) {
        const unsigned o = (j + 1) * 2048;
        n0 = ldstr(L, wp0 + o, wp0 + o + 1024);
        n1 = ldstr(L, wp1 + o, wp1 + o + 1024);
      }
      __builtin_amdgcn_sched_barrier(0);
      const bf16x8 bf = __builtin_bit_cast(bf16x8, of[j]);
      c0 = mfma(a0, bf, c0);
      c1 = mfma(a1, bf, c1);
      __builtin_amdgcn_sched_barrier(0);
      if (j + 1 < 32) { a0 = n0; a1 = n1; }
    }
    ST(14);
    const float gs = br2 ? 1.0f - gv : gv;
    f32x16 keep, send;
#pragma unroll
    for (int i = 0; i < 16; ++i) { keep[i] = (br2 ? c1[i] : c0[i]) * gs; send[i] = (br2 ? c0[i] : c1[i]) * gs; }
#pragma unroll
    for (int qq = 0; qq < 4; ++qq) {
      const f32x4 v = {send[4 * qq], send[4 * qq + 1], send[4 * qq + 2], send[4 * qq + 3]};
      *(LAS f32x4*)(L + EXC + wave * 4096 + qq * 1024 + lane * 16) = v;
    }
    WAITL0();
    BAR();
    unsigned pk[8];
#pragma unroll
    for (int qq = 0; qq < 4; ++qq) {
      const f32x4 v = *(LAS f32x4*)(L + EXC + (wave ^ 2) * 4096 + qq * 1024 + lane * 16);
      pk[2 * qq] = __builtin_bit_cast(unsigned, cvt2(keep[4 * qq] + v[0], keep[4 * qq + 1] + v[1]));
      pk[2 * qq + 1] = __builtin_bit_cast(unsigned, cvt2(keep[4 * qq + 2] + v[2], keep[4 * qq + 3] + v[3]));
    }
    const rsrc_t rsO = __builtin_amdgcn_make_buffer_rsrc((void*)out, 0, S_ * D_ * 2, 0x00020000);
    const unsigned orow = (st2 * 32 + r32) * 1024 + (64 * ct + 32 * br2) * 2 + h * 16;
#pragma unroll
    for (int qq = 0; qq < 4; qq += 2) {
      const auto s0 = __builtin_amdgcn_permlane32_swap(pk[2 * qq], pk[2 * qq + 2], false, false);
      const auto s1 = __builtin_amdgcn_permlane32_swap(pk[2 * qq + 1], pk[2 * qq + 3], false, false);
      const u32x4 v = {s0[0], s1[0], s0[1], s1[1]};
      __builtin_amdgcn_raw_buffer_store_b128(v, rsO, orow + qq * 16, 0, 0);
    }
    ST(15); STR(16); STFLUSH();
  }
}

extern "C" void kernel_launch(void* const* d_in, const int* in_sizes, int n_in, void* d_out, int out_size, void* d_ws, size_t ws_size,
                              hipStream_t stream) {
  (void)in_sizes; (void)n_in; (void)out_size; (void)ws_size;
  const u16* x = (const u16*)d_in[0];
  const u16* wA = (const u16*)d_in[1];
  const u16* woA = (const u16*)d_in[2];
  const u16* wB = (const u16*)d_in[4];
  const u16* woB = (const u16*)d_in[5];
  const u16* gw = (const u16*)d_in[6];
  (void)hipFuncSetAttribute((const void*)gated_mix_fused, hipFuncAttributeMaxDynamicSharedMemorySize, LDS_BYTES);
  gated_mix_fused<<<256, 512, LDS_BYTES, stream>>>(x, wA, wB, woA, woB, gw, (u16*)d_out, (char*)d_ws);
}
```

```cpp
#include <hip/hip_runtime.h>
#include <hip/hip_bf16.h>
#include <stdint.h>

#define LAS __attribute__((address_space(3)))
#define GAS __attribute__((address_space(1)))
#define DI __device__ __forceinline__

typedef unsigned short u16;
typedef __bf16 bf16x8 __attribute__((ext_vector_type(8)));
typedef __bf16 bf16x4 __attribute__((ext_vector_type(4)));
typedef __bf16 bf16x2 __attribute__((ext_vector_type(2)));
typedef float f32x16 __attribute__((ext_vector_type(16)));
typedef float f32x4 __attribute__((ext_vector_type(4)));
typedef float f32x2 __attribute__((ext_vector_type(2)));
typedef unsigned u32x4 __attribute__((ext_vector_type(4)));
typedef unsigned u32x2 __attribute__((ext_vector_type(2)));
typedef __amdgpu_buffer_rsrc_t rsrc_t;

#define ROT 2

constexpr int S_ = 2048, D_ = 512;
constexpr unsigned XR = 0;
constexpr unsigned WR = 49152;
constexpr unsigned WQ = 98304;
constexpr unsigned WOA = 0;
constexpr unsigned WOB = 98304;
constexpr unsigned SCR = 81920;
constexpr unsigned AFR = SCR + 12288;
constexpr unsigned EXC = SCR;
constexpr unsigned LDS_BYTES = 163840;
constexpr unsigned WS_EPOCH = 0;
constexpr unsigned WS_FLAGA = 4096;
constexpr unsigned WS_FLAGB = WS_FLAGA + 1024 * 128;
constexpr unsigned WS_GATE = WS_FLAGB + 1024 * 128;
constexpr unsigned WS_PART = WS_GATE + 8192;
constexpr unsigned PART_STRIDE = 1152;
constexpr unsigned WS_O = WS_PART + 1024 * PART_STRIDE;
constexpr unsigned WS_END = WS_O + 64 * 64 * 1024;
constexpr unsigned SPIN_CAP = 1u << 18;

#define ST(i) do {} while (0)
#define STR(i) do {} while (0)
#define STFLUSH() do {} while (0)

DI void glds(unsigned voff, rsrc_t rs, unsigned soff, unsigned ldsaddr) {
  asm volatile("s_mov_b32 m0, %0\n\ts_nop 0\n\tbuffer_load_dwordx4 %1, %2, %3 offen lds"
               :: "s"(ldsaddr), "v"(voff), "s"(rs), "s"(soff) : "memory");
}
#define WAITV(n) asm volatile("s_waitcnt vmcnt(" #n ")" ::: "memory")
#define WAITL0() asm volatile("s_waitcnt lgkmcnt(0)" ::: "memory")
#define BAR() asm volatile("s_barrier" ::: "memory")

DI f32x16 mfma(bf16x8 a, bf16x8 b, f32x16 c) { return __builtin_amdgcn_mfma_f32_32x32x16_bf16(a, b, c, 0, 0, 0); }
DI bf16x8 ldsr128(LAS char* L, unsigned a) { return *(LAS bf16x8*)(L + a); }
DI bf16x8 ldstr(LAS char* L, unsigned a0, unsigned a1) {
  bf16x4 lo = __builtin_amdgcn_ds_read_tr16_b64_v4bf16((LAS bf16x4*)(L + a0));
  bf16x4 hi = __builtin_amdgcn_ds_read_tr16_b64_v4bf16((LAS bf16x4*)(L + a1));
  return __builtin_shufflevector(lo, hi, 0, 1, 2, 3, 4, 5, 6, 7);
}
DI bf16x2 cvt2(float a, float b) { f32x2 p = {a, b}; return __builtin_convertvector(p, bf16x2); }
DI bf16x8 pack8(float a0, float a1, float a2, float a3, float a4, float a5, float a6, float a7) {
  bf16x2 p0 = cvt2(a0, a1), p1 = cvt2(a2, a3), p2 = cvt2(a4, a5), p3 = cvt2(a6, a7);
  bf16x4 lo = __builtin_shufflevector(p0, p1, 0, 1, 2, 3), hi = __builtin_shufflevector(p2, p3, 0, 1, 2, 3);
  return __builtin_shufflevector(lo, hi, 0, 1, 2, 3, 4, 5, 6, 7);
}
DI float phi(float t) {
  float e = __builtin_amdgcn_exp2f(t * 1.44269504088896340736f);
  e = __builtin_amdgcn_fmed3f(e, 0.0f, 1.0f);
  return (fmaxf(t, 0.0f) + e) + 1e-6f;
}
DI bf16x8 phi8(const f32x16& x, int s) {
  return pack8(phi(x[8 * s]), phi(x[8 * s + 1]), phi(x[8 * s + 2]), phi(x[8 * s + 3]),
               phi(x[8 * s + 4]), phi(x[8 * s + 5]), phi(x[8 * s + 6]), phi(x[8 * s + 7]));
}
DI bf16x8 cvt8(const f32x16& x, int s) {
  return pack8(x[8 * s], x[8 * s + 1], x[8 * s + 2], x[8 * s + 3], x[8 * s + 4], x[8 * s + 5], x[8 * s + 6], x[8 * s + 7]);
}
DI float sum8(bf16x8 v, float acc) {
  const bf16x2 ones = {(__bf16)1.0f, (__bf16)1.0f};
  acc = __builtin_amdgcn_fdot2_f32_bf16(__builtin_shufflevector(v, v, 0, 1), ones, acc, false);
  acc = __builtin_amdgcn_fdot2_f32_bf16(__builtin_shufflevector(v, v, 2, 3), ones, acc, false);
  acc = __builtin_amdgcn_fdot2_f32_bf16(__builtin_shufflevector(v, v, 4, 5), ones, acc, false);
  acc = __builtin_amdgcn_fdot2_f32_bf16(__builtin_shufflevector(v, v, 6, 7), ones, acc, false);
  return acc;
}
DI unsigned long long ld_flag(GAS char* ws, unsigned off) {
  return __hip_atomic_load((GAS unsigned long long*)(ws + off), __ATOMIC_RELAXED, __HIP_MEMORY_SCOPE_AGENT);
}
DI void st_flag(GAS char* ws, unsigned off, unsigned long long v) {
  __hip_atomic_store((GAS unsigned long long*)(ws + off), v, __ATOMIC_RELAXED, __HIP_MEMORY_SCOPE_AGENT);
}
DI unsigned long long load_epoch(const char* wsp) {
  unsigned long long e;
  asm volatile("s_load_dwordx2 %0, %1, 0x0\n\ts_waitcnt lgkmcnt(0)" : "=s"(e) : "s"(wsp) : "memory");
  return e;
}
DI void wait16(GAS char* ws, unsigned off0, unsigned long long tag, int lane) {
  const unsigned off = off0 + (unsigned)(lane & 15) * 128u;
  for (unsigned spins = 0; spins < SPIN_CAP; ++spins) {
    const unsigned long long v = ld_flag(ws, off);
    if (__all(v == tag)) break;
    __builtin_amdgcn_s_sleep(1);
  }
  asm volatile("" ::: "memory");
}

__global__ __launch_bounds__(512, 2) void gated_mix_fused(const u16* __restrict__ x, const u16* __restrict__ wA, const u16* __restrict__ wB,
                                                          const u16* __restrict__ woA, const u16* __restrict__ woB, const u16* __restrict__ gw,
                                                          u16* __restrict__ out, char* __restrict__ wsp
                                                          ) {
  extern __shared__ __attribute__((aligned(16))) char lds[];
  LAS char* L = (LAS char*)lds;
  const int tid = threadIdx.x;
  const int wave = __builtin_amdgcn_readfirstlane(tid >> 6), lane = tid & 63;
  const int b = blockIdx.x, xcd = b & 7, r = b >> 3;
  const int ti = (xcd & 1) * 8 + (r & 7), g = (xcd >> 1) * 4 + (r >> 3);
  const int gg = g & 7;
  const int rt = 4 * xcd + (r & 3), ct = r >> 2;
  GAS char* ws = (GAS char*)wsp;
  STR(0); ST(1);

  unsigned ctouch = 0;
  {
    unsigned long long pc;
    asm volatile("s_getpc_b64 %0" : "=s"(pc));
    pc &= ~255ull;
    if (wave < 4) {
      const unsigned off = (wave * 64 + lane) * 128;
      if (off < 24320) { const char* a = (const char*)pc + off; asm volatile("global_load_dword %0, %1, off" : "=v"(ctouch) : "v"(a) : "memory"); }
    }
  }
  if (wave >= 4) {
    const int hw = wave - 4;
    int ro = 0;
    if (ROT == 1) ro = (ti + 2 * g) & 7;
    if (ROT == 2) ro = 2 * ((ti + g) & 1);
    if (ROT == 3) ro = 4 * ((ti + g) & 1);
    if (ROT == 5) ro = ((ti + g) % 3) * 2;
    const u16* w = (g >= 8) ? wB : wA;
    const rsrc_t rsX = __builtin_amdgcn_make_buffer_rsrc((void*)x, 0, S_ * D_ * 2, 0x00020000);
    const rsrc_t rsW = __builtin_amdgcn_make_buffer_rsrc((void*)w, 0, D_ * 3 * D_ * 2, 0x00020000);
    const int rin = lane >> 3, ch = lane & 7;
    const unsigned vx = rin * 1024 + ((ch ^ (4 * (hw & 1) + (rin >> 1))) << 4);
    const unsigned csw = (ch ^ (((rin >> 1) & 1) << 2)) << 4;
    const unsigned vw = rin * 3072 + csw;
    const unsigned sx0 = (ti * 128 + 8 * hw) * 1024;
    const unsigned sw0 = 8 * hw * 3072 + gg * 128;
    auto stage = [&](int s) {
      const unsigned ks = (s + ro) & 7, slot = (s % 3) * 16384;
#pragma unroll
      for (int i = 0; i < 4; ++i) glds(vx, rsX, sx0 + ks * 128 + i * 32768, XR + slot + (hw + 4 * i) * 1024);
#pragma unroll
      for (int i = 0; i < 2; ++i) glds(vw, rsW, sw0 + ks * 196608 + i * 98304 + 1024, WR + slot + (hw + 4 * i) * 1024);
#pragma unroll
      for (int i = 0; i < 2; ++i) glds(vw, rsW, sw0 + ks * 196608 + i * 98304 + 2048, WR + slot + 8192 + (hw + 4 * i) * 1024);
    };
    auto stageq = [&](int s) {
      const unsigned ks = (s + ro) & 7;
#pragma unroll
      for (int i = 0; i < 2; ++i) glds(vw, rsW, sw0 + ks * 196608 + i * 98304, WQ + s * 8192 + (hw + 4 * i) * 1024);
    };
    stage(0); stage(1);
    ST(2);
    const unsigned long long tag = load_epoch(wsp) + 1ull;
    WAITV(8);
    BAR();
    ST(3);
#pragma unroll
    for (int s = 1; s < 8; ++s) {
      if (s + 1 < 8) { stage(s + 1); WAITV(8); }
      else { stageq(0); stageq(1); stageq(2); stageq(3); WAITV(8); }
      BAR();
    }
    ST(4);
    stageq(4); stageq(5); stageq(6); stageq(7);
    {
      const rsrc_t rsG = __builtin_amdgcn_make_buffer_rsrc((void*)gw, 0, D_ * 2, 0x00020000);
      const unsigned row = ti * 128 + 8 * g + 2 * hw;
      u32x4 x0, x1, gv;
      asm volatile("buffer_load_dwordx4 %0, %3, %4, %6 offen\n\tbuffer_load_dwordx4 %1, %3, %4, %6 offen offset:1024\n\t"
                   "buffer_load_dwordx4 %2, %3, %5, 0 offen\n\ts_waitcnt vmcnt(0)"
                   : "=&v"(x0), "=&v"(x1), "=&v"(gv) : "v"(lane * 16), "s"(rsX), "s"(rsG), "s"(row * 1024) : "memory");
      float a0 = 0.f, a1 = 0.f;
#pragma unroll
      for (int i = 0; i < 4; ++i) {
        const unsigned ug = gv[i], u0 = x0[i], u1 = x1[i];
        const bf16x2 gp = __builtin_bit_cast(bf16x2, ug);
        a0 = __builtin_amdgcn_fdot2_f32_bf16(__builtin_bit_cast(bf16x2, u0), gp, a0, false);
        a1 = __builtin_amdgcn_fdot2_f32_bf16(__builtin_bit_cast(bf16x2, u1), gp, a1, false);
      }
#pragma unroll
      for (int m = 32; m >= 1; m >>= 1) { a0 += __shfl_xor(a0, m); a1 += __shfl_xor(a1, m); }
      const float lg = (lane & 1) ? a1 : a0;
      const float gval = __builtin_amdgcn_rcpf(1.0f + __builtin_amdgcn_exp2f(-1.44269504088896340736f * lg));
      if (lane < 2) __hip_atomic_store((GAS float*)(ws + WS_GATE) + row + lane, gval, __ATOMIC_RELAXED, __HIP_MEMORY_SCOPE_AGENT);
    }
    ST(5);
    BAR();
    ST(6);
    const rsrc_t rsWS = __builtin_amdgcn_make_buffer_rsrc((void*)wsp, 0, WS_END, 0x00020000);
    const unsigned hid = g * 4 + hw;
    {
      const f32x4 p0 = *(LAS f32x4*)(L + SCR + hw * 1024 + lane * 16), p1 = *(LAS f32x4*)(L + SCR + (4 + hw) * 1024 + lane * 16);
      const f32x4 z0 = *(LAS f32x4*)(L + SCR + (8 + hw) * 1024 + (lane & 3) * 16), z1 = *(LAS f32x4*)(L + SCR + (8 + hw) * 1024 + 64 + (lane & 3) * 16);
      const unsigned po = WS_PART + (hid * 16 + ti) * PART_STRIDE;
      __builtin_amdgcn_raw_buffer_store_b128(__builtin_bit_cast(u32x4, p0 + p1), rsWS, lane * 16, po, 16);
      if (lane < 4) __builtin_amdgcn_raw_buffer_store_b128(__builtin_bit_cast(u32x4, z0 + z1), rsWS, 1024 + lane * 16, po, 16);
      WAITV(0);
      if (lane == 0) st_flag(ws, WS_FLAGA + (hid * 16 + ti) * 128, tag);
    }
    ST(7);
    const rsrc_t rsOA = __builtin_amdgcn_make_buffer_rsrc((void*)woA, 0, D_ * D_ * 2, 0x00020000);
    const rsrc_t rsOB = __builtin_amdgcn_make_buffer_rsrc((void*)woB, 0, D_ * D_ * 2, 0x00020000);
    const unsigned vo = rin * 1024 + csw;
#pragma unroll
    for (int i = 0; i < 16; ++i) glds(vo, rsOA, (hw + 4 * i) * 8192 + ct * 128, WOA + (hw + 4 * i) * 1024);
    ST(8);
    wait16(ws, WS_FLAGA + hid * 16 * 128, tag, lane);
    ST(9);
    {
      const unsigned pb = WS_PART + hid * 16 * PART_STRIDE;
      u32x4 pv[16];
#pragma unroll
      for (int p = 0; p < 16; ++p) pv[p] = __builtin_amdgcn_raw_buffer_load_b128(rsWS, lane * 16, pb + p * PART_STRIDE, 16);
      const u32x4 zv = __builtin_amdgcn_raw_buffer_load_b128(rsWS, (lane >> 2) * PART_STRIDE + 1024 + (lane & 3) * 16, pb, 16);
      f32x4 a = __builtin_bit_cast(f32x4, pv[0]);
#pragma unroll
      for (int p = 1; p < 16; ++p) a += __builtin_bit_cast(f32x4, pv[p]);
      f32x4 z = __builtin_bit_cast(f32x4, zv);
#pragma unroll
      for (int m = 4; m <= 32; m <<= 1) {
        z[0] += __shfl_xor(z[0], m); z[1] += __shfl_xor(z[1], m); z[2] += __shfl_xor(z[2], m); z[3] += __shfl_xor(z[3], m);
      }
      const int slot = lane & 31, jh = lane >> 5;
      u32x2 ka = {__builtin_bit_cast(unsigned, cvt2(a[0], a[1])), __builtin_bit_cast(unsigned, cvt2(a[2], a[3]))};
      *(LAS u32x2*)(L + AFR + hw * 1024 + ((slot >> 4) * 32 + (slot & 15)) * 16 + jh * 8) = ka;
      const int q4 = lane & 3;
      u32x2 kz = {__builtin_bit_cast(unsigned, cvt2(z[0], z[1])), __builtin_bit_cast(unsigned, cvt2(z[2], z[3]))};
      *(LAS u32x2*)(L + AFR + hw * 1024 + ((q4 & 1) * 32 + 16 + (lane >> 2)) * 16 + (q4 >> 1) * 8) = kz;
    }
    WAITL0();
    ST(10);
    BAR();
    ST(11);
#pragma unroll
    for (int i = 0; i < 16; ++i) glds(vo, rsOB, (hw + 4 * i) * 8192 + ct * 128, WOB + (hw + 4 * i) * 1024);
    WAITV(0);
    ST(12);
    BAR();
    STR(16); STFLUSH();
    return;
  }

  const int wr = wave >> 1, wc = wave & 1;
  const int r32 = lane & 31, h = lane >> 5;
  const int g1 = (lane >> 4) & 1, q = (lane & 15) >> 2, p = lane & 3;
  __builtin_amdgcn_s_setprio(1);
  const int own = 2 * wr + wc, oth = 2 * wr + 1 - wc;
  const unsigned fx = (h ^ ((r32 >> 1) & 7)) << 4;
  const unsigned xo0 = XR + (own * 32 + r32) * 128 + fx, xt0 = XR + (oth * 32 + r32) * 128 + fx;
  const unsigned sw = ((2 * g1 + (p >> 1)) ^ (((q >> 1) & 1) << 2)) << 4;
  const unsigned wn0 = (8 * h + q) * 128 + sw + (p & 1) * 8;
  const unsigned wkv0 = WR + (wn0 ^ (wc << 6));

  bf16x8 xr[32];
  f32x16 kO = {}, kT = {}, vO = {}, vT = {};
  const unsigned long long tag = load_epoch(wsp) + 1ull;
  {
    BAR();
    ST(3);
    bf16x8 xt = ldsr128(L, xt0), wk = ldstr(L, wkv0, wkv0 + 512), wv = ldstr(L, wkv0 + 8192, wkv0 + 8192 + 512);
    xr[0] = ldsr128(L, xo0);
#pragma unroll
    for (int t = 0; t < 32; ++t) {
      bf16x8 nxt, nwk, nwv;
      if (t + 1 < 32) {
        const int s1 = (t + 1) >> 2, k1 = (t + 1) & 3;
        const unsigned slot = (s1 % 3) * 16384;
        if (k1 == 0) { WAITL0(); BAR(); }
        xr[t + 1] = ldsr128(L, (xo0 ^ (k1 << 5)) + slot);
        nxt = ldsr128(L, (xt0 ^ (k1 << 5)) + slot);
        nwk = ldstr(L, wkv0 + slot + k1 * 2048, wkv0 + slot + k1 * 2048 + 512);
        nwv = ldstr(L, wkv0 + slot + 8192 + k1 * 2048, wkv0 + slot + 8192 + k1 * 2048 + 512);
      }
      __builtin_amdgcn_sched_barrier(0);
      kO = mfma(xr[t], wk, kO);
      kT = mfma(xt, wk, kT);
      vO = mfma(xr[t], wv, vO);
      vT = mfma(xt, wv, vT);
      __builtin_amdgcn_sched_barrier(0);
      if (t + 1 < 32) { xt = nxt; wk = nwk; wv = nwv; }
    }
  }
  ST(4);
  {
    f32x16 kv = {};
    float zs = 0.f;
#pragma unroll
    for (int s = 0; s < 2; ++s) {
      const bf16x8 pa = phi8(kO, s), pb = phi8(kT, s);
      kv = mfma(pa, cvt8(vO, s), kv);
      kv = mfma(pb, cvt8(vT, s), kv);
      zs = sum8(pa, zs); zs = sum8(pb, zs);
    }
    zs += __shfl_xor(zs, 32);
    f32x4 lo, hi;
#pragma unroll
    for (int j = 0; j < 4; ++j) { lo[j] = g1 ? kv[8 + j] : kv[j]; hi[j] = g1 ? kv[12 + j] : kv[4 + j]; }
    const int hh = 2 * wc + g1;
    const unsigned a = SCR + (4 * wr + hh) * 1024 + (h * 16 + (lane & 15)) * 16;
    *(LAS f32x4*)(L + a) = lo;
    *(LAS f32x4*)(L + a + 512) = hi;
    if (h == 0) *(LAS float*)(L + SCR + (8 + hh) * 1024 + wr * 64 + (lane & 15) * 4) = zs;
  }
  WAITL0();
  ST(5);
  BAR();
  ST(6);
  f32x16 qa = {}, qb = {};
  {
    const unsigned wq0 = WQ + wn0, wq1 = WQ + (wn0 ^ 64);
    bf16x8 a0 = ldstr(L, wq0, wq0 + 512), a1 = ldstr(L, wq1, wq1 + 512);
#pragma unroll
    for (int t = 0; t < 32; ++t) {
      bf16x8 n0, n1;
      if (t + 1 < 32) {
        const unsigned o = (t + 1) * 2048;
        n0 = ldstr(L, wq0 + o, wq0 + o + 512);
        n1 = ldstr(L, wq1 + o, wq1 + o + 512);
      }
      __builtin_amdgcn_sched_barrier(0);
      qa = mfma(a0, xr[t], qa);
      qb = mfma(a1, xr[t], qb);
      __builtin_amdgcn_sched_barrier(0);
      if (t + 1 < 32) { a0 = n0; a1 = n1; }
    }
  }
  ST(7);
  bf16x8 pq[4];
  pq[0] = phi8(qa, 0); pq[1] = phi8(qa, 1); pq[2] = phi8(qb, 0); pq[3] = phi8(qb, 1);
  const rsrc_t rsWS = __builtin_amdgcn_make_buffer_rsrc((void*)wsp, 0, WS_END, 0x00020000);
  WAITL0();
  ST(8);
  BAR();
  ST(9);
  {
    const unsigned st = 4 * ti + own;
#pragma unroll
    for (int hh = 0; hh < 4; ++hh) {
      const bf16x8 af = ldsr128(L, AFR + hh * 1024 + lane * 16);
      f32x16 d = {};
      d = mfma(af, pq[hh], d);
      const float inv = __builtin_amdgcn_rcpf(d[8] + 1e-6f);
      const bf16x8 o = pack8(d[0] * inv, d[1] * inv, d[2] * inv, d[3] * inv, d[4] * inv, d[5] * inv, d[6] * inv, d[7] * inv);
      __builtin_amdgcn_raw_buffer_store_b128(__builtin_bit_cast(u32x4, o), rsWS, lane * 16, WS_O + (st * 64 + 4 * g + hh) * 1024, 16);
    }
    ST(10);
    WAITV(0);
    asm volatile("" :: "v"(ctouch));
    if (lane == 0) st_flag(ws, WS_FLAGB + (st * 16 + g) * 128, tag);
  }
  ST(11);
  BAR();
  ST(12);
  {
    const int br2 = wave >> 1, sh = wave & 1;
    const unsigned st2 = 2 * rt + sh;
    wait16(ws, WS_FLAGB + st2 * 16 * 128, tag, lane);
    ST(13);
    if (b == 0 && wave == 0 && lane == 0) st_flag(ws, WS_EPOCH, tag);
    u32x4 of[32];
    const unsigned ob = WS_O + (st2 * 64 + 32 * br2) * 1024;
#pragma unroll
    for (int j = 0; j < 32; ++j) of[j] = __builtin_amdgcn_raw_buffer_load_b128(rsWS, lane * 16, ob + j * 1024, 16);
    const float gv = __hip_atomic_load((GAS float*)(ws + WS_GATE) + st2 * 32 + r32, __ATOMIC_RELAXED, __HIP_MEMORY_SCOPE_AGENT);
    const unsigned wp0 = (br2 ? WOB : WOA) + (4 * h + q) * 128 + sw + (p & 1) * 8, wp1 = wp0 ^ 64;
    f32x16 c0 = {}, c1 = {};
    bf16x8 a0 = ldstr(L, wp0, wp0 + 1024), a1 = ldstr(L, wp1, wp1 + 1024);
#pragma unroll
    for (int j = 0; j < 32; ++j) {
      bf16x8 n0, n1;
      if (j + 1 < 32) {
        const unsigned o = (j + 1) * 2048;
        n0 = ldstr(L, wp0 + o, wp0 + o + 1024);
        n1 = ldstr(L, wp1 + o, wp1 + o + 1024);
      }
      __builtin_amdgcn_sched_barrier(0);
      const bf16x8 bf = __builtin_bit_cast(bf16x8, of[j]);
      c0 = mfma(a0, bf, c0);
      c1 = mfma(a1, bf, c1);
      __builtin_amdgcn_sched_barrier(0);
      if (j + 1 < 32) { a0 = n0; a1 = n1; }
    }
    ST(14);
    const float gs = br2 ? 1.0f - gv : gv;
    f32x16 keep, send;
#pragma unroll
    for (int i = 0; i < 16; ++i) { keep[i] = (br2 ? c1[i] : c0[i]) * gs; send[i] = (br2 ? c0[i] : c1[i]) * gs; }
#pragma unroll
    for (int qq = 0; qq < 4; ++qq) {
      const f32x4 v = {send[4 * qq], send[4 * qq + 1], send[4 * qq + 2], send[4 * qq + 3]};
      *(LAS f32x4*)(L + EXC + wave * 4096 + qq * 1024 + lane * 16) = v;
    }
    WAITL0();
    BAR();
    unsigned pk[8];
#pragma unroll
    for (int qq = 0; qq < 4; ++qq) {
      const f32x4 v = *(LAS f32x4*)(L + EXC + (wave ^ 2) * 4096 + qq * 1024 + lane * 16);
      pk[2 * qq] = __builtin_bit_cast(unsigned, cvt2(keep[4 * qq] + v[0], keep[4 * qq + 1] + v[1]));
      pk[2 * qq + 1] = __builtin_bit_cast(unsigned, cvt2(keep[4 * qq + 2] + v[2], keep[4 * qq + 3] + v[3]));
    }
    const rsrc_t rsO = __builtin_amdgcn_make_buffer_rsrc((void*)out, 0, S_ * D_ * 2, 0x00020000);
    const unsigned orow = (st2 * 32 + r32) * 1024 + (64 * ct + 32 * br2) * 2 + h * 16;
#pragma unroll
    for (int qq = 0; qq < 4; qq += 2) {
      const auto s0 = __builtin_amdgcn_permlane32_swap(pk[2 * qq], pk[2 * qq + 2], false, false);
      const auto s1 = __builtin_amdgcn_permlane32_swap(pk[2 * qq + 1], pk[2 * qq + 3], false, false);
      const u32x4 v = {s0[0], s1[0], s0[1], s1[1]};
      __builtin_amdgcn_raw_buffer_store_b128(v, rsO, orow + qq * 16, 0, 0);
    }
    ST(15); STR(16); STFLUSH();
  }
}

extern "C" void kernel_launch(void* const* d_in, const int* in_sizes, int n_in, void* d_out, int out_size, void* d_ws, size_t ws_size,
                              hipStream_t stream) {
  (void)in_sizes; (void)n_in; (void)out_size; (void)ws_size;
  const u16* x = (const u16*)d_in[0];
  const u16* wA = (const u16*)d_in[1];
  const u16* woA = (const u16*)d_in[2];
  const u16* wB = (const u16*)d_in[4];
  const u16* woB = (const u16*)d_in[5];
  const u16* gw = (const u16*)d_in[6];
  (void)hipFuncSetAttribute((const void*)gated_mix_fused, hipFuncAttributeMaxDynamicSharedMemorySize, LDS_BYTES);
  gated_mix_fused<<<256, 512, LDS_BYTES, stream>>>(x, wA, wB, woA, woB, gw, (u16*)d_out, (char*)d_ws);
}
```

```cpp
#define PW 0
#define PA 1
#define PB 1
#define PM 0
#include <hip/hip_runtime.h>
#include <hip/hip_bf16.h>
#include <stdint.h>

#define LAS __attribute__((address_space(3)))
#define GAS __attribute__((address_space(1)))
#define DI __device__ __forceinline__

typedef unsigned short u16;
typedef __bf16 bf16x8 __attribute__((ext_vector_type(8)));
typedef __bf16 bf16x4 __attribute__((ext_vector_type(4)));
typedef __bf16 bf16x2 __attribute__((ext_vector_type(2)));
typedef float f32x16 __attribute__((ext_vector_type(16)));
typedef float f32x4 __attribute__((ext_vector_type(4)));
typedef float f32x2 __attribute__((ext_vector_type(2)));
typedef unsigned u32x4 __attribute__((ext_vector_type(4)));
typedef unsigned u32x2 __attribute__((ext_vector_type(2)));
typedef __amdgpu_buffer_rsrc_t rsrc_t;

#define ROT 2

constexpr int S_ = 2048, D_ = 512;
constexpr unsigned XR = 0;
constexpr unsigned WR = 49152;
constexpr unsigned WQ = 98304;
constexpr unsigned WOA = 0;
constexpr unsigned WOB = 98304;
constexpr unsigned SCR = 81920;
constexpr unsigned AFR = SCR + 12288;
constexpr unsigned EXC = SCR;
constexpr unsigned LDS_BYTES = 163840;
constexpr unsigned WS_EPOCH = 0;
constexpr unsigned WS_FLAGA = 4096;
constexpr unsigned WS_FLAGB = WS_FLAGA + 1024 * 128;
constexpr unsigned WS_GATE = WS_FLAGB + 1024 * 128;
constexpr unsigned WS_PART = WS_GATE + 8192;
constexpr unsigned PART_STRIDE = 1152;
constexpr unsigned WS_O = WS_PART + 1024 * PART_STRIDE;
constexpr unsigned WS_END = WS_O + 64 * 64 * 1024;
constexpr unsigned SPIN_CAP = 1u << 18;

#define ST(i) do { if ((i) == PA || (i) == PB) stv[(i) == PA ? 0 : 1] = (unsigned)__builtin_amdgcn_s_memrealtime(); } while (0)
#define STR(i) do {} while (0)
#define STFLUSH() do { if (PW == (wave >= 4 ? 1 : 0)) { WAITV(0); if (PB == 99) stv[1] = (unsigned)__builtin_amdgcn_s_memrealtime(); \
   unsigned X = stv[1] - stv[0]; if (X > 4000) X = 4000; const unsigned long long until = __builtin_amdgcn_s_memrealtime() + (unsigned long long)X * PM; \
   while (__builtin_amdgcn_s_memrealtime() < until) __builtin_amdgcn_s_sleep(1); } } while (0)

DI void glds(unsigned voff, rsrc_t rs, unsigned soff, unsigned ldsaddr) {
  asm volatile("s_mov_b32 m0, %0\n\ts_nop 0\n\tbuffer_load_dwordx4 %1, %2, %3 offen lds"
               :: "s"(ldsaddr), "v"(voff), "s"(rs), "s"(soff) : "memory");
}
#define WAITV(n) asm volatile("s_waitcnt vmcnt(" #n ")" ::: "memory")
#define WAITL0() asm volatile("s_waitcnt lgkmcnt(0)" ::: "memory")
#define BAR() asm volatile("s_barrier" ::: "memory")

DI f32x16 mfma(bf16x8 a, bf16x8 b, f32x16 c) { return __builtin_amdgcn_mfma_f32_32x32x16_bf16(a, b, c, 0, 0, 0); }
DI bf16x8 ldsr128(LAS char* L, unsigned a) { return *(LAS bf16x8*)(L + a); }
DI bf16x8 ldstr(LAS char* L, unsigned a0, unsigned a1) {
  bf16x4 lo = __builtin_amdgcn_ds_read_tr16_b64_v4bf16((LAS bf16x4*)(L + a0));
  bf16x4 hi = __builtin_amdgcn_ds_read_tr16_b64_v4bf16((LAS bf16x4*)(L + a1));
  return __builtin_shufflevector(lo, hi, 0, 1, 2, 3, 4, 5, 6, 7);
}
DI bf16x2 cvt2(float a, float b) { f32x2 p = {a, b}; return __builtin_convertvector(p, bf16x2); }
DI bf16x8 pack8(float a0, float a1, float a2, float a3, float a4, float a5, float a6, float a7) {
  bf16x2 p0 = cvt2(a0, a1), p1 = cvt2(a2, a3), p2 = cvt2(a4, a5), p3 = cvt2(a6, a7);
  bf16x4 lo = __builtin_shufflevector(p0, p1, 0, 1, 2, 3), hi = __builtin_shufflevector(p2, p3, 0, 1, 2, 3);
  return __builtin_shufflevector(lo, hi, 0, 1, 2, 3, 4, 5, 6, 7);
}
DI float phi(float t) {
  float e = __builtin_amdgcn_exp2f(t * 1.44269504088896340736f);
  e = __builtin_amdgcn_fmed3f(e, 0.0f, 1.0f);
  return (fmaxf(t, 0.0f) + e) + 1e-6f;
}
DI bf16x8 phi8(const f32x16& x, int s) {
  return pack8(phi(x[8 * s]), phi(x[8 * s + 1]), phi(x[8 * s + 2]), phi(x[8 * s + 3]),
               phi(x[8 * s + 4]), phi(x[8 * s + 5]), phi(x[8 * s + 6]), phi(x[8 * s + 7]));
}
DI bf16x8 cvt8(const f32x16& x, int s) {
  return pack8(x[8 * s], x[8 * s + 1], x[8 * s + 2], x[8 * s + 3], x[8 * s + 4], x[8 * s + 5], x[8 * s + 6], x[8 * s + 7]);
}
DI float sum8(bf16x8 v, float acc) {
  const bf16x2 ones = {(__bf16)1.0f, (__bf16)1.0f};
  acc = __builtin_amdgcn_fdot2_f32_bf16(__builtin_shufflevector(v, v, 0, 1), ones, acc, false);
  acc = __builtin_amdgcn_fdot2_f32_bf16(__builtin_shufflevector(v, v, 2, 3), ones, acc, false);
  acc = __builtin_amdgcn_fdot2_f32_bf16(__builtin_shufflevector(v, v, 4, 5), ones, acc, false);
  acc = __builtin_amdgcn_fdot2_f32_bf16(__builtin_shufflevector(v, v, 6, 7), ones, acc, false);
  return acc;
}
DI unsigned long long ld_flag(GAS char* ws, unsigned off) {
  return __hip_atomic_load((GAS unsigned long long*)(ws + off), __ATOMIC_RELAXED, __HIP_MEMORY_SCOPE_AGENT);
}
DI void st_flag(GAS char* ws, unsigned off, unsigned long long v) {
  __hip_atomic_store((GAS unsigned long long*)(ws + off), v, __ATOMIC_RELAXED, __HIP_MEMORY_SCOPE_AGENT);
}
DI unsigned long long load_epoch(const char* wsp) {
  unsigned long long e;
  asm volatile("s_load_dwordx2 %0, %1, 0x0\n\ts_waitcnt lgkmcnt(0)" : "=s"(e) : "s"(wsp) : "memory");
  return e;
}
DI void wait16(GAS char* ws, unsigned off0, unsigned long long tag, int lane) {
  const unsigned off = off0 + (unsigned)(lane & 15) * 128u;
  for (unsigned spins = 0; spins < SPIN_CAP; ++spins) {
    const unsigned long long v = ld_flag(ws, off);
    if (__all(v == tag)) break;
    __builtin_amdgcn_s_sleep(1);
  }
  asm volatile("" ::: "memory");
}

__global__ __launch_bounds__(512, 2) void gated_mix_fused(const u16* __restrict__ x, const u16* __restrict__ wA, const u16* __restrict__ wB,
                                                          const u16* __restrict__ woA, const u16* __restrict__ woB, const u16* __restrict__ gw,
                                                          u16* __restrict__ out, char* __restrict__ wsp
                                                          ) {
  extern __shared__ __attribute__((aligned(16))) char lds[];
  LAS char* L = (LAS char*)lds;
  const int tid = threadIdx.x;
  const int wave = __builtin_amdgcn_readfirstlane(tid >> 6), lane = tid & 63;
  const int b = blockIdx.x, xcd = b & 7, r = b >> 3;
  const int ti = (xcd & 1) * 8 + (r & 7), g = (xcd >> 1) * 4 + (r >> 3);
  const int gg = g & 7;
  const int rt = 4 * xcd + (r & 3), ct = r >> 2;
  GAS char* ws = (GAS char*)wsp;
  unsigned stv[2] = {};
  STR(0); ST(1);

  unsigned ctouch = 0;
  {
    unsigned long long pc;
    asm volatile("s_getpc_b64 %0" : "=s"(pc));
    pc &= ~255ull;
    if (wave < 4) {
      const unsigned off = (wave * 64 + lane) * 128;
      if (off < 24320) { const char* a = (const char*)pc + off; asm volatile("global_load_dword %0, %1, off" : "=v"(ctouch) : "v"(a) : "memory"); }
    }
  }
  if (wave >= 4) {
    const int hw = wave - 4;
    int ro = 0;
    if (ROT == 1) ro = (ti + 2 * g) & 7;
    if (ROT == 2) ro = 2 * ((ti + g) & 1);
    if (ROT == 3) ro = 4 * ((ti + g) & 1);
    if (ROT == 5) ro = ((ti + g) % 3) * 2;
    const u16* w = (g >= 8) ? wB : wA;
    const rsrc_t rsX = __builtin_amdgcn_make_buffer_rsrc((void*)x, 0, S_ * D_ * 2, 0x00020000);
    const rsrc_t rsW = __builtin_amdgcn_make_buffer_rsrc((void*)w, 0, D_ * 3 * D_ * 2, 0x00020000);
    const int rin = lane >> 3, ch = lane & 7;
    const unsigned vx = rin * 1024 + ((ch ^ (4 * (hw & 1) + (rin >> 1))) << 4);
    const unsigned csw = (ch ^ (((rin >> 1) & 1) << 2)) << 4;
    const unsigned vw = rin * 3072 + csw;
    const unsigned sx0 = (ti * 128 + 8 * hw) * 1024;
    const unsigned sw0 = 8 * hw * 3072 + gg * 128;
    auto stage = [&](int s) {
      const unsigned ks = (s + ro) & 7, slot = (s % 3) * 16384;
#pragma unroll
      for (int i = 0; i < 4; ++i) glds(vx, rsX, sx0 + ks * 128 + i * 32768, XR + slot + (hw + 4 * i) * 1024);
#pragma unroll
      for (int i = 0; i < 2; ++i) glds(vw, rsW, sw0 + ks * 196608 + i * 98304 + 1024, WR + slot + (hw + 4 * i) * 1024);
#pragma unroll
      for (int i = 0; i < 2; ++i) glds(vw, rsW, sw0 + ks * 196608 + i * 98304 + 2048, WR + slot + 8192 + (hw + 4 * i) * 1024);
    };
    auto stageq = [&](int s) {
      const unsigned ks = (s + ro) & 7;
#pragma unroll
      for (int i = 0; i < 2; ++i) glds(vw, rsW, sw0 + ks * 196608 + i * 98304, WQ + s * 8192 + (hw + 4 * i) * 1024);
    };
    stage(0); stage(1);
    ST(2);
    const unsigned long long tag = load_epoch(wsp) + 1ull;
    WAITV(8);
    BAR();
    ST(3);
#pragma unroll
    for (int s = 1; s < 8; ++s) {
      if (s + 1 < 8) { stage(s + 1); WAITV(8); }
      else { stageq(0); stageq(1); stageq(2); stageq(3); WAITV(8); }
      BAR();
    }
    ST(4);
    stageq(4); stageq(5); stageq(6); stageq(7);
    {
      const rsrc_t rsG = __builtin_amdgcn_make_buffer_rsrc((void*)gw, 0, D_ * 2, 0x00020000);
      const unsigned row = ti * 128 + 8 * g + 2 * hw;
      u32x4 x0, x1, gv;
      asm volatile("buffer_load_dwordx4 %0, %3, %4, %6 offen\n\tbuffer_load_dwordx4 %1, %3, %4, %6 offen offset:1024\n\t"
                   "buffer_load_dwordx4 %2, %3, %5, 0 offen\n\ts_waitcnt vmcnt(0)"
                   : "=&v"(x0), "=&v"(x1), "=&v"(gv) : "v"(lane * 16), "s"(rsX), "s"(rsG), "s"(row * 1024) : "memory");
      float a0 = 0.f, a1 = 0.f;
#pragma unroll
      for (int i = 0; i < 4; ++i) {
        const unsigned ug = gv[i], u0 = x0[i], u1 = x1[i];
        const bf16x2 gp = __builtin_bit_cast(bf16x2, ug);
        a0 = __builtin_amdgcn_fdot2_f32_bf16(__builtin_bit_cast(bf16x2, u0), gp, a0, false);
        a1 = __builtin_amdgcn_fdot2_f32_bf16(__builtin_bit_cast(bf16x2, u1), gp, a1, false);
      }
#pragma unroll
      for (int m = 32; m >= 1; m >>= 1) { a0 += __shfl_xor(a0, m); a1 += __shfl_xor(a1, m); }
      const float lg = (lane & 1) ? a1 : a0;
      const float gval = __builtin_amdgcn_rcpf(1.0f + __builtin_amdgcn_exp2f(-1.44269504088896340736f * lg));
      if (lane < 2) __hip_atomic_store((GAS float*)(ws + WS_GATE) + row + lane, gval, __ATOMIC_RELAXED, __HIP_MEMORY_SCOPE_AGENT);
    }
    ST(5);
    BAR();
    ST(6);
    const rsrc_t rsWS = __builtin_amdgcn_make_buffer_rsrc((void*)wsp, 0, WS_END, 0x00020000);
    const unsigned hid = g * 4 + hw;
    {
      const f32x4 p0 = *(LAS f32x4*)(L + SCR + hw * 1024 + lane * 16), p1 = *(LAS f32x4*)(L + SCR + (4 + hw) * 1024 + lane * 16);
      const f32x4 z0 = *(LAS f32x4*)(L + SCR + (8 + hw) * 1024 + (lane & 3) * 16), z1 = *(LAS f32x4*)(L + SCR + (8 + hw) * 1024 + 64 + (lane & 3) * 16);
      const unsigned po = WS_PART + (hid * 16 + ti) * PART_STRIDE;
      __builtin_amdgcn_raw_buffer_store_b128(__builtin_bit_cast(u32x4, p0 + p1), rsWS, lane * 16, po, 16);
      if (lane < 4) __builtin_amdgcn_raw_buffer_store_b128(__builtin_bit_cast(u32x4, z0 + z1), rsWS, 1024 + lane * 16, po, 16);
      WAITV(0);
      if (lane == 0) st_flag(ws, WS_FLAGA + (hid * 16 + ti) * 128, tag);
    }
    ST(7);
    const rsrc_t rsOA = __builtin_amdgcn_make_buffer_rsrc((void*)woA, 0, D_ * D_ * 2, 0x00020000);
    const rsrc_t rsOB = __builtin_amdgcn_make_buffer_rsrc((void*)woB, 0, D_ * D_ * 2, 0x00020000);
    const unsigned vo = rin * 1024 + csw;
#pragma unroll
    for (int i = 0; i < 16; ++i) glds(vo, rsOA, (hw + 4 * i) * 8192 + ct * 128, WOA + (hw + 4 * i) * 1024);
    ST(8);
    wait16(ws, WS_FLAGA + hid * 16 * 128, tag, lane);
    ST(9);
    {
      const unsigned pb = WS_PART + hid * 16 * PART_STRIDE;
      u32x4 pv[16];
#pragma unroll
      for (int p = 0; p < 16; ++p) pv[p] = __builtin_amdgcn_raw_buffer_load_b128(rsWS, lane * 16, pb + p * PART_STRIDE, 16);
      const u32x4 zv = __builtin_amdgcn_raw_buffer_load_b128(rsWS, (lane >> 2) * PART_STRIDE + 1024 + (lane & 3) * 16, pb, 16);
      f32x4 a = __builtin_bit_cast(f32x4, pv[0]);
#pragma unroll
      for (int p = 1; p < 16; ++p) a += __builtin_bit_cast(f32x4, pv[p]);
      f32x4 z = __builtin_bit_cast(f32x4, zv);
#pragma unroll
      for (int m = 4; m <= 32; m <<= 1) {
        z[0] += __shfl_xor(z[0], m); z[1] += __shfl_xor(z[1], m); z[2] += __shfl_xor(z[2], m); z[3] += __shfl_xor(z[3], m);
      }
      const int slot = lane & 31, jh = lane >> 5;
      u32x2 ka = {__builtin_bit_cast(unsigned, cvt2(a[0], a[1])), __builtin_bit_cast(unsigned, cvt2(a[2], a[3]))};
      *(LAS u32x2*)(L + AFR + hw * 1024 + ((slot >> 4) * 32 + (slot & 15)) * 16 + jh * 8) = ka;
      const int q4 = lane & 3;
      u32x2 kz = {__builtin_bit_cast(unsigned, cvt2(z[0], z[1])), __builtin_bit_cast(unsigned, cvt2(z[2], z[3]))};
      *(LAS u32x2*)(L + AFR + hw * 1024 + ((q4 & 1) * 32 + 16 + (lane >> 2)) * 16 + (q4 >> 1) * 8) = kz;
    }
    WAITL0();
    ST(10);
    BAR();
    ST(11);
#pragma unroll
    for (int i = 0; i < 16; ++i) glds(vo, rsOB, (hw + 4 * i) * 8192 + ct * 128, WOB + (hw + 4 * i) * 1024);
    WAITV(0);
    ST(12);
    BAR();
    STR(16); STFLUSH();
    return;
  }

  const int wr = wave >> 1, wc = wave & 1;
  const int r32 = lane & 31, h = lane >> 5;
  const int g1 = (lane >> 4) & 1, q = (lane & 15) >> 2, p = lane & 3;
  __builtin_amdgcn_s_setprio(1);
  const int own = 2 * wr + wc, oth = 2 * wr + 1 - wc;
  const unsigned fx = (h ^ ((r32 >> 1) & 7)) << 4;
  const unsigned xo0 = XR + (own * 32 + r32) * 128 + fx, xt0 = XR + (oth * 32 + r32) * 128 + fx;
  const unsigned sw = ((2 * g1 + (p >> 1)) ^ (((q >> 1) & 1) << 2)) << 4;
  const unsigned wn0 = (8 * h + q) * 128 + sw + (p & 1) * 8;
  const unsigned wkv0 = WR + (wn0 ^ (wc << 6));

  bf16x8 xr[32];
  f32x16 kO = {}, kT = {}, vO = {}, vT = {};
  const unsigned long long tag = load_epoch(wsp) + 1ull;
  {
    BAR();
    ST(3);
    bf16x8 xt = ldsr128(L, xt0), wk = ldstr(L, wkv0, wkv0 + 512), wv = ldstr(L, wkv0 + 8192, wkv0 + 8192 + 512);
    xr[0] = ldsr128(L, xo0);
#pragma unroll
    for (int t = 0; t < 32; ++t) {
      bf16x8 nxt, nwk, nwv;
      if (t + 1 < 32) {
        const int s1 = (t + 1) >> 2, k1 = (t + 1) & 3;
        const unsigned slot = (s1 % 3) * 16384;
        if (k1 == 0) { WAITL0(); BAR(); }
        xr[t + 1] = ldsr128(L, (xo0 ^ (k1 << 5)) + slot);
        nxt = ldsr128(L, (xt0 ^ (k1 << 5)) + slot);
        nwk = ldstr(L, wkv0 + slot + k1 * 2048, wkv0 + slot + k1 * 2048 + 512);
        nwv = ldstr(L, wkv0 + slot + 8192 + k1 * 2048, wkv0 + slot + 8192 + k1 * 2048 + 512);
      }
      __builtin_amdgcn_sched_barrier(0);
      kO = mfma(xr[t], wk, kO);
      kT = mfma(xt, wk, kT);
      vO = mfma(xr[t], wv, vO);
      vT = mfma(xt, wv, vT);
      __builtin_amdgcn_sched_barrier(0);
      if (t + 1 < 32) { xt = nxt; wk = nwk; wv = nwv; }
    }
  }
  ST(4);
  {
    f32x16 kv = {};
    float zs = 0.f;
#pragma unroll
    for (int s = 0; s < 2; ++s) {
      const bf16x8 pa = phi8(kO, s), pb = phi8(kT, s);
      kv = mfma(pa, cvt8(vO, s), kv);
      kv = mfma(pb, cvt8(vT, s), kv);
      zs = sum8(pa, zs); zs = sum8(pb, zs);
    }
    zs += __shfl_xor(zs, 32);
    f32x4 lo, hi;
#pragma unroll
    for (int j = 0; j < 4; ++j) { lo[j] = g1 ? kv[8 + j] : kv[j]; hi[j] = g1 ? kv[12 + j] : kv[4 + j]; }
    const int hh = 2 * wc + g1;
    const unsigned a = SCR + (4 * wr + hh) * 1024 + (h * 16 + (lane & 15)) * 16;
    *(LAS f32x4*)(L + a) = lo;
    *(LAS f32x4*)(L + a + 512) = hi;
    if (h == 0) *(LAS float*)(L + SCR + (8 + hh) * 1024 + wr * 64 + (lane & 15) * 4) = zs;
  }
  WAITL0();
  ST(5);
  BAR();
  ST(6);
  f32x16 qa = {}, qb = {};
  {
    const unsigned wq0 = WQ + wn0, wq1 = WQ + (wn0 ^ 64);
    bf16x8 a0 = ldstr(L, wq0, wq0 + 512), a1 = ldstr(L, wq1, wq1 + 512);
#pragma unroll
    for (int t = 0; t < 32; ++t) {
      bf16x8 n0, n1;
      if (t + 1 < 32) {
        const unsigned o = (t + 1) * 2048;
        n0 = ldstr(L, wq0 + o, wq0 + o + 512);
        n1 = ldstr(L, wq1 + o, wq1 + o + 512);
      }
      __builtin_amdgcn_sched_barrier(0);
      qa = mfma(a0, xr[t], qa);
      qb = mfma(a1, xr[t], qb);
      __builtin_amdgcn_sched_barrier(0);
      if (t + 1 < 32) { a0 = n0; a1 = n1; }
    }
  }
  ST(7);
  bf16x8 pq[4];
  pq[0] = phi8(qa, 0); pq[1] = phi8(qa, 1); pq[2] = phi8(qb, 0); pq[3] = phi8(qb, 1);
  const rsrc_t rsWS = __builtin_amdgcn_make_buffer_rsrc((void*)wsp, 0, WS_END, 0x00020000);
  WAITL0();
  ST(8);
  BAR();
  ST(9);
  {
    const unsigned st = 4 * ti + own;
#pragma unroll
    for (int hh = 0; hh < 4; ++hh) {
      const bf16x8 af = ldsr128(L, AFR + hh * 1024 + lane * 16);
      f32x16 d = {};
      d = mfma(af, pq[hh], d);
      const float inv = __builtin_amdgcn_rcpf(d[8] + 1e-6f);
      const bf16x8 o = pack8(d[0] * inv, d[1] * inv, d[2] * inv, d[3] * inv, d[4] * inv, d[5] * inv, d[6] * inv, d[7] * inv);
      __builtin_amdgcn_raw_buffer_store_b128(__builtin_bit_cast(u32x4, o), rsWS, lane * 16, WS_O + (st * 64 + 4 * g + hh) * 1024, 16);
    }
    ST(10);
    WAITV(0);
    asm volatile("" :: "v"(ctouch));
    if (lane == 0) st_flag(ws, WS_FLAGB + (st * 16 + g) * 128, tag);
  }
  ST(11);
  BAR();
  ST(12);
  {
    const int br2 = wave >> 1, sh = wave & 1;
    const unsigned st2 = 2 * rt + sh;
    wait16(ws, WS_FLAGB + st2 * 16 * 128, tag, lane);
    ST(13);
    if (b == 0 && wave == 0 && lane == 0) st_flag(ws, WS_EPOCH, tag);
    u32x4 of[32];
    const unsigned ob = WS_O + (st2 * 64 + 32 * br2) * 1024;
#pragma unroll
    for (int j = 0; j < 32; ++j) of[j] = __builtin_amdgcn_raw_buffer_load_b128(rsWS, lane * 16, ob + j * 1024, 16);
    const float gv = __hip_atomic_load((GAS float*)(ws + WS_GATE) + st2 * 32 + r32, __ATOMIC_RELAXED, __HIP_MEMORY_SCOPE_AGENT);
    const unsigned wp0 = (br2 ? WOB : WOA) + (4 * h + q) * 128 + sw + (p & 1) * 8, wp1 = wp0 ^ 64;
    f32x16 c0 = {}, c1 = {};
    bf16x8 a0 = ldstr(L, wp0, wp0 + 1024), a1 = ldstr(L, wp1, wp1 + 1024);
#pragma unroll
    for (int j = 0; j < 32; ++j) {
      bf16x8 n0, n1;
      if (j + 1 < 32) {
        const unsigned o = (j + 1) * 2048;
        n0 = ldstr(L, wp0 + o, wp0 + o + 1024);
        n1 = ldstr(L, wp1 + o, wp1 + o + 1024);
      }
      __builtin_amdgcn_sched_barrier(0);
      const bf16x8 bf = __builtin_bit_cast(bf16x8, of[j]);
      c0 = mfma(a0, bf, c0);
      c1 = mfma(a1, bf, c1);
      __builtin_amdgcn_sched_barrier(0);
      if (j + 1 < 32) { a0 = n0; a1 = n1; }
    }
    ST(14);
    const float gs = br2 ? 1.0f - gv : gv;
    f32x16 keep, send;
#pragma unroll
    for (int i = 0; i < 16; ++i) { keep[i] = (br2 ? c1[i] : c0[i]) * gs; send[i] = (br2 ? c0[i] : c1[i]) * gs; }
#pragma unroll
    for (int qq = 0; qq < 4; ++qq) {
      const f32x4 v = {send[4 * qq], send[4 * qq + 1], send[4 * qq + 2], send[4 * qq + 3]};
      *(LAS f32x4*)(L + EXC + wave * 4096 + qq * 1024 + lane * 16) = v;
    }
    WAITL0();
    BAR();
    unsigned pk[8];
#pragma unroll
    for (int qq = 0; qq < 4; ++qq) {
      const f32x4 v = *(LAS f32x4*)(L + EXC + (wave ^ 2) * 4096 + qq * 1024 + lane * 16);
      pk[2 * qq] = __builtin_bit_cast(unsigned, cvt2(keep[4 * qq] + v[0], keep[4 * qq + 1] + v[1]));
      pk[2 * qq + 1] = __builtin_bit_cast(unsigned, cvt2(keep[4 * qq + 2] + v[2], keep[4 * qq + 3] + v[3]));
    }
    const rsrc_t rsO = __builtin_amdgcn_make_buffer_rsrc((void*)out, 0, S_ * D_ * 2, 0x00020000);
    const unsigned orow = (st2 * 32 + r32) * 1024 + (64 * ct + 32 * br2) * 2 + h * 16;
#pragma unroll
    for (int qq = 0; qq < 4; qq += 2) {
      const auto s0 = __builtin_amdgcn_permlane32_swap(pk[2 * qq], pk[2 * qq + 2], false, false);
      const auto s1 = __builtin_amdgcn_permlane32_swap(pk[2 * qq + 1], pk[2 * qq + 3], false, false);
      const u32x4 v = {s0[0], s1[0], s0[1], s1[1]};
      __builtin_amdgcn_raw_buffer_store_b128(v, rsO, orow + qq * 16, 0, 0);
    }
    ST(15); STR(16); STFLUSH();
  }
}

extern "C" void kernel_launch(void* const* d_in, const int* in_sizes, int n_in, void* d_out, int out_size, void* d_ws, size_t ws_size,
                              hipStream_t stream) {
  (void)in_sizes; (void)n_in; (void)out_size; (void)ws_size;
  const u16* x = (const u16*)d_in[0];
  const u16* wA = (const u16*)d_in[1];
  const u16* woA = (const u16*)d_in[2];
  const u16* wB = (const u16*)d_in[4];
  const u16* woB = (const u16*)d_in[5];
  const u16* gw = (const u16*)d_in[6];
  (void)hipFuncSetAttribute((const void*)gated_mix_fused, hipFuncAttributeMaxDynamicSharedMemorySize, LDS_BYTES);
  gated_mix_fused<<<256, 512, LDS_BYTES, stream>>>(x, wA, wB, woA, woB, gw, (u16*)d_out, (char*)d_ws);
}
```

```cpp
#include <hip/hip_runtime.h>
#include <hip/hip_bf16.h>
#include <stdint.h>

#define LAS __attribute__((address_space(3)))
#define GAS __attribute__((address_space(1)))
#define DI __device__ __forceinline__

typedef unsigned short u16;
typedef __bf16 bf16x8 __attribute__((ext_vector_type(8)));
typedef __bf16 bf16x4 __attribute__((ext_vector_type(4)));
typedef __bf16 bf16x2 __attribute__((ext_vector_type(2)));
typedef float f32x16 __attribute__((ext_vector_type(16)));
typedef float f32x4 __attribute__((ext_vector_type(4)));
typedef float f32x2 __attribute__((ext_vector_type(2)));
typedef unsigned u32x4 __attribute__((ext_vector_type(4)));
typedef unsigned u32x2 __attribute__((ext_vector_type(2)));
typedef __amdgpu_buffer_rsrc_t rsrc_t;

#define ROT 1
#define CODE_PREFETCH 22016
#define CLR_STAGE 6
#define HPRIO 3
#define OD 10
#define CODE_BACK 0

constexpr int S_ = 2048, D_ = 512;
constexpr unsigned XR = 0;
constexpr unsigned WR = 49152;
constexpr unsigned WQ = 98304;
constexpr unsigned WOA = 0;
constexpr unsigned WOB = 98304;
constexpr unsigned SCR = 81920;
constexpr unsigned AFR = SCR + 12288;
constexpr unsigned EXC = SCR;
constexpr unsigned LDS_BYTES = 163840;
constexpr unsigned WS_EPOCH = 0;
constexpr unsigned WS_FLAGC = 4096;
constexpr unsigned WS_GATE = WS_FLAGC + 1024 * 128;
constexpr unsigned WS_PART = WS_GATE + 8192;
constexpr unsigned PART_STRIDE = 1152;
constexpr unsigned WS_O = WS_PART + 1024 * PART_STRIDE;
constexpr unsigned WS_END = WS_O + 64 * 64 * 1024;
constexpr unsigned SPIN_CAP = 1u << 18;

#define ST(i) do {} while (0)
#define STR(i) do {} while (0)
#define STFLUSH() do {} while (0)

DI void glds(unsigned voff, rsrc_t rs, unsigned soff, unsigned ldsaddr) {
  asm volatile("s_mov_b32 m0, %0\n\ts_nop 0\n\tbuffer_load_dwordx4 %1, %2, %3 offen lds"
               :: "s"(ldsaddr), "v"(voff), "s"(rs), "s"(soff) : "memory");
}
template <int AUX> DI void st128(u32x4 v, rsrc_t rs, unsigned off) { __builtin_amdgcn_raw_buffer_store_b128(v, rs, off, 0, AUX); }
#define WAITV(n) asm volatile("s_waitcnt vmcnt(" #n ")" ::: "memory")
#define WAITL0() asm volatile("s_waitcnt lgkmcnt(0)" ::: "memory")
#define BAR() asm volatile("s_barrier" ::: "memory")

DI f32x16 mfma(bf16x8 a, bf16x8 b, f32x16 c) { return __builtin_amdgcn_mfma_f32_32x32x16_bf16(a, b, c, 0, 0, 0); }
DI bf16x8 ldsr128(LAS char* L, unsigned a) { return *(LAS bf16x8*)(L + a); }
DI bf16x8 ldstr(LAS char* L, unsigned a0, unsigned a1) {
  bf16x4 lo = __builtin_amdgcn_ds_read_tr16_b64_v4bf16((LAS bf16x4*)(L + a0));
  bf16x4 hi = __builtin_amdgcn_ds_read_tr16_b64_v4bf16((LAS bf16x4*)(L + a1));
  return __builtin_shufflevector(lo, hi, 0, 1, 2, 3, 4, 5, 6, 7);
}
DI bf16x2 cvt2(float a, float b) { f32x2 p = {a, b}; return __builtin_convertvector(p, bf16x2); }
DI bf16x8 pack8(float a0, float a1, float a2, float a3, float a4, float a5, float a6, float a7) {
  bf16x2 p0 = cvt2(a0, a1), p1 = cvt2(a2, a3), p2 = cvt2(a4, a5), p3 = cvt2(a6, a7);
  bf16x4 lo = __builtin_shufflevector(p0, p1, 0, 1, 2, 3), hi = __builtin_shufflevector(p2, p3, 0, 1, 2, 3);
  return __builtin_shufflevector(lo, hi, 0, 1, 2, 3, 4, 5, 6, 7);
}
DI unsigned phi2(float a, float b) {
  const f32x2 t = {a, b};
  const f32x2 u = t * 1.44269504088896340736f;
  const f32x2 e = {__builtin_amdgcn_fmed3f(__builtin_amdgcn_exp2f(u[0]), 0.0f, 1.0f), __builtin_amdgcn_fmed3f(__builtin_amdgcn_exp2f(u[1]), 0.0f, 1.0f)};
  const int ia = __builtin_bit_cast(int, a), ib = __builtin_bit_cast(int, b);
  const f32x2 m = {__builtin_bit_cast(float, ia > 0 ? ia : 0), __builtin_bit_cast(float, ib > 0 ? ib : 0)};
  const f32x2 r = (m + e) + 1e-6f;
  return __builtin_bit_cast(unsigned, __builtin_convertvector(r, bf16x2));
}
DI bf16x8 phi8(const f32x16& x, int s) {
  const u32x4 v = {phi2(x[8 * s], x[8 * s + 1]), phi2(x[8 * s + 2], x[8 * s + 3]), phi2(x[8 * s + 4], x[8 * s + 5]), phi2(x[8 * s + 6], x[8 * s + 7])};
  return __builtin_bit_cast(bf16x8, v);
}
DI float bsel(unsigned m, float a, float b) {
  return __builtin_bit_cast(float, (__builtin_bit_cast(unsigned, a) & ~m) | (__builtin_bit_cast(unsigned, b) & m));
}
DI float fadd(float a, float b) { float r; asm("v_add_f32 %0, %1, %2" : "=v"(r) : "v"(a), "v"(b)); return r; }
DI f32x4 fadd4(f32x4 a, f32x4 b) { return f32x4{fadd(a[0], b[0]), fadd(a[1], b[1]), fadd(a[2], b[2]), fadd(a[3], b[3])}; }
DI float ffma(float a, float b, float c) { float r; asm("v_fma_f32 %0, %1, %2, %3" : "=v"(r) : "v"(a), "v"(b), "v"(c)); return r; }
template <int CTRL, int RM> DI float dpp_add(float v) {
  return fadd(v, __builtin_bit_cast(float, __builtin_amdgcn_update_dpp(0, __builtin_bit_cast(int, v), CTRL, RM, 0xf, true)));
}
DI float wave_sum63(float v) {
  v = dpp_add<0x111, 0xf>(v); v = dpp_add<0x112, 0xf>(v); v = dpp_add<0x114, 0xf>(v); v = dpp_add<0x118, 0xf>(v);
  v = dpp_add<0x142, 0xa>(v); v = dpp_add<0x143, 0xc>(v);
  return v;
}
constexpr unsigned SENT = 0xFFFFFFFFu;
DI unsigned nosent(unsigned v) { return v < SENT ? v : SENT - 1u; }
DI u32x4 nosent4(u32x4 v) { return u32x4{nosent(v[0]), nosent(v[1]), nosent(v[2]), nosent(v[3])}; }
DI unsigned umax3(unsigned a, unsigned b, unsigned c) { unsigned m = a > b ? a : b; return m > c ? m : c; }
#define SB() __builtin_amdgcn_sched_barrier(0)
DI bf16x8 cvt8(const f32x16& x, int s) {
  return pack8(x[8 * s], x[8 * s + 1], x[8 * s + 2], x[8 * s + 3], x[8 * s + 4], x[8 * s + 5], x[8 * s + 6], x[8 * s + 7]);
}
DI float sum8(bf16x8 v, float acc) {
  const bf16x2 ones = {(__bf16)1.0f, (__bf16)1.0f};
  acc = __builtin_amdgcn_fdot2_f32_bf16(__builtin_shufflevector(v, v, 0, 1), ones, acc, false);
  acc = __builtin_amdgcn_fdot2_f32_bf16(__builtin_shufflevector(v, v, 2, 3), ones, acc, false);
  acc = __builtin_amdgcn_fdot2_f32_bf16(__builtin_shufflevector(v, v, 4, 5), ones, acc, false);
  acc = __builtin_amdgcn_fdot2_f32_bf16(__builtin_shufflevector(v, v, 6, 7), ones, acc, false);
  return acc;
}
DI unsigned long long ld_flag(GAS char* ws, unsigned off) {
  return __hip_atomic_load((GAS unsigned long long*)(ws + off), __ATOMIC_RELAXED, __HIP_MEMORY_SCOPE_AGENT);
}
DI void st_flag(GAS char* ws, unsigned off, unsigned long long v) {
  __hip_atomic_store((GAS unsigned long long*)(ws + off), v, __ATOMIC_RELAXED, __HIP_MEMORY_SCOPE_AGENT);
}
DI unsigned long long load_epoch(const char* wsp) {
  unsigned long long e;
  asm volatile("s_load_dwordx2 %0, %1, 0x0\n\ts_waitcnt lgkmcnt(0)" : "=s"(e) : "s"(wsp) : "memory");
  return e;
}
DI void wait_flags(GAS char* ws, unsigned off, unsigned long long tag) {
  for (unsigned spins = 0; spins < SPIN_CAP; ++spins) {
    const unsigned long long v = ld_flag(ws, off);
    if (__all(v == tag)) break;
    __builtin_amdgcn_s_sleep(1);
  }
  asm volatile("" ::: "memory");
}
DI unsigned ld_u32(GAS char* ws, unsigned off) {
  return __hip_atomic_load((GAS unsigned*)(ws + off), __ATOMIC_RELAXED, __HIP_MEMORY_SCOPE_AGENT);
}
DI void wait_written(GAS char* ws, unsigned off) {
  for (unsigned spins = 0; spins < SPIN_CAP; ++spins) {
    const unsigned v = ld_u32(ws, off);
    if (__all(v != SENT)) break;
    __builtin_amdgcn_s_sleep(1);
  }
  asm volatile("" ::: "memory");
}

__global__ __launch_bounds__(512, 2) void gated_mix_fused(const u16* __restrict__ x, const u16* __restrict__ wA, const u16* __restrict__ wB,
                                                          const u16* __restrict__ woA, const u16* __restrict__ woB, const u16* __restrict__ gw,
                                                          u16* __restrict__ out, char* __restrict__ wsp
                                                          ) {
  extern __shared__ __attribute__((aligned(16))) char lds[];
  LAS char* L = (LAS char*)lds;
  const int tid = threadIdx.x;
  const int wave = __builtin_amdgcn_readfirstlane(tid >> 6), lane = tid & 63;
  const int b = blockIdx.x, xcd = b & 7, r = b >> 3;
  const int ti = (xcd & 1) * 8 + (r & 7), g = (xcd >> 1) * 4 + (r >> 3);
  const int gg = g & 7;
  const int rt = 4 * xcd + (r & 3), ct = r >> 2;
  GAS char* ws = (GAS char*)wsp;
  STR(0); ST(1);
  unsigned long long pc;
  asm volatile("s_getpc_b64 %0" : "=s"(pc));
  pc &= ~255ull;

  if (wave >= 4) {
    const int hw = wave - 4;
    __builtin_amdgcn_s_setprio(HPRIO);
    int ro = 0;
    if (ROT == 1) ro = (ti + 2 * g) & 7;
    if (ROT == 2) ro = 2 * ((ti + g) & 1);
    if (ROT == 3) ro = 4 * ((ti + g) & 1);
    if (ROT == 5) ro = ((ti + g) % 3) * 2;
    const u16* w = (g >= 8) ? wB : wA;
    const rsrc_t rsX = __builtin_amdgcn_make_buffer_rsrc((void*)x, 0, S_ * D_ * 2, 0x00020000);
    const rsrc_t rsW = __builtin_amdgcn_make_buffer_rsrc((void*)w, 0, D_ * 3 * D_ * 2, 0x00020000);
    const int rin = lane >> 3, ch = lane & 7;
    const unsigned vx = rin * 1024 + ((ch ^ (4 * (hw & 1) + (rin >> 1))) << 4);
    const unsigned csw = (ch ^ (((rin >> 1) & 1) << 2)) << 4;
    const unsigned vw = rin * 3072 + csw;
    const unsigned sx0 = (ti * 128 + 8 * hw) * 1024;
    const unsigned sw0 = 8 * hw * 3072 + gg * 128;
    auto stage = [&](int s) {
      const unsigned ks = (s + ro) & 7, slot = (s % 3) * 16384;
#pragma unroll
      for (int i = 0; i < 4; ++i) glds(vx, rsX, sx0 + ks * 128 + i * 32768, XR + slot + (hw + 4 * i) * 1024);
#pragma unroll
      for (int i = 0; i < 2; ++i) glds(vw, rsW, sw0 + ks * 196608 + i * 98304 + 1024, WR + slot + (hw + 4 * i) * 1024);
#pragma unroll
      for (int i = 0; i < 2; ++i) glds(vw, rsW, sw0 + ks * 196608 + i * 98304 + 2048, WR + slot + 8192 + (hw + 4 * i) * 1024);
    };
    auto stageq = [&](int s) {
      const unsigned ks = (s + ro) & 7;
#pragma unroll
      for (int i = 0; i < 2; ++i) glds(vw, rsW, sw0 + ks * 196608 + i * 98304, WQ + s * 8192 + (hw + 4 * i) * 1024);
    };
    stage(0); stage(1);
    BAR();
    const unsigned long long tag = load_epoch(wsp) + 1ull;
    ST(2);
    WAITV(8);
    BAR();
    ST(3);
#pragma unroll
    for (int s = 1; s < 8; ++s) {
      if (s + 1 < 8) { stage(s + 1); WAITV(8); }
      else { stageq(0); stageq(1); stageq(2); stageq(3); WAITV(8); }
      BAR();
    }
    ST(4);
    stageq(4); stageq(5); stageq(6); stageq(7);
    const rsrc_t rsWS = __builtin_amdgcn_make_buffer_rsrc((void*)wsp, 0, WS_END, 0x00020000);
    const rsrc_t rsOA = __builtin_amdgcn_make_buffer_rsrc((void*)woA, 0, D_ * D_ * 2, 0x00020000);
    const rsrc_t rsOB = __builtin_amdgcn_make_buffer_rsrc((void*)woB, 0, D_ * D_ * 2, 0x00020000);
    const unsigned vo = rin * 1024 + csw;
    const unsigned hid = g * 4 + hw;
    u32x4 wob[16];
    const rsrc_t rsG = __builtin_amdgcn_make_buffer_rsrc((void*)gw, 0, D_ * 2, 0x00020000);
    const unsigned grow = ti * 128 + 8 * g + 2 * hw;
    const u32x4 x0 = __builtin_amdgcn_raw_buffer_load_b128(rsX, lane * 16, grow * 1024, 0);
    const u32x4 x1 = __builtin_amdgcn_raw_buffer_load_b128(rsX, lane * 16 + 1024, grow * 1024, 0);
    const u32x4 gv = __builtin_amdgcn_raw_buffer_load_b128(rsG, lane * 16, 0, 0);
    const unsigned fo = (lane & 16) ? ((lane & 15) * 4 + ((2 * rt + (hw & 1)) & 3)) * 16 + (rt >> 1) : (g * 4 + hw) * 16 + (lane & 15);
    const unsigned foff = WS_FLAGC + fo * 128;
    const unsigned long long fc = ld_flag(ws, foff);
    asm volatile("" ::: "memory");
#pragma unroll
    for (int i = 0; i < 16; ++i) {
      wob[i] = __builtin_amdgcn_raw_buffer_load_b128(rsOB, vo, (hw + 4 * i) * 8192 + ct * 128, 0);
    }
    ST(15);
    WAITV(20);
    ST(5);
    BAR();
    ST(6);
    {
      const f32x4 p0 = *(LAS f32x4*)(L + SCR + hw * 1024 + lane * 16), p1 = *(LAS f32x4*)(L + SCR + (4 + hw) * 1024 + lane * 16);
      const f32x4 z0 = *(LAS f32x4*)(L + SCR + (8 + hw) * 1024 + (lane & 3) * 16), z1 = *(LAS f32x4*)(L + SCR + (8 + hw) * 1024 + 64 + (lane & 3) * 16);
      const unsigned po = WS_PART + (hid * 16 + ti) * PART_STRIDE;
      st128<16>(nosent4(__builtin_bit_cast(u32x4, fadd4(p0, p1))), rsWS, (lane * 16) + (po));
      if (lane < 4) st128<16>(nosent4(__builtin_bit_cast(u32x4, fadd4(z0, z1))), rsWS, (1024 + lane * 16) + (po));
    }
    ST(7);
#pragma unroll
    for (int i = 0; i < 16; ++i) glds(vo, rsOA, (hw + 4 * i) * 8192 + ct * 128, WOA + (hw + 4 * i) * 1024);
    ST(8);
    {
      float a0 = 0.f, a1 = 0.f;
#pragma unroll
      for (int i = 0; i < 4; ++i) {
        const unsigned ug = gv[i], u0 = x0[i], u1 = x1[i];
        const float gl = __builtin_bit_cast(float, ug << 16), gh = __builtin_bit_cast(float, ug & 0xffff0000u);
        a0 = ffma(__builtin_bit_cast(float, u0 << 16), gl, a0); a0 = ffma(__builtin_bit_cast(float, u0 & 0xffff0000u), gh, a0);
        a1 = ffma(__builtin_bit_cast(float, u1 << 16), gl, a1); a1 = ffma(__builtin_bit_cast(float, u1 & 0xffff0000u), gh, a1);
      }
      a0 = wave_sum63(a0); a1 = wave_sum63(a1);
      const float s0 = __builtin_bit_cast(float, __builtin_amdgcn_readlane(__builtin_bit_cast(int, a0), 63));
      const float s1 = __builtin_bit_cast(float, __builtin_amdgcn_readlane(__builtin_bit_cast(int, a1), 63));
      const float lg = (lane & 1) ? s1 : s0;
      const float gval = __builtin_amdgcn_rcpf(1.0f + __builtin_amdgcn_exp2f(-1.44269504088896340736f * lg));
      if (lane < 2) __hip_atomic_store((GAS unsigned*)(ws + WS_GATE) + grow + lane, nosent(__builtin_bit_cast(unsigned, gval)), __ATOMIC_RELAXED, __HIP_MEMORY_SCOPE_AGENT);
    }
    if (!__all(fc == tag)) wait_flags(ws, foff, tag);
    const unsigned pb = WS_PART + hid * 16 * PART_STRIDE;
    wait_written(ws, pb + (lane & 15) * PART_STRIDE + 1024 + 60);
    ST(9);
    {
      f32x4 a, z;
      for (unsigned spins = 0; spins < SPIN_CAP; ++spins) {
        u32x4 pv[16];
#pragma unroll
        for (int p = 0; p < 16; ++p) pv[p] = __builtin_amdgcn_raw_buffer_load_b128(rsWS, lane * 16, pb + p * PART_STRIDE, 16);
        const u32x4 zv = __builtin_amdgcn_raw_buffer_load_b128(rsWS, (lane >> 2) * PART_STRIDE + 1024 + (lane & 3) * 16, pb, 16);
        unsigned mx = umax3(zv[0], zv[1], umax3(zv[2], zv[3], 0u));
        a = __builtin_bit_cast(f32x4, pv[0]);
        mx = umax3(pv[0][0], pv[0][1], umax3(pv[0][2], pv[0][3], mx));
#pragma unroll
        for (int p = 1; p < 16; ++p) {
          a += __builtin_bit_cast(f32x4, pv[p]);
          mx = umax3(pv[p][0], pv[p][1], umax3(pv[p][2], pv[p][3], mx));
        }
        z = __builtin_bit_cast(f32x4, zv);
        if (__all(mx != SENT)) break;
      }
#pragma unroll
      for (int m = 4; m <= 32; m <<= 1) {
        z[0] += __shfl_xor(z[0], m); z[1] += __shfl_xor(z[1], m); z[2] += __shfl_xor(z[2], m); z[3] += __shfl_xor(z[3], m);
      }
      const int slot = lane & 31, jh = lane >> 5;
      u32x2 ka = {__builtin_bit_cast(unsigned, cvt2(a[0], a[1])), __builtin_bit_cast(unsigned, cvt2(a[2], a[3]))};
      *(LAS u32x2*)(L + AFR + hw * 1024 + ((slot >> 4) * 32 + (slot & 15)) * 16 + jh * 8) = ka;
      const int q4 = lane & 3;
      u32x2 kz = {__builtin_bit_cast(unsigned, cvt2(z[0], z[1])), __builtin_bit_cast(unsigned, cvt2(z[2], z[3]))};
      *(LAS u32x2*)(L + AFR + hw * 1024 + ((q4 & 1) * 32 + 16 + (lane >> 2)) * 16 + (q4 >> 1) * 8) = kz;
    }
    WAITL0();
    ST(10);
    BAR();
    ST(11);
#pragma unroll
    for (int i = 0; i < 16; ++i) *(LAS u32x4*)(L + WOB + (hw + 4 * i) * 1024 + lane * 16) = wob[i];
    WAITV(0); WAITL0();
    ST(12);
    BAR();
    {
      const int r32 = lane & 31, h = lane >> 5, g1 = (lane >> 4) & 1, q = (lane & 15) >> 2, p = lane & 3;
      const unsigned sw = ((2 * g1 + (p >> 1)) ^ (((q >> 1) & 1) << 2)) << 4;
      const int br2 = hw >> 1, sh = hw & 1;
      const unsigned st2 = 2 * rt + sh;
      const unsigned ob = WS_O + (st2 * 64 + 32 * br2) * 1024;
      wait_written(ws, ob + ((lane & 7) * 4 + 3) * 1024 + 1020);
      ST(13);
      unsigned wp0 = ((br2 ? WOB : WOA) + (4 * h + q) * 128 + sw + (p & 1) * 8) ^ (br2 << 6), wp1 = wp0 ^ 64;
      asm volatile("" : "+v"(wp0), "+v"(wp1));
      f32x16 c0, c1;
      unsigned gvb;
      for (unsigned spins = 0; spins < SPIN_CAP; ++spins) {
        u32x4 of[32];
        gvb = ld_u32(ws, WS_GATE + (st2 * 32 + r32) * 4);
  #pragma unroll
        for (int j = 0; j < OD; ++j) of[j] = __builtin_amdgcn_raw_buffer_load_b128(rsWS, lane * 16, ob + j * 1024, 16);
        c0 = f32x16{}; c1 = f32x16{};
        unsigned mx = 0;
        bf16x8 a0[3], a1[3];
        a0[0] = ldstr(L, wp0, wp0 + 1024); a1[0] = ldstr(L, wp1, wp1 + 1024);
        a0[1] = ldstr(L, wp0 + 2048, wp0 + 2048 + 1024); a1[1] = ldstr(L, wp1 + 2048, wp1 + 2048 + 1024);
  #pragma unroll
        for (int j = 0; j < 32; ++j) {
          const unsigned o = (j + 2) * 2048;
          const bf16x8 bf = __builtin_bit_cast(bf16x8, of[j]);
          SB();
          c0 = mfma(a0[j % 3], bf, c0);
          SB();
          if (j + OD < 32) of[j + OD] = __builtin_amdgcn_raw_buffer_load_b128(rsWS, lane * 16, ob + (j + OD) * 1024, 16);
          if (j + 2 < 32) a0[(j + 2) % 3] = ldstr(L, wp0 + o, wp0 + o + 1024);
          mx = umax3(of[j][0], of[j][1], mx);
          SB();
          c1 = mfma(a1[j % 3], bf, c1);
          SB();
          if (j + 2 < 32) a1[(j + 2) % 3] = ldstr(L, wp1 + o, wp1 + o + 1024);
          mx = umax3(of[j][2], of[j][3], mx);
        }
        SB();
        mx = mx > gvb ? mx : gvb;
        if (__all(mx != SENT)) break;
      }
      ST(14);
      __builtin_amdgcn_s_setprio(0);
      const float gv = __builtin_bit_cast(float, gvb);
      const float gs = br2 ? 1.0f - gv : gv;
  #pragma unroll
      for (int qq = 0; qq < 4; ++qq) {
        const f32x4 v = {c1[4 * qq] * gs, c1[4 * qq + 1] * gs, c1[4 * qq + 2] * gs, c1[4 * qq + 3] * gs};
        *(LAS f32x4*)(L + EXC + hw * 4096 + qq * 1024 + lane * 16) = v;
      }
      WAITL0();
      BAR();
      if (b == 0 && hw == 0 && lane == 0) st_flag(ws, WS_EPOCH, tag);
      unsigned pk[8];
  #pragma unroll
      for (int qq = 0; qq < 4; ++qq) {
        const f32x4 v = *(LAS f32x4*)(L + EXC + (hw ^ 2) * 4096 + qq * 1024 + lane * 16);
        pk[2 * qq] = __builtin_bit_cast(unsigned, cvt2(c0[4 * qq] * gs + v[0], c0[4 * qq + 1] * gs + v[1]));
        pk[2 * qq + 1] = __builtin_bit_cast(unsigned, cvt2(c0[4 * qq + 2] * gs + v[2], c0[4 * qq + 3] * gs + v[3]));
      }
      const rsrc_t rsO = __builtin_amdgcn_make_buffer_rsrc((void*)out, 0, S_ * D_ * 2, 0x00020000);
      const unsigned orow = (st2 * 32 + r32) * 1024 + (64 * ct + 32 * br2) * 2 + h * 16;
  #pragma unroll
      for (int qq = 0; qq < 4; qq += 2) {
        const auto s0 = __builtin_amdgcn_permlane32_swap(pk[2 * qq], pk[2 * qq + 2], false, false);
        const auto s1 = __builtin_amdgcn_permlane32_swap(pk[2 * qq + 1], pk[2 * qq + 3], false, false);
        const u32x4 v = {s0[0], s1[0], s0[1], s1[1]};
        st128<0>(v, rsO, orow + qq * 16);
      }
      STR(16); STFLUSH();
    }
    return;
  }

  const int wr = wave >> 1, wc = wave & 1;
  const int r32 = lane & 31, h = lane >> 5;
  const int g1 = (lane >> 4) & 1, q = (lane & 15) >> 2, p = lane & 3;
  const int own = wave, oth = wave ^ 1;
  const rsrc_t rsWS = __builtin_amdgcn_make_buffer_rsrc((void*)wsp, 0, WS_END, 0x00020000);
  unsigned ctouch = 0;
  {
    const unsigned off = (wave * 64 + lane) * 128;
    if (off < CODE_PREFETCH) { const char* a = (const char*)pc + off; asm volatile("global_load_dword %0, %1, off" : "=v"(ctouch) : "v"(a) : "memory"); }
  }
  BAR();
  {
    const u32x4 sent = {SENT, SENT, SENT, SENT};
#pragma unroll
    for (int hh = 0; hh < 4; ++hh) {
      st128<16>(sent, rsWS, (lane * 16) + (WS_O + ((4 * ti + own) * 64 + 4 * g + hh) * 1024));
    }
    const unsigned po = WS_PART + ((g * 4 + wave) * 16 + ti) * PART_STRIDE;
    st128<16>(sent, rsWS, (lane * 16) + (po));
    if (lane < 4) st128<16>(sent, rsWS, (1024 + lane * 16) + (po));
    if (lane < 2) st128<16>(sent, rsWS, (lane * 16) + (WS_GATE + (ti * 128 + 8 * g) * 4));
  }
  const unsigned fx = (h ^ ((r32 >> 1) & 7)) << 4;
  unsigned xo0 = XR + (own * 32 + r32) * 128 + fx, xt0 = XR + (oth * 32 + r32) * 128 + fx;
  const unsigned sw = ((2 * g1 + (p >> 1)) ^ (((q >> 1) & 1) << 2)) << 4;
  const unsigned wn0 = (8 * h + q) * 128 + sw + (p & 1) * 8;
  unsigned wkv0 = WR + (wn0 ^ (wc << 6));
  asm volatile("" : "+v"(wkv0));
  unsigned xoa[4], xta[4];
#pragma unroll
  for (int k = 0; k < 4; ++k) { xoa[k] = xo0 ^ (k << 5); xta[k] = xt0 ^ (k << 5); asm volatile("" : "+v"(xoa[k]), "+v"(xta[k])); }
  const unsigned long long tag = load_epoch(wsp) + 1ull;

  bf16x8 xr[32];
  f32x16 kO = {}, kT = {}, vO = {}, vT = {};
  {
    BAR();
    ST(3);
    bf16x8 xt = ldsr128(L, xta[0]), wk = ldstr(L, wkv0, wkv0 + 512), wv = ldstr(L, wkv0 + 8192, wkv0 + 8192 + 512);
    xr[0] = ldsr128(L, xoa[0]);
#pragma unroll
    for (int t = 0; t < 32; ++t) {
      bf16x8 nxt, nwk, nwv;
      const int s1 = (t + 1) >> 2, k1 = (t + 1) & 3;
      const unsigned slot = (s1 % 3) * 16384;
      if (t + 1 < 32 && k1 == 0) {
        WAITL0();
        if (s1 == CLR_STAGE) { WAITV(0); asm volatile("" :: "v"(ctouch)); if (lane == 0) st_flag(ws, WS_FLAGC + ((g * 4 + wave) * 16 + ti) * 128, tag); }
        BAR();
      }
      SB();
      kO = mfma(xr[t], wk, kO);
      SB();
      if (t + 1 < 32) {
        xr[t + 1] = ldsr128(L, xoa[k1] + slot);
        nwk = ldstr(L, wkv0 + slot + k1 * 2048, wkv0 + slot + k1 * 2048 + 512);
      }
      SB();
      kT = mfma(xt, wk, kT);
      SB();
      if (t + 1 < 32) {
        nxt = ldsr128(L, xta[k1] + slot);
        nwv = ldstr(L, wkv0 + slot + 8192 + k1 * 2048, wkv0 + slot + 8192 + k1 * 2048 + 512);
      }
      SB();
      vO = mfma(xr[t], wv, vO);
      vT = mfma(xt, wv, vT);
      SB();
      if (t + 1 < 32) { xt = nxt; wk = nwk; wv = nwv; }
    }
  }
  ST(4);
  {
    f32x16 kv = {};
    float zs = 0.f;
#pragma unroll
    for (int s = 0; s < 2; ++s) {
      const bf16x8 va = cvt8(vO, s), vb = cvt8(vT, s);
      const bf16x8 pa = phi8(kO, s);
      kv = mfma(pa, va, kv);
      const bf16x8 pb = phi8(kT, s);
      kv = mfma(pb, vb, kv);
      zs = sum8(pa, zs); zs = sum8(pb, zs);
    }
    {
      const unsigned zb = __builtin_bit_cast(unsigned, zs);
      const auto sw2 = __builtin_amdgcn_permlane32_swap(zb, zb, false, false);
      zs = __builtin_bit_cast(float, (unsigned)sw2[0]) + __builtin_bit_cast(float, (unsigned)sw2[1]);
    }
    const unsigned gm = g1 ? 0xFFFFFFFFu : 0u;
    f32x4 lo, hi;
#pragma unroll
    for (int j = 0; j < 4; ++j) { lo[j] = bsel(gm, kv[j], kv[8 + j]); hi[j] = bsel(gm, kv[4 + j], kv[12 + j]); }
    const int hh = 2 * wc + g1;
    const unsigned a = SCR + (4 * wr + hh) * 1024 + (h * 16 + (lane & 15)) * 16;
    *(LAS f32x4*)(L + a) = lo;
    *(LAS f32x4*)(L + a + 512) = hi;
    if (h == 0) *(LAS float*)(L + SCR + (8 + hh) * 1024 + wr * 64 + (lane & 15) * 4) = zs;
  }
  WAITL0();
  ST(5);
  BAR();
  ST(6);
  f32x16 qa = {}, qb = {};
  {
    unsigned wq0 = WQ + wn0, wq1 = WQ + (wn0 ^ 64);
    asm volatile("" : "+v"(wq0), "+v"(wq1));
    bf16x8 a0[3], a1[3];
    a0[0] = ldstr(L, wq0, wq0 + 512); a1[0] = ldstr(L, wq1, wq1 + 512);
    a0[1] = ldstr(L, wq0 + 2048, wq0 + 2048 + 512); a1[1] = ldstr(L, wq1 + 2048, wq1 + 2048 + 512);
#pragma unroll
    for (int t = 0; t < 32; ++t) {
      const unsigned o = (t + 2) * 2048;
      SB();
      qa = mfma(a0[t % 3], xr[t], qa);
      SB();
      if (t + 2 < 32) a0[(t + 2) % 3] = ldstr(L, wq0 + o, wq0 + o + 512);
      SB();
      qb = mfma(a1[t % 3], xr[t], qb);
      SB();
      if (t + 2 < 32) a1[(t + 2) % 3] = ldstr(L, wq1 + o, wq1 + o + 512);
    }
    SB();
  }
  ST(7);
  bf16x8 pq[4];
  pq[0] = phi8(qa, 0); pq[1] = phi8(qa, 1); pq[2] = phi8(qb, 0); pq[3] = phi8(qb, 1);
  WAITL0();
  ST(8);
  BAR();
  ST(9);
  {
    const unsigned st = 4 * ti + own;
    bf16x8 af[4];
#pragma unroll
    for (int hh = 0; hh < 4; ++hh) af[hh] = ldsr128(L, AFR + hh * 1024 + lane * 16);
    f32x16 d[4];
#pragma unroll
    for (int hh = 0; hh < 4; ++hh) { d[hh] = f32x16{}; d[hh] = mfma(af[hh], pq[hh], d[hh]); }
#pragma unroll
    for (int hh = 0; hh < 4; ++hh) {
      const float inv = __builtin_amdgcn_rcpf(d[hh][8] + 1e-6f);
      const bf16x8 o = pack8(d[hh][0] * inv, d[hh][1] * inv, d[hh][2] * inv, d[hh][3] * inv, d[hh][4] * inv, d[hh][5] * inv, d[hh][6] * inv, d[hh][7] * inv);
      st128<16>(nosent4(__builtin_bit_cast(u32x4, o)), rsWS, (lane * 16) + (WS_O + (st * 64 + 4 * g + hh) * 1024));
    }
  }
  ST(10);
  BAR();
  STR(16); STFLUSH();
}

extern "C" void kernel_launch(void* const* d_in, const int* in_sizes, int n_in, void* d_out, int out_size, void* d_ws, size_t ws_size,
                              hipStream_t stream) {
  (void)in_sizes; (void)n_in; (void)out_size; (void)ws_size;
  const u16* x = (const u16*)d_in[0];
  const u16* wA = (const u16*)d_in[1];
  const u16* woA = (const u16*)d_in[2];
  const u16* wB = (const u16*)d_in[4];
  const u16* woB = (const u16*)d_in[5];
  const u16* gw = (const u16*)d_in[6];
  (void)hipFuncSetAttribute((const void*)gated_mix_fused, hipFuncAttributeMaxDynamicSharedMemorySize, LDS_BYTES);
  gated_mix_fused<<<256, 512, LDS_BYTES, stream>>>(x, wA, wB, woA, woB, gw, (u16*)d_out, (char*)d_ws);
}
```

```cpp
#include <hip/hip_runtime.h>
#include <hip/hip_bf16.h>
#include <stdint.h>

#define LAS __attribute__((address_space(3)))
#define GAS __attribute__((address_space(1)))
#define DI __device__ __forceinline__

typedef unsigned short u16;
typedef __bf16 bf16x8 __attribute__((ext_vector_type(8)));
typedef __bf16 bf16x4 __attribute__((ext_vector_type(4)));
typedef __bf16 bf16x2 __attribute__((ext_vector_type(2)));
typedef float f32x16 __attribute__((ext_vector_type(16)));
typedef float f32x4 __attribute__((ext_vector_type(4)));
typedef float f32x2 __attribute__((ext_vector_type(2)));
typedef unsigned u32x4 __attribute__((ext_vector_type(4)));
typedef unsigned u32x2 __attribute__((ext_vector_type(2)));
typedef __amdgpu_buffer_rsrc_t rsrc_t;

#define ROT 1
#define CODE_PREFETCH 0
#define CLR_STAGE 6
#define HPRIO 3
#define OD 10
#define TOUCH 0
#define PSLEEP 1
#define CODE_BACK 0

constexpr int S_ = 2048, D_ = 512;
constexpr unsigned XR = 0;
constexpr unsigned WR = 49152;
constexpr unsigned WQ = 98304;
constexpr unsigned WOA = 0;
constexpr unsigned WOB = 98304;
constexpr unsigned SCR = 81920;
constexpr unsigned AFR = SCR + 12288;
constexpr unsigned EXC = SCR;
constexpr unsigned LDS_BYTES = 163840;
constexpr unsigned WS_EPOCH = 0;
constexpr unsigned WS_FLAGC = 4096;
constexpr unsigned WS_FLAGB = WS_FLAGC + 1024 * 128;
constexpr unsigned WS_GATE = WS_FLAGB + 1024 * 128;
constexpr unsigned WS_PART = WS_GATE + 8192;
constexpr unsigned PART_STRIDE = 1152;
constexpr unsigned WS_O = WS_PART + 1024 * PART_STRIDE;
constexpr unsigned WS_END = WS_O + 64 * 64 * 1024;
constexpr unsigned SPIN_CAP = 1u << 18;

#define ST(i) do {} while (0)
#define STR(i) do {} while (0)
#define STFLUSH() do {} while (0)

DI void glds(unsigned voff, rsrc_t rs, unsigned soff, unsigned ldsaddr) {
  asm volatile("s_mov_b32 m0, %0\n\ts_nop 0\n\tbuffer_load_dwordx4 %1, %2, %3 offen lds"
               :: "s"(ldsaddr), "v"(voff), "s"(rs), "s"(soff) : "memory");
}
template <int AUX> DI void st128(u32x4 v, rsrc_t rs, unsigned off) { __builtin_amdgcn_raw_buffer_store_b128(v, rs, off, 0, AUX); }
#define WAITV(n) asm volatile("s_waitcnt vmcnt(" #n ")" ::: "memory")
#define WAITL0() asm volatile("s_waitcnt lgkmcnt(0)" ::: "memory")
#define BAR() asm volatile("s_barrier" ::: "memory")

DI f32x16 mfma(bf16x8 a, bf16x8 b, f32x16 c) { return __builtin_amdgcn_mfma_f32_32x32x16_bf16(a, b, c, 0, 0, 0); }
DI bf16x8 ldsr128(LAS char* L, unsigned a) { return *(LAS bf16x8*)(L + a); }
DI bf16x8 ldstr(LAS char* L, unsigned a0, unsigned a1) {
  bf16x4 lo = __builtin_amdgcn_ds_read_tr16_b64_v4bf16((LAS bf16x4*)(L + a0));
  bf16x4 hi = __builtin_amdgcn_ds_read_tr16_b64_v4bf16((LAS bf16x4*)(L + a1));
  return __builtin_shufflevector(lo, hi, 0, 1, 2, 3, 4, 5, 6, 7);
}
DI bf16x2 cvt2(float a, float b) { f32x2 p = {a, b}; return __builtin_convertvector(p, bf16x2); }
DI bf16x8 pack8(float a0, float a1, float a2, float a3, float a4, float a5, float a6, float a7) {
  bf16x2 p0 = cvt2(a0, a1), p1 = cvt2(a2, a3), p2 = cvt2(a4, a5), p3 = cvt2(a6, a7);
  bf16x4 lo = __builtin_shufflevector(p0, p1, 0, 1, 2, 3), hi = __builtin_shufflevector(p2, p3, 0, 1, 2, 3);
  return __builtin_shufflevector(lo, hi, 0, 1, 2, 3, 4, 5, 6, 7);
}
DI unsigned phi2(float a, float b) {
  const f32x2 t = {a, b};
  const f32x2 u = t * 1.44269504088896340736f;
  const f32x2 e = {__builtin_amdgcn_fmed3f(__builtin_amdgcn_exp2f(u[0]), 0.0f, 1.0f), __builtin_amdgcn_fmed3f(__builtin_amdgcn_exp2f(u[1]), 0.0f, 1.0f)};
  const int ia = __builtin_bit_cast(int, a), ib = __builtin_bit_cast(int, b);
  const f32x2 m = {__builtin_bit_cast(float, ia > 0 ? ia : 0), __builtin_bit_cast(float, ib > 0 ? ib : 0)};
  const f32x2 r = (m + e) + 1e-6f;
  return __builtin_bit_cast(unsigned, __builtin_convertvector(r, bf16x2));
}
DI bf16x8 phi8(const f32x16& x, int s) {
  const u32x4 v = {phi2(x[8 * s], x[8 * s + 1]), phi2(x[8 * s + 2], x[8 * s + 3]), phi2(x[8 * s + 4], x[8 * s + 5]), phi2(x[8 * s + 6], x[8 * s + 7])};
  return __builtin_bit_cast(bf16x8, v);
}
DI float bsel(unsigned m, float a, float b) {
  return __builtin_bit_cast(float, (__builtin_bit_cast(unsigned, a) & ~m) | (__builtin_bit_cast(unsigned, b) & m));
}
DI float fadd(float a, float b) { float r; asm("v_add_f32 %0, %1, %2" : "=v"(r) : "v"(a), "v"(b)); return r; }
DI f32x4 fadd4(f32x4 a, f32x4 b) { return f32x4{fadd(a[0], b[0]), fadd(a[1], b[1]), fadd(a[2], b[2]), fadd(a[3], b[3])}; }
DI float ffma(float a, float b, float c) { float r; asm("v_fma_f32 %0, %1, %2, %3" : "=v"(r) : "v"(a), "v"(b), "v"(c)); return r; }
template <int CTRL, int RM> DI float dpp_add(float v) {
  return fadd(v, __builtin_bit_cast(float, __builtin_amdgcn_update_dpp(0, __builtin_bit_cast(int, v), CTRL, RM, 0xf, true)));
}
DI float wave_sum63(float v) {
  v = dpp_add<0x111, 0xf>(v); v = dpp_add<0x112, 0xf>(v); v = dpp_add<0x114, 0xf>(v); v = dpp_add<0x118, 0xf>(v);
  v = dpp_add<0x142, 0xa>(v); v = dpp_add<0x143, 0xc>(v);
  return v;
}
constexpr unsigned SENT = 0xFFFFFFFFu;
DI unsigned nosent(unsigned v) { return v < SENT ? v : SENT - 1u; }
DI u32x4 nosent4(u32x4 v) { return u32x4{nosent(v[0]), nosent(v[1]), nosent(v[2]), nosent(v[3])}; }
DI unsigned umax3(unsigned a, unsigned b, unsigned c) { unsigned m = a > b ? a : b; return m > c ? m : c; }
#define SB() __builtin_amdgcn_sched_barrier(0)
DI bf16x8 cvt8(const f32x16& x, int s) {
  return pack8(x[8 * s], x[8 * s + 1], x[8 * s + 2], x[8 * s + 3], x[8 * s + 4], x[8 * s + 5], x[8 * s + 6], x[8 * s + 7]);
}
DI float sum8(bf16x8 v, float acc) {
  const bf16x2 ones = {(__bf16)1.0f, (__bf16)1.0f};
  acc = __builtin_amdgcn_fdot2_f32_bf16(__builtin_shufflevector(v, v, 0, 1), ones, acc, false);
  acc = __builtin_amdgcn_fdot2_f32_bf16(__builtin_shufflevector(v, v, 2, 3), ones, acc, false);
  acc = __builtin_amdgcn_fdot2_f32_bf16(__builtin_shufflevector(v, v, 4, 5), ones, acc, false);
  acc = __builtin_amdgcn_fdot2_f32_bf16(__builtin_shufflevector(v, v, 6, 7), ones, acc, false);
  return acc;
}
DI unsigned long long ld_flag(GAS char* ws, unsigned off) {
  return __hip_atomic_load((GAS unsigned long long*)(ws + off), __ATOMIC_RELAXED, __HIP_MEMORY_SCOPE_AGENT);
}
DI void st_flag(GAS char* ws, unsigned off, unsigned long long v) {
  __hip_atomic_store((GAS unsigned long long*)(ws + off), v, __ATOMIC_RELAXED, __HIP_MEMORY_SCOPE_AGENT);
}
DI unsigned long long epoch_issue(const char* wsp) {
  unsigned long long e;
  asm volatile("s_load_dwordx2 %0, %1, 0x0" : "=s"(e) : "s"(wsp));
  return e;
}
DI unsigned long long epoch_wait(unsigned long long e) {
  asm volatile("s_waitcnt lgkmcnt(0)" : "+s"(e) :: "memory");
  return e + 1ull;
}
DI void wait_flags(GAS char* ws, unsigned off, unsigned long long tag) {
  for (unsigned spins = 0; spins < SPIN_CAP; ++spins) {
    const unsigned long long v = ld_flag(ws, off);
    if (__all(v == tag)) break;
    __builtin_amdgcn_s_sleep(PSLEEP);
  }
  asm volatile("" ::: "memory");
}
DI unsigned ld_u32(GAS char* ws, unsigned off) {
  return __hip_atomic_load((GAS unsigned*)(ws + off), __ATOMIC_RELAXED, __HIP_MEMORY_SCOPE_AGENT);
}
DI void wait_written(GAS char* ws, unsigned off) {
  for (unsigned spins = 0; spins < SPIN_CAP; ++spins) {
    const unsigned v = ld_u32(ws, off);
    if (__all(v != SENT)) break;
    __builtin_amdgcn_s_sleep(PSLEEP);
  }
  asm volatile("" ::: "memory");
}

__global__ __launch_bounds__(512, 2) void gated_mix_fused(const u16* __restrict__ x, const u16* __restrict__ wA, const u16* __restrict__ wB,
                                                          const u16* __restrict__ woA, const u16* __restrict__ woB, const u16* __restrict__ gw,
                                                          u16* __restrict__ out, char* __restrict__ wsp
                                                          ) {
  extern __shared__ __attribute__((aligned(16))) char lds[];
  LAS char* L = (LAS char*)lds;
  const int tid = threadIdx.x;
  const int wave = __builtin_amdgcn_readfirstlane(tid >> 6), lane = tid & 63;
  const int b = blockIdx.x, xcd = b & 7, r = b >> 3;
  const int ti = (xcd & 1) * 8 + (r & 7), g = (xcd >> 1) * 4 + (r >> 3);
  const int gg = g & 7;
  const int rt = 4 * xcd + (r & 3), ct = r >> 2;
  GAS char* ws = (GAS char*)wsp;
  asm volatile("" :: "s"(x), "s"(wA), "s"(wB), "s"(woA), "s"(woB), "s"(gw), "s"(out), "s"(wsp));
  STR(0); ST(1);

  if (wave >= 4) {
    const int hw = wave - 4;
    const unsigned long long epoch_raw = epoch_issue(wsp);
    __builtin_amdgcn_s_setprio(HPRIO);
    int ro = 0;
    if (ROT == 1) ro = (ti + 2 * g) & 7;
    if (ROT == 2) ro = 2 * ((ti + g) & 1);
    if (ROT == 3) ro = 4 * ((ti + g) & 1);
    if (ROT == 5) ro = ((ti + g) % 3) * 2;
    const u16* w = (g >= 8) ? wB : wA;
    const rsrc_t rsX = __builtin_amdgcn_make_buffer_rsrc((void*)x, 0, S_ * D_ * 2, 0x00020000);
    const rsrc_t rsW = __builtin_amdgcn_make_buffer_rsrc((void*)w, 0, D_ * 3 * D_ * 2, 0x00020000);
    const int rin = lane >> 3, ch = lane & 7;
    const unsigned vx = rin * 1024 + ((ch ^ (4 * (hw & 1) + (rin >> 1))) << 4);
    const unsigned csw = (ch ^ (((rin >> 1) & 1) << 2)) << 4;
    const unsigned vw = rin * 3072 + csw;
    const unsigned sx0 = (ti * 128 + 8 * hw) * 1024;
    const unsigned sw0 = 8 * hw * 3072 + gg * 128;
    auto stage = [&](int s) {
      const unsigned ks = (s + ro) & 7, slot = (s % 3) * 16384;
#pragma unroll
      for (int i = 0; i < 4; ++i) glds(vx, rsX, sx0 + ks * 128 + i * 32768, XR + slot + (hw + 4 * i) * 1024);
#pragma unroll
      for (int i = 0; i < 2; ++i) glds(vw, rsW, sw0 + ks * 196608 + i * 98304 + 1024, WR + slot + (hw + 4 * i) * 1024);
#pragma unroll
      for (int i = 0; i < 2; ++i) glds(vw, rsW, sw0 + ks * 196608 + i * 98304 + 2048, WR + slot + 8192 + (hw + 4 * i) * 1024);
    };
    auto stageq = [&](int s) {
      const unsigned ks = (s + ro) & 7;
#pragma unroll
      for (int i = 0; i < 2; ++i) glds(vw, rsW, sw0 + ks * 196608 + i * 98304, WQ + s * 8192 + (hw + 4 * i) * 1024);
    };
    ST(15);
    stage(0);
    stage(1);
    ST(2);
    WAITV(8);
    const unsigned long long tag = epoch_wait(epoch_raw);
    BAR();
    ST(3);
#pragma unroll
    for (int s = 1; s < 8; ++s) {
      if (s + 1 < 8) { stage(s + 1); WAITV(8); }
      else { stageq(0); stageq(1); stageq(2); stageq(3); WAITV(8); }
      BAR();
    }
    ST(4);
    stageq(4); stageq(5); stageq(6); stageq(7);
    const rsrc_t rsWS = __builtin_amdgcn_make_buffer_rsrc((void*)wsp, 0, WS_END, 0x00020000);
    const rsrc_t rsOA = __builtin_amdgcn_make_buffer_rsrc((void*)woA, 0, D_ * D_ * 2, 0x00020000);
    const rsrc_t rsOB = __builtin_amdgcn_make_buffer_rsrc((void*)woB, 0, D_ * D_ * 2, 0x00020000);
    const unsigned vo = rin * 1024 + csw;
    const unsigned hid = g * 4 + hw;
    u32x4 wob[16];
    const rsrc_t rsG = __builtin_amdgcn_make_buffer_rsrc((void*)gw, 0, D_ * 2, 0x00020000);
    const unsigned grow = ti * 128 + 8 * g + 2 * hw;
    const u32x4 x0 = __builtin_amdgcn_raw_buffer_load_b128(rsX, lane * 16, grow * 1024, 0);
    const u32x4 x1 = __builtin_amdgcn_raw_buffer_load_b128(rsX, lane * 16 + 1024, grow * 1024, 0);
    const u32x4 gv = __builtin_amdgcn_raw_buffer_load_b128(rsG, lane * 16, 0, 0);
    const unsigned foff = WS_FLAGC + (g * 4 + hw) * 128 + (lane & 15) * 8;
    const unsigned long long fc = ld_flag(ws, foff);
    asm volatile("" ::: "memory");
#pragma unroll
    for (int i = 0; i < 16; ++i) {
      wob[i] = __builtin_amdgcn_raw_buffer_load_b128(rsOB, vo, (hw + 4 * i) * 8192 + ct * 128, 0);
    }
    WAITV(20);
    ST(5);
    BAR();
    ST(6);
    {
      const f32x4 p0 = *(LAS f32x4*)(L + SCR + hw * 1024 + lane * 16), p1 = *(LAS f32x4*)(L + SCR + (4 + hw) * 1024 + lane * 16);
      const f32x4 z0 = *(LAS f32x4*)(L + SCR + (8 + hw) * 1024 + (lane & 3) * 16), z1 = *(LAS f32x4*)(L + SCR + (8 + hw) * 1024 + 64 + (lane & 3) * 16);
      const unsigned po = WS_PART + (hid * 16 + ti) * PART_STRIDE;
      st128<16>(nosent4(__builtin_bit_cast(u32x4, fadd4(p0, p1))), rsWS, (lane * 16) + (po));
      if (lane < 4) st128<16>(nosent4(__builtin_bit_cast(u32x4, fadd4(z0, z1))), rsWS, (1024 + lane * 16) + (po));
    }
    ST(7);
#pragma unroll
    for (int i = 0; i < 16; ++i) glds(vo, rsOA, (hw + 4 * i) * 8192 + ct * 128, WOA + (hw + 4 * i) * 1024);
    ST(8);
    {
      float a0 = 0.f, a1 = 0.f;
#pragma unroll
      for (int i = 0; i < 4; ++i) {
        const unsigned ug = gv[i], u0 = x0[i], u1 = x1[i];
        const float gl = __builtin_bit_cast(float, ug << 16), gh = __builtin_bit_cast(float, ug & 0xffff0000u);
        a0 = ffma(__builtin_bit_cast(float, u0 << 16), gl, a0); a0 = ffma(__builtin_bit_cast(float, u0 & 0xffff0000u), gh, a0);
        a1 = ffma(__builtin_bit_cast(float, u1 << 16), gl, a1); a1 = ffma(__builtin_bit_cast(float, u1 & 0xffff0000u), gh, a1);
      }
      a0 = wave_sum63(a0); a1 = wave_sum63(a1);
      const float s0 = __builtin_bit_cast(float, __builtin_amdgcn_readlane(__builtin_bit_cast(int, a0), 63));
      const float s1 = __builtin_bit_cast(float, __builtin_amdgcn_readlane(__builtin_bit_cast(int, a1), 63));
      const float lg = (lane & 1) ? s1 : s0;
      const float gval = __builtin_amdgcn_rcpf(1.0f + __builtin_amdgcn_exp2f(-1.44269504088896340736f * lg));
      if (lane < 2) __hip_atomic_store((GAS unsigned*)(ws + WS_GATE) + grow + lane, __builtin_bit_cast(unsigned, gval), __ATOMIC_RELAXED, __HIP_MEMORY_SCOPE_AGENT);
    }
    if (!__all(fc == tag)) wait_flags(ws, foff, tag);
    const unsigned pb = WS_PART + hid * 16 * PART_STRIDE;
    wait_written(ws, pb + (lane & 15) * PART_STRIDE + 1024 + 60);
    ST(9);
    {
      f32x4 a, z;
      for (unsigned spins = 0; spins < SPIN_CAP; ++spins) {
        u32x4 pv[16];
#pragma unroll
        for (int p = 0; p < 16; ++p) pv[p] = __builtin_amdgcn_raw_buffer_load_b128(rsWS, lane * 16, pb + p * PART_STRIDE, 16);
        const u32x4 zv = __builtin_amdgcn_raw_buffer_load_b128(rsWS, (lane >> 2) * PART_STRIDE + 1024 + (lane & 3) * 16, pb, 16);
        unsigned mx = umax3(zv[0], zv[1], umax3(zv[2], zv[3], 0u));
        a = __builtin_bit_cast(f32x4, pv[0]);
        mx = umax3(pv[0][0], pv[0][1], umax3(pv[0][2], pv[0][3], mx));
#pragma unroll
        for (int p = 1; p < 16; ++p) {
          a += __builtin_bit_cast(f32x4, pv[p]);
          mx = umax3(pv[p][0], pv[p][1], umax3(pv[p][2], pv[p][3], mx));
        }
        z = __builtin_bit_cast(f32x4, zv);
        if (__all(mx != SENT)) break;
      }
#pragma unroll
      for (int m = 4; m <= 32; m <<= 1) {
        z[0] += __shfl_xor(z[0], m); z[1] += __shfl_xor(z[1], m); z[2] += __shfl_xor(z[2], m); z[3] += __shfl_xor(z[3], m);
      }
      const int slot = lane & 31, jh = lane >> 5;
      u32x2 ka = {__builtin_bit_cast(unsigned, cvt2(a[0], a[1])), __builtin_bit_cast(unsigned, cvt2(a[2], a[3]))};
      *(LAS u32x2*)(L + AFR + hw * 1024 + ((slot >> 4) * 32 + (slot & 15)) * 16 + jh * 8) = ka;
      const int q4 = lane & 3;
      u32x2 kz = {__builtin_bit_cast(unsigned, cvt2(z[0], z[1])), __builtin_bit_cast(unsigned, cvt2(z[2], z[3]))};
      *(LAS u32x2*)(L + AFR + hw * 1024 + ((q4 & 1) * 32 + 16 + (lane >> 2)) * 16 + (q4 >> 1) * 8) = kz;
    }
    WAITL0();
    ST(10);
    BAR();
    ST(11);
#pragma unroll
    for (int i = 0; i < 16; ++i) *(LAS u32x4*)(L + WOB + (hw + 4 * i) * 1024 + lane * 16) = wob[i];
    WAITV(0); WAITL0();
    ST(12);
    BAR();
    {
      const int r32 = lane & 31, h = lane >> 5, g1 = (lane >> 4) & 1, q = (lane & 15) >> 2, p = lane & 3;
      const unsigned sw = ((2 * g1 + (p >> 1)) ^ (((q >> 1) & 1) << 2)) << 4;
      const int br2 = hw >> 1, sh = hw & 1;
      const unsigned st2 = 2 * rt + sh;
      const unsigned ob = WS_O + (st2 * 64 + 32 * br2) * 1024;
      wait_flags(ws, WS_FLAGB + st2 * 128 + (lane & 15) * 8, tag);
      ST(13);
      unsigned wp0 = ((br2 ? WOB : WOA) + (4 * h + q) * 128 + sw + (p & 1) * 8) ^ (br2 << 6), wp1 = wp0 ^ 64;
      asm volatile("" : "+v"(wp0), "+v"(wp1));
      f32x16 c0 = {}, c1 = {};
      u32x4 of[32];
#pragma unroll
      for (int j = 0; j < OD; ++j) of[j] = __builtin_amdgcn_raw_buffer_load_b128(rsWS, lane * 16, ob + j * 1024, 16);
      const unsigned gvb = ld_u32(ws, WS_GATE + (st2 * 32 + r32) * 4);
      {
        bf16x8 a0[3], a1[3];
        a0[0] = ldstr(L, wp0, wp0 + 1024); a1[0] = ldstr(L, wp1, wp1 + 1024);
        a0[1] = ldstr(L, wp0 + 2048, wp0 + 2048 + 1024); a1[1] = ldstr(L, wp1 + 2048, wp1 + 2048 + 1024);
#pragma unroll
        for (int j = 0; j < 32; ++j) {
          const unsigned o = (j + 2) * 2048;
          const bf16x8 bf = __builtin_bit_cast(bf16x8, of[j]);
          SB();
          c0 = mfma(a0[j % 3], bf, c0);
          SB();
          if (j + OD < 32) of[j + OD] = __builtin_amdgcn_raw_buffer_load_b128(rsWS, lane * 16, ob + (j + OD) * 1024, 16);
          if (j + 2 < 32) a0[(j + 2) % 3] = ldstr(L, wp0 + o, wp0 + o + 1024);
          SB();
          c1 = mfma(a1[j % 3], bf, c1);
          SB();
          if (j + 2 < 32) a1[(j + 2) % 3] = ldstr(L, wp1 + o, wp1 + o + 1024);
        }
        SB();
      }
      ST(14);
      __builtin_amdgcn_s_setprio(0);
      const float gv = __builtin_bit_cast(float, gvb);
      const float gs = br2 ? 1.0f - gv : gv;
  #pragma unroll
      for (int qq = 0; qq < 4; ++qq) {
        const f32x4 v = {c1[4 * qq] * gs, c1[4 * qq + 1] * gs, c1[4 * qq + 2] * gs, c1[4 * qq + 3] * gs};
        *(LAS f32x4*)(L + EXC + hw * 4096 + qq * 1024 + lane * 16) = v;
      }
      WAITL0();
      BAR();
      if (b == 0 && hw == 0 && lane == 0) st_flag(ws, WS_EPOCH, tag);
      unsigned pk[8];
  #pragma unroll
      for (int qq = 0; qq < 4; ++qq) {
        const f32x4 v = *(LAS f32x4*)(L + EXC + (hw ^ 2) * 4096 + qq * 1024 + lane * 16);
        pk[2 * qq] = __builtin_bit_cast(unsigned, cvt2(c0[4 * qq] * gs + v[0], c0[4 * qq + 1] * gs + v[1]));
        pk[2 * qq + 1] = __builtin_bit_cast(unsigned, cvt2(c0[4 * qq + 2] * gs + v[2], c0[4 * qq + 3] * gs + v[3]));
      }
      const rsrc_t rsO = __builtin_amdgcn_make_buffer_rsrc((void*)out, 0, S_ * D_ * 2, 0x00020000);
      const unsigned orow = (st2 * 32 + r32) * 1024 + (64 * ct + 32 * br2) * 2 + h * 16;
  #pragma unroll
      for (int qq = 0; qq < 4; qq += 2) {
        const auto s0 = __builtin_amdgcn_permlane32_swap(pk[2 * qq], pk[2 * qq + 2], false, false);
        const auto s1 = __builtin_amdgcn_permlane32_swap(pk[2 * qq + 1], pk[2 * qq + 3], false, false);
        const u32x4 v = {s0[0], s1[0], s0[1], s1[1]};
        st128<0>(v, rsO, orow + qq * 16);
      }
      STR(16); STFLUSH();
    }
    return;
  }

  const unsigned long long epoch_raw = epoch_issue(wsp);
  const int wr = wave >> 1, wc = wave & 1;
  const int r32 = lane & 31, h = lane >> 5;
  const int g1 = (lane >> 4) & 1, q = (lane & 15) >> 2, p = lane & 3;
  const int own = wave, oth = wave ^ 1;
  const rsrc_t rsWS = __builtin_amdgcn_make_buffer_rsrc((void*)wsp, 0, WS_END, 0x00020000);
  unsigned ctouch = 0;
  {
    const u32x4 sent = {SENT, SENT, SENT, SENT};
    const unsigned po = WS_PART + ((g * 4 + wave) * 16 + ti) * PART_STRIDE;
    st128<16>(sent, rsWS, lane * 16 + po);
    if (lane < 4) st128<16>(sent, rsWS, 1024 + lane * 16 + po);
  }
  const unsigned fx = (h ^ ((r32 >> 1) & 7)) << 4;
  unsigned xo0 = XR + (own * 32 + r32) * 128 + fx, xt0 = XR + (oth * 32 + r32) * 128 + fx;
  const unsigned sw = ((2 * g1 + (p >> 1)) ^ (((q >> 1) & 1) << 2)) << 4;
  const unsigned wn0 = (8 * h + q) * 128 + sw + (p & 1) * 8;
  unsigned wkv0 = WR + (wn0 ^ (wc << 6));
  asm volatile("" : "+v"(wkv0));
  unsigned xoa[4], xta[4];
#pragma unroll
  for (int k = 0; k < 4; ++k) { xoa[k] = xo0 ^ (k << 5); xta[k] = xt0 ^ (k << 5); asm volatile("" : "+v"(xoa[k]), "+v"(xta[k])); }
  const unsigned long long tag = epoch_wait(epoch_raw);

  bf16x8 xr[32];
  f32x16 kO = {}, kT = {}, vO = {}, vT = {};
  {
    BAR();
    ST(3);
    bf16x8 xt = ldsr128(L, xta[0]), wk = ldstr(L, wkv0, wkv0 + 512), wv = ldstr(L, wkv0 + 8192, wkv0 + 8192 + 512);
    xr[0] = ldsr128(L, xoa[0]);
#pragma unroll
    for (int t = 0; t < 32; ++t) {
      bf16x8 nxt, nwk, nwv;
      const int s1 = (t + 1) >> 2, k1 = (t + 1) & 3;
      const unsigned slot = (s1 % 3) * 16384;
      if (t + 1 < 32 && k1 == 0) {
        WAITL0();
        if (s1 == CLR_STAGE) { WAITV(0); asm volatile("" :: "v"(ctouch));
          if (lane == 0) st_flag(ws, WS_FLAGC + (g * 4 + wave) * 128 + ti * 8, tag); }
        BAR();
      }
      SB();
      kO = mfma(xr[t], wk, kO);
      SB();
      if (t + 1 < 32) {
        xr[t + 1] = ldsr128(L, xoa[k1] + slot);
        nwk = ldstr(L, wkv0 + slot + k1 * 2048, wkv0 + slot + k1 * 2048 + 512);
      }
      SB();
      kT = mfma(xt, wk, kT);
      SB();
      if (t + 1 < 32) {
        nxt = ldsr128(L, xta[k1] + slot);
        nwv = ldstr(L, wkv0 + slot + 8192 + k1 * 2048, wkv0 + slot + 8192 + k1 * 2048 + 512);
      }
      SB();
      vO = mfma(xr[t], wv, vO);
      vT = mfma(xt, wv, vT);
      SB();
      if (t + 1 < 32) { xt = nxt; wk = nwk; wv = nwv; }
    }
  }
  ST(4);
  {
    f32x16 kv = {};
    float zs = 0.f;
#pragma unroll
    for (int s = 0; s < 2; ++s) {
      const bf16x8 va = cvt8(vO, s), vb = cvt8(vT, s);
      const bf16x8 pa = phi8(kO, s);
      kv = mfma(pa, va, kv);
      const bf16x8 pb = phi8(kT, s);
      kv = mfma(pb, vb, kv);
      zs = sum8(pa, zs); zs = sum8(pb, zs);
    }
    {
      const unsigned zb = __builtin_bit_cast(unsigned, zs);
      const auto sw2 = __builtin_amdgcn_permlane32_swap(zb, zb, false, false);
      zs = __builtin_bit_cast(float, (unsigned)sw2[0]) + __builtin_bit_cast(float, (unsigned)sw2[1]);
    }
    const unsigned gm = g1 ? 0xFFFFFFFFu : 0u;
    f32x4 lo, hi;
#pragma unroll
    for (int j = 0; j < 4; ++j) { lo[j] = bsel(gm, kv[j], kv[8 + j]); hi[j] = bsel(gm, kv[4 + j], kv[12 + j]); }
    const int hh = 2 * wc + g1;
    const unsigned a = SCR + (4 * wr + hh) * 1024 + (h * 16 + (lane & 15)) * 16;
    *(LAS f32x4*)(L + a) = lo;
    *(LAS f32x4*)(L + a + 512) = hi;
    if (h == 0) *(LAS float*)(L + SCR + (8 + hh) * 1024 + wr * 64 + (lane & 15) * 4) = zs;
  }
  WAITL0();
  ST(5);
  BAR();
  ST(6);
  f32x16 qa = {}, qb = {};
  {
    unsigned wq0 = WQ + wn0, wq1 = WQ + (wn0 ^ 64);
    asm volatile("" : "+v"(wq0), "+v"(wq1));
    bf16x8 a0[3], a1[3];
    a0[0] = ldstr(L, wq0, wq0 + 512); a1[0] = ldstr(L, wq1, wq1 + 512);
    a0[1] = ldstr(L, wq0 + 2048, wq0 + 2048 + 512); a1[1] = ldstr(L, wq1 + 2048, wq1 + 2048 + 512);
#pragma unroll
    for (int t = 0; t < 32; ++t) {
      const unsigned o = (t + 2) * 2048;
      SB();
      qa = mfma(a0[t % 3], xr[t], qa);
      SB();
      if (t + 2 < 32) a0[(t + 2) % 3] = ldstr(L, wq0 + o, wq0 + o + 512);
      SB();
      qb = mfma(a1[t % 3], xr[t], qb);
      SB();
      if (t + 2 < 32) a1[(t + 2) % 3] = ldstr(L, wq1 + o, wq1 + o + 512);
    }
    SB();
  }
  ST(7);
  bf16x8 pq[4];
  pq[0] = phi8(qa, 0); pq[1] = phi8(qa, 1); pq[2] = phi8(qb, 0); pq[3] = phi8(qb, 1);
  WAITL0();
  ST(8);
  BAR();
  ST(9);
  {
    const unsigned st = 4 * ti + own;
    bf16x8 af[4];
#pragma unroll
    for (int hh = 0; hh < 4; ++hh) af[hh] = ldsr128(L, AFR + hh * 1024 + lane * 16);
    f32x16 d[4];
#pragma unroll
    for (int hh = 0; hh < 4; ++hh) { d[hh] = f32x16{}; d[hh] = mfma(af[hh], pq[hh], d[hh]); }
#pragma unroll
    for (int hh = 0; hh < 4; ++hh) {
      const float inv = __builtin_amdgcn_rcpf(d[hh][8] + 1e-6f);
      const bf16x8 o = pack8(d[hh][0] * inv, d[hh][1] * inv, d[hh][2] * inv, d[hh][3] * inv, d[hh][4] * inv, d[hh][5] * inv, d[hh][6] * inv, d[hh][7] * inv);
      st128<16>(__builtin_bit_cast(u32x4, o), rsWS, (lane * 16) + (WS_O + (st * 64 + 4 * g + hh) * 1024));
    }
  }
  ST(10);
  BAR();
  WAITV(0);
  if (lane == 0) st_flag(ws, WS_FLAGB + (4 * ti + own) * 128 + g * 8, tag);
  ST(11);
  STR(16); STFLUSH();
}

extern "C" void kernel_launch(void* const* d_in, const int* in_sizes, int n_in, void* d_out, int out_size, void* d_ws, size_t ws_size,
                              hipStream_t stream) {
  (void)in_sizes; (void)n_in; (void)out_size;
  const u16* x = (const u16*)d_in[0];
  const u16* wA = (const u16*)d_in[1];
  const u16* woA = (const u16*)d_in[2];
  const u16* wB = (const u16*)d_in[4];
  const u16* woB = (const u16*)d_in[5];
  const u16* gw = (const u16*)d_in[6];
  (void)hipFuncSetAttribute((const void*)gated_mix_fused, hipFuncAttributeMaxDynamicSharedMemorySize, LDS_BYTES);
  gated_mix_fused<<<256, 512, LDS_BYTES, stream>>>(x, wA, wB, woA, woB, gw, (u16*)d_out, (char*)d_ws + ((ws_size / 2) & ~(size_t)0x1FFFFF));
}
```

```cpp
#include <hip/hip_runtime.h>
#include <hip/hip_bf16.h>
#include <stdint.h>

#define LAS __attribute__((address_space(3)))
#define GAS __attribute__((address_space(1)))
#define DI __device__ __forceinline__

typedef unsigned short u16;
typedef __bf16 bf16x8 __attribute__((ext_vector_type(8)));
typedef __bf16 bf16x4 __attribute__((ext_vector_type(4)));
typedef __bf16 bf16x2 __attribute__((ext_vector_type(2)));
typedef float f32x16 __attribute__((ext_vector_type(16)));
typedef float f32x4 __attribute__((ext_vector_type(4)));
typedef float f32x2 __attribute__((ext_vector_type(2)));
typedef unsigned u32x4 __attribute__((ext_vector_type(4)));
typedef unsigned u32x2 __attribute__((ext_vector_type(2)));
typedef __amdgpu_buffer_rsrc_t rsrc_t;

#define ROT 1
#define CODE_PREFETCH 21504
#define CLR_STAGE 6
#define HPRIO 3
#define OD 10
#define TOUCH 0
#define PSLEEP 4
#define OTOUCH 0
#define WTOUCH 0
#define PDELAY 5
#define PDELAY1 0
#define CODE_BACK 0

constexpr int S_ = 2048, D_ = 512;
constexpr unsigned XR = 0;
constexpr unsigned WR = 49152;
constexpr unsigned WQ = 98304;
constexpr unsigned WOA = 0;
constexpr unsigned WOB = 98304;
constexpr unsigned SCR = 81920;
constexpr unsigned AFR = SCR + 12288;
constexpr unsigned EXC = SCR;
constexpr unsigned LDS_BYTES = 163840;
constexpr unsigned WS_EPOCH = 0;
constexpr unsigned WS_FLAGC = 4096;
constexpr unsigned WS_FLAGB = WS_FLAGC + 1024 * 128;
constexpr unsigned WS_GATE = WS_FLAGB + 1024 * 128;
constexpr unsigned WS_PART = WS_GATE + 8192;
constexpr unsigned PART_STRIDE = 1152;
constexpr unsigned WS_O = WS_PART + 1024 * PART_STRIDE;
constexpr unsigned WS_END = WS_O + 64 * 64 * 1024;
constexpr unsigned SPIN_CAP = 1u << 18;

#define ST(i) do {} while (0)
#define STR(i) do {} while (0)
#define STFLUSH() do {} while (0)

DI void glds(unsigned voff, rsrc_t rs, unsigned soff, unsigned ldsaddr) {
  asm volatile("s_mov_b32 m0, %0\n\ts_nop 0\n\tbuffer_load_dwordx4 %1, %2, %3 offen lds"
               :: "s"(ldsaddr), "v"(voff), "s"(rs), "s"(soff) : "memory");
}
template <int AUX> DI void st128(u32x4 v, rsrc_t rs, unsigned off) { __builtin_amdgcn_raw_buffer_store_b128(v, rs, off, 0, AUX); }
#define WAITV(n) asm volatile("s_waitcnt vmcnt(" #n ")" ::: "memory")
#define WAITL0() asm volatile("s_waitcnt lgkmcnt(0)" ::: "memory")
#define BAR() asm volatile("s_barrier" ::: "memory")

DI f32x16 mfma(bf16x8 a, bf16x8 b, f32x16 c) { return __builtin_amdgcn_mfma_f32_32x32x16_bf16(a, b, c, 0, 0, 0); }
DI bf16x8 ldsr128(LAS char* L, unsigned a) { return *(LAS bf16x8*)(L + a); }
DI bf16x8 ldstr(LAS char* L, unsigned a0, unsigned a1) {
  bf16x4 lo = __builtin_amdgcn_ds_read_tr16_b64_v4bf16((LAS bf16x4*)(L + a0));
  bf16x4 hi = __builtin_amdgcn_ds_read_tr16_b64_v4bf16((LAS bf16x4*)(L + a1));
  return __builtin_shufflevector(lo, hi, 0, 1, 2, 3, 4, 5, 6, 7);
}
DI bf16x2 cvt2(float a, float b) { f32x2 p = {a, b}; return __builtin_convertvector(p, bf16x2); }
DI bf16x8 pack8(float a0, float a1, float a2, float a3, float a4, float a5, float a6, float a7) {
  bf16x2 p0 = cvt2(a0, a1), p1 = cvt2(a2, a3), p2 = cvt2(a4, a5), p3 = cvt2(a6, a7);
  bf16x4 lo = __builtin_shufflevector(p0, p1, 0, 1, 2, 3), hi = __builtin_shufflevector(p2, p3, 0, 1, 2, 3);
  return __builtin_shufflevector(lo, hi, 0, 1, 2, 3, 4, 5, 6, 7);
}
DI unsigned phi2(float a, float b) {
  const f32x2 t = {a, b};
  const f32x2 u = t * 1.44269504088896340736f;
  const f32x2 e = {__builtin_amdgcn_fmed3f(__builtin_amdgcn_exp2f(u[0]), 0.0f, 1.0f), __builtin_amdgcn_fmed3f(__builtin_amdgcn_exp2f(u[1]), 0.0f, 1.0f)};
  const int ia = __builtin_bit_cast(int, a), ib = __builtin_bit_cast(int, b);
  const f32x2 m = {__builtin_bit_cast(float, ia > 0 ? ia : 0), __builtin_bit_cast(float, ib > 0 ? ib : 0)};
  const f32x2 r = (m + e) + 1e-6f;
  return __builtin_bit_cast(unsigned, __builtin_convertvector(r, bf16x2));
}
DI bf16x8 phi8(const f32x16& x, int s) {
  const u32x4 v = {phi2(x[8 * s], x[8 * s + 1]), phi2(x[8 * s + 2], x[8 * s + 3]), phi2(x[8 * s + 4], x[8 * s + 5]), phi2(x[8 * s + 6], x[8 * s + 7])};
  return __builtin_bit_cast(bf16x8, v);
}
DI float bsel(unsigned m, float a, float b) {
  return __builtin_bit_cast(float, (__builtin_bit_cast(unsigned, a) & ~m) | (__builtin_bit_cast(unsigned, b) & m));
}
DI float fadd(float a, float b) { float r; asm("v_add_f32 %0, %1, %2" : "=v"(r) : "v"(a), "v"(b)); return r; }
DI f32x4 fadd4(f32x4 a, f32x4 b) { return f32x4{fadd(a[0], b[0]), fadd(a[1], b[1]), fadd(a[2], b[2]), fadd(a[3], b[3])}; }
DI float ffma(float a, float b, float c) { float r; asm("v_fma_f32 %0, %1, %2, %3" : "=v"(r) : "v"(a), "v"(b), "v"(c)); return r; }
template <int CTRL, int RM> DI float dpp_add(float v) {
  return fadd(v, __builtin_bit_cast(float, __builtin_amdgcn_update_dpp(0, __builtin_bit_cast(int, v), CTRL, RM, 0xf, true)));
}
DI float wave_sum63(float v) {
  v = dpp_add<0x111, 0xf>(v); v = dpp_add<0x112, 0xf>(v); v = dpp_add<0x114, 0xf>(v); v = dpp_add<0x118, 0xf>(v);
  v = dpp_add<0x142, 0xa>(v); v = dpp_add<0x143, 0xc>(v);
  return v;
}
constexpr unsigned SENT = 0xFFFFFFFFu;
DI unsigned nosent(unsigned v) { return v < SENT ? v : SENT - 1u; }
DI u32x4 nosent4(u32x4 v) { return u32x4{nosent(v[0]), nosent(v[1]), nosent(v[2]), nosent(v[3])}; }
DI unsigned umax3(unsigned a, unsigned b, unsigned c) { unsigned m = a > b ? a : b; return m > c ? m : c; }
#define SB() __builtin_amdgcn_sched_barrier(0)
DI bf16x8 cvt8(const f32x16& x, int s) {
  return pack8(x[8 * s], x[8 * s + 1], x[8 * s + 2], x[8 * s + 3], x[8 * s + 4], x[8 * s + 5], x[8 * s + 6], x[8 * s + 7]);
}
DI float sum8(bf16x8 v, float acc) {
  const bf16x2 ones = {(__bf16)1.0f, (__bf16)1.0f};
  acc = __builtin_amdgcn_fdot2_f32_bf16(__builtin_shufflevector(v, v, 0, 1), ones, acc, false);
  acc = __builtin_amdgcn_fdot2_f32_bf16(__builtin_shufflevector(v, v, 2, 3), ones, acc, false);
  acc = __builtin_amdgcn_fdot2_f32_bf16(__builtin_shufflevector(v, v, 4, 5), ones, acc, false);
  acc = __builtin_amdgcn_fdot2_f32_bf16(__builtin_shufflevector(v, v, 6, 7), ones, acc, false);
  return acc;
}
DI unsigned long long ld_flag(GAS char* ws, unsigned off) {
  return __hip_atomic_load((GAS unsigned long long*)(ws + off), __ATOMIC_RELAXED, __HIP_MEMORY_SCOPE_AGENT);
}
DI void st_flag(GAS char* ws, unsigned off, unsigned long long v) {
  __hip_atomic_store((GAS unsigned long long*)(ws + off), v, __ATOMIC_RELAXED, __HIP_MEMORY_SCOPE_AGENT);
}
DI unsigned long long epoch_issue(const char* wsp) {
  unsigned long long e;
  asm volatile("s_load_dwordx2 %0, %1, 0x0" : "=s"(e) : "s"(wsp));
  return e;
}
DI unsigned long long epoch_wait(unsigned long long e) {
  asm volatile("s_waitcnt lgkmcnt(0)" : "+s"(e) :: "memory");
  return e + 1ull;
}
DI void wait_flags(GAS char* ws, unsigned off, unsigned long long tag) {
  for (unsigned spins = 0; spins < SPIN_CAP; ++spins) {
    const unsigned long long v = ld_flag(ws, off);
    if (__all(v == tag)) break;
    __builtin_amdgcn_s_sleep(PSLEEP);
  }
  asm volatile("" ::: "memory");
}
DI unsigned ld_u32(GAS char* ws, unsigned off) {
  return __hip_atomic_load((GAS unsigned*)(ws + off), __ATOMIC_RELAXED, __HIP_MEMORY_SCOPE_AGENT);
}
DI void wait_written(GAS char* ws, unsigned off) {
  for (unsigned spins = 0; spins < SPIN_CAP; ++spins) {
    const unsigned v = ld_u32(ws, off);
    if (__all(v != SENT)) break;
    __builtin_amdgcn_s_sleep(PSLEEP);
  }
  asm volatile("" ::: "memory");
}

__global__ __launch_bounds__(512, 2) void gated_mix_fused(const u16* __restrict__ x, const u16* __restrict__ wA, const u16* __restrict__ wB,
                                                          const u16* __restrict__ woA, const u16* __restrict__ woB, const u16* __restrict__ gw,
                                                          u16* __restrict__ out, char* __restrict__ wsp
                                                          ) {
  extern __shared__ __attribute__((aligned(16))) char lds[];
  LAS char* L = (LAS char*)lds;
  const int tid = threadIdx.x;
  const int wave = __builtin_amdgcn_readfirstlane(tid >> 6), lane = tid & 63;
  const int b = blockIdx.x, xcd = b & 7, r = b >> 3;
  const int ti = (xcd & 1) * 8 + (r & 7), g = (xcd >> 1) * 4 + (r >> 3);
  const int gg = g & 7;
  const int rt = 4 * xcd + (r & 3), ct = r >> 2;
  GAS char* ws = (GAS char*)wsp;
  asm volatile("" :: "s"(x), "s"(wA), "s"(wB), "s"(woA), "s"(woB), "s"(gw), "s"(out), "s"(wsp));
  STR(0); ST(1);
  unsigned long long pc;
  asm volatile("s_getpc_b64 %0" : "=s"(pc));
  pc &= ~255ull;

  if (wave >= 4) {
    const int hw = wave - 4;
    const unsigned long long epoch_raw = epoch_issue(wsp);
    __builtin_amdgcn_s_setprio(HPRIO);
    int ro = 0;
    if (ROT == 1) ro = (ti + 2 * g) & 7;
    if (ROT == 2) ro = 2 * ((ti + g) & 1);
    if (ROT == 3) ro = 4 * ((ti + g) & 1);
    if (ROT == 5) ro = ((ti + g) % 3) * 2;
    const u16* w = (g >= 8) ? wB : wA;
    const rsrc_t rsX = __builtin_amdgcn_make_buffer_rsrc((void*)x, 0, S_ * D_ * 2, 0x00020000);
    const rsrc_t rsW = __builtin_amdgcn_make_buffer_rsrc((void*)w, 0, D_ * 3 * D_ * 2, 0x00020000);
    const int rin = lane >> 3, ch = lane & 7;
    const unsigned vx = rin * 1024 + ((ch ^ (4 * (hw & 1) + (rin >> 1))) << 4);
    const unsigned csw = (ch ^ (((rin >> 1) & 1) << 2)) << 4;
    const unsigned vw = rin * 3072 + csw;
    const unsigned sx0 = (ti * 128 + 8 * hw) * 1024;
    const unsigned sw0 = 8 * hw * 3072 + gg * 128;
    auto stage = [&](int s) {
      const unsigned ks = (s + ro) & 7, slot = (s % 3) * 16384;
#pragma unroll
      for (int i = 0; i < 4; ++i) glds(vx, rsX, sx0 + ks * 128 + i * 32768, XR + slot + (hw + 4 * i) * 1024);
#pragma unroll
      for (int i = 0; i < 2; ++i) glds(vw, rsW, sw0 + ks * 196608 + i * 98304 + 1024, WR + slot + (hw + 4 * i) * 1024);
#pragma unroll
      for (int i = 0; i < 2; ++i) glds(vw, rsW, sw0 + ks * 196608 + i * 98304 + 2048, WR + slot + 8192 + (hw + 4 * i) * 1024);
    };
    auto stageq = [&](int s) {
      const unsigned ks = (s + ro) & 7;
#pragma unroll
      for (int i = 0; i < 2; ++i) glds(vw, rsW, sw0 + ks * 196608 + i * 98304, WQ + s * 8192 + (hw + 4 * i) * 1024);
    };
    ST(15);
    stage(0);
    stage(1);
    ST(2);
    WAITV(8);
    const unsigned long long tag = epoch_wait(epoch_raw);
    BAR();
    ST(3);
#pragma unroll
    for (int s = 1; s < 8; ++s) {
      if (s + 1 < 8) { stage(s + 1); WAITV(8); }
      else { stageq(0); stageq(1); stageq(2); stageq(3); WAITV(8); }
      BAR();
    }
    ST(4);
    stageq(4); stageq(5); stageq(6); stageq(7);
    const rsrc_t rsWS = __builtin_amdgcn_make_buffer_rsrc((void*)wsp, 0, WS_END, 0x00020000);
    const rsrc_t rsOA = __builtin_amdgcn_make_buffer_rsrc((void*)woA, 0, D_ * D_ * 2, 0x00020000);
    const rsrc_t rsOB = __builtin_amdgcn_make_buffer_rsrc((void*)woB, 0, D_ * D_ * 2, 0x00020000);
    const unsigned vo = rin * 1024 + csw;
    const unsigned hid = g * 4 + hw;
    u32x4 wob[16];
    const rsrc_t rsG = __builtin_amdgcn_make_buffer_rsrc((void*)gw, 0, D_ * 2, 0x00020000);
    const unsigned grow = ti * 128 + 8 * g + 2 * hw;
    const u32x4 x0 = __builtin_amdgcn_raw_buffer_load_b128(rsX, lane * 16, grow * 1024, 0);
    const u32x4 x1 = __builtin_amdgcn_raw_buffer_load_b128(rsX, lane * 16 + 1024, grow * 1024, 0);
    const u32x4 gv = __builtin_amdgcn_raw_buffer_load_b128(rsG, lane * 16, 0, 0);
    const unsigned foff = WS_FLAGC + (g * 4 + hw) * 128 + (lane & 15) * 8;
    const unsigned long long fc = ld_flag(ws, foff);
    asm volatile("" ::: "memory");
#pragma unroll
    for (int i = 0; i < 16; ++i) {
      wob[i] = __builtin_amdgcn_raw_buffer_load_b128(rsOB, vo, (hw + 4 * i) * 8192 + ct * 128, 0);
    }
    WAITV(20);
    ST(5);
    BAR();
    ST(6);
    {
      const f32x4 p0 = *(LAS f32x4*)(L + SCR + hw * 1024 + lane * 16), p1 = *(LAS f32x4*)(L + SCR + (4 + hw) * 1024 + lane * 16);
      const f32x4 z0 = *(LAS f32x4*)(L + SCR + (8 + hw) * 1024 + (lane & 3) * 16), z1 = *(LAS f32x4*)(L + SCR + (8 + hw) * 1024 + 64 + (lane & 3) * 16);
      const unsigned po = WS_PART + (hid * 16 + ti) * PART_STRIDE;
      st128<16>(nosent4(__builtin_bit_cast(u32x4, fadd4(p0, p1))), rsWS, (lane * 16) + (po));
      if (lane < 4) st128<16>(nosent4(__builtin_bit_cast(u32x4, fadd4(z0, z1))), rsWS, (1024 + lane * 16) + (po));
    }
    ST(7);
#pragma unroll
    for (int i = 0; i < 16; ++i) glds(vo, rsOA, (hw + 4 * i) * 8192 + ct * 128, WOA + (hw + 4 * i) * 1024);
    ST(8);
    {
      float a0 = 0.f, a1 = 0.f;
#pragma unroll
      for (int i = 0; i < 4; ++i) {
        const unsigned ug = gv[i], u0 = x0[i], u1 = x1[i];
        const float gl = __builtin_bit_cast(float, ug << 16), gh = __builtin_bit_cast(float, ug & 0xffff0000u);
        a0 = ffma(__builtin_bit_cast(float, u0 << 16), gl, a0); a0 = ffma(__builtin_bit_cast(float, u0 & 0xffff0000u), gh, a0);
        a1 = ffma(__builtin_bit_cast(float, u1 << 16), gl, a1); a1 = ffma(__builtin_bit_cast(float, u1 & 0xffff0000u), gh, a1);
      }
      a0 = wave_sum63(a0); a1 = wave_sum63(a1);
      const float s0 = __builtin_bit_cast(float, __builtin_amdgcn_readlane(__builtin_bit_cast(int, a0), 63));
      const float s1 = __builtin_bit_cast(float, __builtin_amdgcn_readlane(__builtin_bit_cast(int, a1), 63));
      const float lg = (lane & 1) ? s1 : s0;
      const float gval = __builtin_amdgcn_rcpf(1.0f + __builtin_amdgcn_exp2f(-1.44269504088896340736f * lg));
      if (lane < 2) __hip_atomic_store((GAS unsigned*)(ws + WS_GATE) + grow + lane, __builtin_bit_cast(unsigned, gval), __ATOMIC_RELAXED, __HIP_MEMORY_SCOPE_AGENT);
    }
    if (!__all(fc == tag)) wait_flags(ws, foff, tag);
    const unsigned pb = WS_PART + hid * 16 * PART_STRIDE;
    wait_written(ws, pb + (lane & 15) * PART_STRIDE + 1024 + 60);
    ST(9);
    {
      f32x4 a, z;
      for (unsigned spins = 0; spins < SPIN_CAP; ++spins) {
        u32x4 pv[16];
#pragma unroll
        for (int p = 0; p < 16; ++p) pv[p] = __builtin_amdgcn_raw_buffer_load_b128(rsWS, lane * 16, pb + p * PART_STRIDE, 16);
        const u32x4 zv = __builtin_amdgcn_raw_buffer_load_b128(rsWS, (lane >> 2) * PART_STRIDE + 1024 + (lane & 3) * 16, pb, 16);
        unsigned mx = umax3(zv[0], zv[1], umax3(zv[2], zv[3], 0u));
        a = __builtin_bit_cast(f32x4, pv[0]);
        mx = umax3(pv[0][0], pv[0][1], umax3(pv[0][2], pv[0][3], mx));
#pragma unroll
        for (int p = 1; p < 16; ++p) {
          a += __builtin_bit_cast(f32x4, pv[p]);
          mx = umax3(pv[p][0], pv[p][1], umax3(pv[p][2], pv[p][3], mx));
        }
        z = __builtin_bit_cast(f32x4, zv);
        if (__all(mx != SENT)) break;
      }
#pragma unroll
      for (int m = 4; m <= 32; m <<= 1) {
        z[0] += __shfl_xor(z[0], m); z[1] += __shfl_xor(z[1], m); z[2] += __shfl_xor(z[2], m); z[3] += __shfl_xor(z[3], m);
      }
      const int slot = lane & 31, jh = lane >> 5;
      u32x2 ka = {__builtin_bit_cast(unsigned, cvt2(a[0], a[1])), __builtin_bit_cast(unsigned, cvt2(a[2], a[3]))};
      *(LAS u32x2*)(L + AFR + hw * 1024 + ((slot >> 4) * 32 + (slot & 15)) * 16 + jh * 8) = ka;
      const int q4 = lane & 3;
      u32x2 kz = {__builtin_bit_cast(unsigned, cvt2(z[0], z[1])), __builtin_bit_cast(unsigned, cvt2(z[2], z[3]))};
      *(LAS u32x2*)(L + AFR + hw * 1024 + ((q4 & 1) * 32 + 16 + (lane >> 2)) * 16 + (q4 >> 1) * 8) = kz;
    }
    WAITL0();
    ST(10);
    BAR();
    ST(11);
#pragma unroll
    for (int i = 0; i < 16; ++i) *(LAS u32x4*)(L + WOB + (hw + 4 * i) * 1024 + lane * 16) = wob[i];
    WAITV(0); WAITL0();
    ST(12);
    BAR();
    {
      const int r32 = lane & 31, h = lane >> 5, g1 = (lane >> 4) & 1, q = (lane & 15) >> 2, p = lane & 3;
      const unsigned sw = ((2 * g1 + (p >> 1)) ^ (((q >> 1) & 1) << 2)) << 4;
      const int br2 = hw >> 1, sh = hw & 1;
      const unsigned st2 = 2 * rt + sh;
      const unsigned ob = WS_O + (st2 * 64 + 32 * br2) * 1024;
      for (int i = 0; i < PDELAY; ++i) __builtin_amdgcn_s_sleep(8);
      wait_flags(ws, WS_FLAGB + st2 * 128 + (lane & 15) * 8, tag);
      ST(13);
      unsigned wp0 = ((br2 ? WOB : WOA) + (4 * h + q) * 128 + sw + (p & 1) * 8) ^ (br2 << 6), wp1 = wp0 ^ 64;
      asm volatile("" : "+v"(wp0), "+v"(wp1));
      f32x16 c0 = {}, c1 = {};
      u32x4 of[32];
#pragma unroll
      for (int j = 0; j < OD; ++j) of[j] = __builtin_amdgcn_raw_buffer_load_b128(rsWS, lane * 16, ob + j * 1024, 16);
      const unsigned gvb = ld_u32(ws, WS_GATE + (st2 * 32 + r32) * 4);
      {
        bf16x8 a0[3], a1[3];
        a0[0] = ldstr(L, wp0, wp0 + 1024); a1[0] = ldstr(L, wp1, wp1 + 1024);
        a0[1] = ldstr(L, wp0 + 2048, wp0 + 2048 + 1024); a1[1] = ldstr(L, wp1 + 2048, wp1 + 2048 + 1024);
#pragma unroll
        for (int j = 0; j < 32; ++j) {
          const unsigned o = (j + 2) * 2048;
          const bf16x8 bf = __builtin_bit_cast(bf16x8, of[j]);
          SB();
          c0 = mfma(a0[j % 3], bf, c0);
          SB();
          if (j + OD < 32) of[j + OD] = __builtin_amdgcn_raw_buffer_load_b128(rsWS, lane * 16, ob + (j + OD) * 1024, 16);
          if (j + 2 < 32) a0[(j + 2) % 3] = ldstr(L, wp0 + o, wp0 + o + 1024);
          SB();
          c1 = mfma(a1[j % 3], bf, c1);
          SB();
          if (j + 2 < 32) a1[(j + 2) % 3] = ldstr(L, wp1 + o, wp1 + o + 1024);
        }
        SB();
      }
      ST(14);
      __builtin_amdgcn_s_setprio(0);
      const float gv = __builtin_bit_cast(float, gvb);
      const float gs = br2 ? 1.0f - gv : gv;
  #pragma unroll
      for (int qq = 0; qq < 4; ++qq) {
        const f32x4 v = {c1[4 * qq] * gs, c1[4 * qq + 1] * gs, c1[4 * qq + 2] * gs, c1[4 * qq + 3] * gs};
        *(LAS f32x4*)(L + EXC + hw * 4096 + qq * 1024 + lane * 16) = v;
      }
      WAITL0();
      BAR();
      if (b == 0 && hw == 0 && lane == 0) st_flag(ws, WS_EPOCH, tag);
      unsigned pk[8];
  #pragma unroll
      for (int qq = 0; qq < 4; ++qq) {
        const f32x4 v = *(LAS f32x4*)(L + EXC + (hw ^ 2) * 4096 + qq * 1024 + lane * 16);
        pk[2 * qq] = __builtin_bit_cast(unsigned, cvt2(c0[4 * qq] * gs + v[0], c0[4 * qq + 1] * gs + v[1]));
        pk[2 * qq + 1] = __builtin_bit_cast(unsigned, cvt2(c0[4 * qq + 2] * gs + v[2], c0[4 * qq + 3] * gs + v[3]));
      }
      const rsrc_t rsO = __builtin_amdgcn_make_buffer_rsrc((void*)out, 0, S_ * D_ * 2, 0x00020000);
      const unsigned orow = (st2 * 32 + r32) * 1024 + (64 * ct + 32 * br2) * 2 + h * 16;
  #pragma unroll
      for (int qq = 0; qq < 4; qq += 2) {
        const auto s0 = __builtin_amdgcn_permlane32_swap(pk[2 * qq], pk[2 * qq + 2], false, false);
        const auto s1 = __builtin_amdgcn_permlane32_swap(pk[2 * qq + 1], pk[2 * qq + 3], false, false);
        const u32x4 v = {s0[0], s1[0], s0[1], s1[1]};
        st128<0>(v, rsO, orow + qq * 16);
      }
      STR(16); STFLUSH();
    }
    return;
  }

  const unsigned long long epoch_raw = epoch_issue(wsp);
  const int wr = wave >> 1, wc = wave & 1;
  const int r32 = lane & 31, h = lane >> 5;
  const int g1 = (lane >> 4) & 1, q = (lane & 15) >> 2, p = lane & 3;
  const int own = wave, oth = wave ^ 1;
  const rsrc_t rsWS = __builtin_amdgcn_make_buffer_rsrc((void*)wsp, 0, WS_END, 0x00020000);
  unsigned ctouch = 0;
  {
    const unsigned off = (wave * 64 + lane) * 128;
    if (off < CODE_PREFETCH) { const char* a = (const char*)pc + off; asm volatile("global_load_dword %0, %1, off" : "=v"(ctouch) : "v"(a) : "memory"); }
  }
  unsigned otouch = 0;
  {
    const u32x4 sent = {SENT, SENT, SENT, SENT};
    const unsigned po = WS_PART + ((g * 4 + wave) * 16 + ti) * PART_STRIDE;
    st128<16>(sent, rsWS, lane * 16 + po);
    if (lane < 4) st128<16>(sent, rsWS, 1024 + lane * 16 + po);
  }
  const unsigned fx = (h ^ ((r32 >> 1) & 7)) << 4;
  unsigned xo0 = XR + (own * 32 + r32) * 128 + fx, xt0 = XR + (oth * 32 + r32) * 128 + fx;
  const unsigned sw = ((2 * g1 + (p >> 1)) ^ (((q >> 1) & 1) << 2)) << 4;
  const unsigned wn0 = (8 * h + q) * 128 + sw + (p & 1) * 8;
  unsigned wkv0 = WR + (wn0 ^ (wc << 6));
  asm volatile("" : "+v"(wkv0));
  unsigned xoa[4], xta[4];
#pragma unroll
  for (int k = 0; k < 4; ++k) { xoa[k] = xo0 ^ (k << 5); xta[k] = xt0 ^ (k << 5); asm volatile("" : "+v"(xoa[k]), "+v"(xta[k])); }
  const unsigned long long tag = epoch_wait(epoch_raw);

  bf16x8 xr[32];
  f32x16 kO = {}, kT = {}, vO = {}, vT = {};
  {
    BAR();
    ST(3);
    bf16x8 xt = ldsr128(L, xta[0]), wk = ldstr(L, wkv0, wkv0 + 512), wv = ldstr(L, wkv0 + 8192, wkv0 + 8192 + 512);
    xr[0] = ldsr128(L, xoa[0]);
#pragma unroll
    for (int t = 0; t < 32; ++t) {
      bf16x8 nxt, nwk, nwv;
      const int s1 = (t + 1) >> 2, k1 = (t + 1) & 3;
      const unsigned slot = (s1 % 3) * 16384;
      if (t + 1 < 32 && k1 == 0) {
        WAITL0();
        if (s1 == CLR_STAGE) { WAITV(0); asm volatile("" :: "v"(ctouch), "v"(otouch));
          if (lane == 0) st_flag(ws, WS_FLAGC + (g * 4 + wave) * 128 + ti * 8, tag); }
        BAR();
      }
      SB();
      kO = mfma(xr[t], wk, kO);
      SB();
      if (t + 1 < 32) {
        xr[t + 1] = ldsr128(L, xoa[k1] + slot);
        nwk = ldstr(L, wkv0 + slot + k1 * 2048, wkv0 + slot + k1 * 2048 + 512);
      }
      SB();
      kT = mfma(xt, wk, kT);
      SB();
      if (t + 1 < 32) {
        nxt = ldsr128(L, xta[k1] + slot);
        nwv = ldstr(L, wkv0 + slot + 8192 + k1 * 2048, wkv0 + slot + 8192 + k1 * 2048 + 512);
      }
      SB();
      vO = mfma(xr[t], wv, vO);
      vT = mfma(xt, wv, vT);
      SB();
      if (t + 1 < 32) { xt = nxt; wk = nwk; wv = nwv; }
    }
  }
  ST(4);
  {
    f32x16 kv = {};
    float zs = 0.f;
#pragma unroll
    for (int s = 0; s < 2; ++s) {
      const bf16x8 va = cvt8(vO, s), vb = cvt8(vT, s);
      const bf16x8 pa = phi8(kO, s);
      kv = mfma(pa, va, kv);
      const bf16x8 pb = phi8(kT, s);
      kv = mfma(pb, vb, kv);
      zs = sum8(pa, zs); zs = sum8(pb, zs);
    }
    {
      const unsigned zb = __builtin_bit_cast(unsigned, zs);
      const auto sw2 = __builtin_amdgcn_permlane32_swap(zb, zb, false, false);
      zs = __builtin_bit_cast(float, (unsigned)sw2[0]) + __builtin_bit_cast(float, (unsigned)sw2[1]);
    }
    const unsigned gm = g1 ? 0xFFFFFFFFu : 0u;
    f32x4 lo, hi;
#pragma unroll
    for (int j = 0; j < 4; ++j) { lo[j] = bsel(gm, kv[j], kv[8 + j]); hi[j] = bsel(gm, kv[4 + j], kv[12 + j]); }
    const int hh = 2 * wc + g1;
    const unsigned a = SCR + (4 * wr + hh) * 1024 + (h * 16 + (lane & 15)) * 16;
    *(LAS f32x4*)(L + a) = lo;
    *(LAS f32x4*)(L + a + 512) = hi;
    if (h == 0) *(LAS float*)(L + SCR + (8 + hh) * 1024 + wr * 64 + (lane & 15) * 4) = zs;
  }
  WAITL0();
  ST(5);
  BAR();
  ST(6);
  f32x16 qa = {}, qb = {};
  {
    unsigned wq0 = WQ + wn0, wq1 = WQ + (wn0 ^ 64);
    asm volatile("" : "+v"(wq0), "+v"(wq1));
    bf16x8 a0[3], a1[3];
    a0[0] = ldstr(L, wq0, wq0 + 512); a1[0] = ldstr(L, wq1, wq1 + 512);
    a0[1] = ldstr(L, wq0 + 2048, wq0 + 2048 + 512); a1[1] = ldstr(L, wq1 + 2048, wq1 + 2048 + 512);
#pragma unroll
    for (int t = 0; t < 32; ++t) {
      const unsigned o = (t + 2) * 2048;
      SB();
      qa = mfma(a0[t % 3], xr[t], qa);
      SB();
      if (t + 2 < 32) a0[(t + 2) % 3] = ldstr(L, wq0 + o, wq0 + o + 512);
      SB();
      qb = mfma(a1[t % 3], xr[t], qb);
      SB();
      if (t + 2 < 32) a1[(t + 2) % 3] = ldstr(L, wq1 + o, wq1 + o + 512);
    }
    SB();
  }
  ST(7);
  bf16x8 pq[4];
  pq[0] = phi8(qa, 0); pq[1] = phi8(qa, 1); pq[2] = phi8(qb, 0); pq[3] = phi8(qb, 1);
  WAITL0();
  ST(8);
  BAR();
  ST(9);
  {
    const unsigned st = 4 * ti + own;
    bf16x8 af[4];
#pragma unroll
    for (int hh = 0; hh < 4; ++hh) af[hh] = ldsr128(L, AFR + hh * 1024 + lane * 16);
    f32x16 d[4];
#pragma unroll
    for (int hh = 0; hh < 4; ++hh) { d[hh] = f32x16{}; d[hh] = mfma(af[hh], pq[hh], d[hh]); }
#pragma unroll
    for (int hh = 0; hh < 4; ++hh) {
      const float inv = __builtin_amdgcn_rcpf(d[hh][8] + 1e-6f);
      const bf16x8 o = pack8(d[hh][0] * inv, d[hh][1] * inv, d[hh][2] * inv, d[hh][3] * inv, d[hh][4] * inv, d[hh][5] * inv, d[hh][6] * inv, d[hh][7] * inv);
      st128<16>(__builtin_bit_cast(u32x4, o), rsWS, (lane * 16) + (WS_O + (st * 64 + 4 * g + hh) * 1024));
    }
  }
  ST(10);
  BAR();
  WAITV(0);
  if (lane == 0) st_flag(ws, WS_FLAGB + (4 * ti + own) * 128 + g * 8, tag);
  ST(11);
  STR(16); STFLUSH();
}

extern "C" void kernel_launch(void* const* d_in, const int* in_sizes, int n_in, void* d_out, int out_size, void* d_ws, size_t ws_size,
                              hipStream_t stream) {
  (void)in_sizes; (void)n_in; (void)out_size;
  const u16* x = (const u16*)d_in[0];
  const u16* wA = (const u16*)d_in[1];
  const u16* woA = (const u16*)d_in[2];
  const u16* wB = (const u16*)d_in[4];
  const u16* woB = (const u16*)d_in[5];
  const u16* gw = (const u16*)d_in[6];
  (void)hipFuncSetAttribute((const void*)gated_mix_fused, hipFuncAttributeMaxDynamicSharedMemorySize, LDS_BYTES);
  gated_mix_fused<<<256, 512, LDS_BYTES, stream>>>(x, wA, wB, woA, woB, gw, (u16*)d_out, (char*)d_ws + ((ws_size / 2) & ~(size_t)0x1FFFFF));
}
```

```cpp
#define PW 1
#define PB 1
#define PM 0
#include <hip/hip_runtime.h>
#include <hip/hip_bf16.h>
#include <stdint.h>

#define LAS __attribute__((address_space(3)))
#define GAS __attribute__((address_space(1)))
#define DI __device__ __forceinline__

typedef unsigned short u16;
typedef __bf16 bf16x8 __attribute__((ext_vector_type(8)));
typedef __bf16 bf16x4 __attribute__((ext_vector_type(4)));
typedef __bf16 bf16x2 __attribute__((ext_vector_type(2)));
typedef float f32x16 __attribute__((ext_vector_type(16)));
typedef float f32x4 __attribute__((ext_vector_type(4)));
typedef float f32x2 __attribute__((ext_vector_type(2)));
typedef unsigned u32x4 __attribute__((ext_vector_type(4)));
typedef unsigned u32x2 __attribute__((ext_vector_type(2)));
typedef __amdgpu_buffer_rsrc_t rsrc_t;

#define ROT 1
#define CODE_PREFETCH 21504
#define CLR_STAGE 6
#define HPRIO 3
#define OD 10
#define TOUCH 0
#define PSLEEP 4
#define OTOUCH 0
#define WTOUCH 0
#define PDELAY 5
#define PDELAY1 0
#define CODE_BACK 0

constexpr int S_ = 2048, D_ = 512;
constexpr unsigned XR = 0;
constexpr unsigned WR = 49152;
constexpr unsigned WQ = 98304;
constexpr unsigned WOA = 0;
constexpr unsigned WOB = 98304;
constexpr unsigned SCR = 81920;
constexpr unsigned AFR = SCR + 12288;
constexpr unsigned EXC = SCR;
constexpr unsigned LDS_BYTES = 163840;
constexpr unsigned WS_EPOCH = 0;
constexpr unsigned WS_FLAGC = 4096;
constexpr unsigned WS_FLAGB = WS_FLAGC + 1024 * 128;
constexpr unsigned WS_GATE = WS_FLAGB + 1024 * 128;
constexpr unsigned WS_PART = WS_GATE + 8192;
constexpr unsigned PART_STRIDE = 1152;
constexpr unsigned WS_O = WS_PART + 1024 * PART_STRIDE;
constexpr unsigned WS_END = WS_O + 64 * 64 * 1024;
constexpr unsigned SPIN_CAP = 1u << 18;

#define ST(i) do { if ((i) == PB && (PW == 1) == (wave >= 4)) { __builtin_amdgcn_sched_barrier(0); pX = (unsigned)__builtin_amdgcn_s_memrealtime() - pT0; \
   if (PW == 0 && lane == 0) __hip_atomic_store((GAS unsigned*)(ws + (12u << 20)) + blockIdx.x * 4 + (wave & 3), pX, __ATOMIC_RELAXED, __HIP_MEMORY_SCOPE_AGENT); __builtin_amdgcn_sched_barrier(0); } } while (0)
#define STR(i) do { if ((i) == 0) pT0 = (unsigned)__builtin_amdgcn_s_memrealtime(); } while (0)
#define STFLUSH() do { if (wave >= 4) { WAITV(0); if (PB == 99) pX = (unsigned)__builtin_amdgcn_s_memrealtime() - pT0; \
   if (PW == 0) pX = __builtin_amdgcn_readfirstlane(__hip_atomic_load((GAS unsigned*)(ws + (12u << 20)) + blockIdx.x * 4 + (wave & 3), __ATOMIC_RELAXED, __HIP_MEMORY_SCOPE_AGENT)); \
   unsigned X = pX; if (X > 3000) X = 3000; const unsigned long long until = __builtin_amdgcn_s_memrealtime() + (unsigned long long)X * PM; \
   while (__builtin_amdgcn_s_memrealtime() < until) __builtin_amdgcn_s_sleep(1); } } while (0)

DI void glds(unsigned voff, rsrc_t rs, unsigned soff, unsigned ldsaddr) {
  asm volatile("s_mov_b32 m0, %0\n\ts_nop 0\n\tbuffer_load_dwordx4 %1, %2, %3 offen lds"
               :: "s"(ldsaddr), "v"(voff), "s"(rs), "s"(soff) : "memory");
}
template <int AUX> DI void st128(u32x4 v, rsrc_t rs, unsigned off) { __builtin_amdgcn_raw_buffer_store_b128(v, rs, off, 0, AUX); }
#define WAITV(n) asm volatile("s_waitcnt vmcnt(" #n ")" ::: "memory")
#define WAITL0() asm volatile("s_waitcnt lgkmcnt(0)" ::: "memory")
#define BAR() asm volatile("s_barrier" ::: "memory")

DI f32x16 mfma(bf16x8 a, bf16x8 b, f32x16 c) { return __builtin_amdgcn_mfma_f32_32x32x16_bf16(a, b, c, 0, 0, 0); }
DI bf16x8 ldsr128(LAS char* L, unsigned a) { return *(LAS bf16x8*)(L + a); }
DI bf16x8 ldstr(LAS char* L, unsigned a0, unsigned a1) {
  bf16x4 lo = __builtin_amdgcn_ds_read_tr16_b64_v4bf16((LAS bf16x4*)(L + a0));
  bf16x4 hi = __builtin_amdgcn_ds_read_tr16_b64_v4bf16((LAS bf16x4*)(L + a1));
  return __builtin_shufflevector(lo, hi, 0, 1, 2, 3, 4, 5, 6, 7);
}
DI bf16x2 cvt2(float a, float b) { f32x2 p = {a, b}; return __builtin_convertvector(p, bf16x2); }
DI bf16x8 pack8(float a0, float a1, float a2, float a3, float a4, float a5, float a6, float a7) {
  bf16x2 p0 = cvt2(a0, a1), p1 = cvt2(a2, a3), p2 = cvt2(a4, a5), p3 = cvt2(a6, a7);
  bf16x4 lo = __builtin_shufflevector(p0, p1, 0, 1, 2, 3), hi = __builtin_shufflevector(p2, p3, 0, 1, 2, 3);
  return __builtin_shufflevector(lo, hi, 0, 1, 2, 3, 4, 5, 6, 7);
}
DI unsigned phi2(float a, float b) {
  const f32x2 t = {a, b};
  const f32x2 u = t * 1.44269504088896340736f;
  const f32x2 e = {__builtin_amdgcn_fmed3f(__builtin_amdgcn_exp2f(u[0]), 0.0f, 1.0f), __builtin_amdgcn_fmed3f(__builtin_amdgcn_exp2f(u[1]), 0.0f, 1.0f)};
  const int ia = __builtin_bit_cast(int, a), ib = __builtin_bit_cast(int, b);
  const f32x2 m = {__builtin_bit_cast(float, ia > 0 ? ia : 0), __builtin_bit_cast(float, ib > 0 ? ib : 0)};
  const f32x2 r = (m + e) + 1e-6f;
  return __builtin_bit_cast(unsigned, __builtin_convertvector(r, bf16x2));
}
DI bf16x8 phi8(const f32x16& x, int s) {
  const u32x4 v = {phi2(x[8 * s], x[8 * s + 1]), phi2(x[8 * s + 2], x[8 * s + 3]), phi2(x[8 * s + 4], x[8 * s + 5]), phi2(x[8 * s + 6], x[8 * s + 7])};
  return __builtin_bit_cast(bf16x8, v);
}
DI float bsel(unsigned m, float a, float b) {
  return __builtin_bit_cast(float, (__builtin_bit_cast(unsigned, a) & ~m) | (__builtin_bit_cast(unsigned, b) & m));
}
DI float fadd(float a, float b) { float r; asm("v_add_f32 %0, %1, %2" : "=v"(r) : "v"(a), "v"(b)); return r; }
DI f32x4 fadd4(f32x4 a, f32x4 b) { return f32x4{fadd(a[0], b[0]), fadd(a[1], b[1]), fadd(a[2], b[2]), fadd(a[3], b[3])}; }
DI float ffma(float a, float b, float c) { float r; asm("v_fma_f32 %0, %1, %2, %3" : "=v"(r) : "v"(a), "v"(b), "v"(c)); return r; }
template <int CTRL, int RM> DI float dpp_add(float v) {
  return fadd(v, __builtin_bit_cast(float, __builtin_amdgcn_update_dpp(0, __builtin_bit_cast(int, v), CTRL, RM, 0xf, true)));
}
DI float wave_sum63(float v) {
  v = dpp_add<0x111, 0xf>(v); v = dpp_add<0x112, 0xf>(v); v = dpp_add<0x114, 0xf>(v); v = dpp_add<0x118, 0xf>(v);
  v = dpp_add<0x142, 0xa>(v); v = dpp_add<0x143, 0xc>(v);
  return v;
}
constexpr unsigned SENT = 0xFFFFFFFFu;
DI unsigned nosent(unsigned v) { return v < SENT ? v : SENT - 1u; }
DI u32x4 nosent4(u32x4 v) { return u32x4{nosent(v[0]), nosent(v[1]), nosent(v[2]), nosent(v[3])}; }
DI unsigned umax3(unsigned a, unsigned b, unsigned c) { unsigned m = a > b ? a : b; return m > c ? m : c; }
#define SB() __builtin_amdgcn_sched_barrier(0)
DI bf16x8 cvt8(const f32x16& x, int s) {
  return pack8(x[8 * s], x[8 * s + 1], x[8 * s + 2], x[8 * s + 3], x[8 * s + 4], x[8 * s + 5], x[8 * s + 6], x[8 * s + 7]);
}
DI float sum8(bf16x8 v, float acc) {
  const bf16x2 ones = {(__bf16)1.0f, (__bf16)1.0f};
  acc = __builtin_amdgcn_fdot2_f32_bf16(__builtin_shufflevector(v, v, 0, 1), ones, acc, false);
  acc = __builtin_amdgcn_fdot2_f32_bf16(__builtin_shufflevector(v, v, 2, 3), ones, acc, false);
  acc = __builtin_amdgcn_fdot2_f32_bf16(__builtin_shufflevector(v, v, 4, 5), ones, acc, false);
  acc = __builtin_amdgcn_fdot2_f32_bf16(__builtin_shufflevector(v, v, 6, 7), ones, acc, false);
  return acc;
}
DI unsigned long long ld_flag(GAS char* ws, unsigned off) {
  return __hip_atomic_load((GAS unsigned long long*)(ws + off), __ATOMIC_RELAXED, __HIP_MEMORY_SCOPE_AGENT);
}
DI void st_flag(GAS char* ws, unsigned off, unsigned long long v) {
  __hip_atomic_store((GAS unsigned long long*)(ws + off), v, __ATOMIC_RELAXED, __HIP_MEMORY_SCOPE_AGENT);
}
DI unsigned long long epoch_issue(const char* wsp) {
  unsigned long long e;
  asm volatile("s_load_dwordx2 %0, %1, 0x0" : "=s"(e) : "s"(wsp));
  return e;
}
DI unsigned long long epoch_wait(unsigned long long e) {
  asm volatile("s_waitcnt lgkmcnt(0)" : "+s"(e) :: "memory");
  return e + 1ull;
}
DI void wait_flags(GAS char* ws, unsigned off, unsigned long long tag) {
  for (unsigned spins = 0; spins < SPIN_CAP; ++spins) {
    const unsigned long long v = ld_flag(ws, off);
    if (__all(v == tag)) break;
    __builtin_amdgcn_s_sleep(PSLEEP);
  }
  asm volatile("" ::: "memory");
}
DI unsigned ld_u32(GAS char* ws, unsigned off) {
  return __hip_atomic_load((GAS unsigned*)(ws + off), __ATOMIC_RELAXED, __HIP_MEMORY_SCOPE_AGENT);
}
DI void wait_written(GAS char* ws, unsigned off) {
  for (unsigned spins = 0; spins < SPIN_CAP; ++spins) {
    const unsigned v = ld_u32(ws, off);
    if (__all(v != SENT)) break;
    __builtin_amdgcn_s_sleep(PSLEEP);
  }
  asm volatile("" ::: "memory");
}

__global__ __launch_bounds__(512, 2) void gated_mix_fused(const u16* __restrict__ x, const u16* __restrict__ wA, const u16* __restrict__ wB,
                                                          const u16* __restrict__ woA, const u16* __restrict__ woB, const u16* __restrict__ gw,
                                                          u16* __restrict__ out, char* __restrict__ wsp
                                                          ) {
  extern __shared__ __attribute__((aligned(16))) char lds[];
  LAS char* L = (LAS char*)lds;
  const int tid = threadIdx.x;
  const int wave = __builtin_amdgcn_readfirstlane(tid >> 6), lane = tid & 63;
  const int b = blockIdx.x, xcd = b & 7, r = b >> 3;
  const int ti = (xcd & 1) * 8 + (r & 7), g = (xcd >> 1) * 4 + (r >> 3);
  const int gg = g & 7;
  const int rt = 4 * xcd + (r & 3), ct = r >> 2;
  GAS char* ws = (GAS char*)wsp;
  asm volatile("" :: "s"(x), "s"(wA), "s"(wB), "s"(woA), "s"(woB), "s"(gw), "s"(out), "s"(wsp));
  unsigned pX = 0, pT0 = 0;
  STR(0); ST(1);
  unsigned long long pc;
  asm volatile("s_getpc_b64 %0" : "=s"(pc));
  pc &= ~255ull;

  if (wave >= 4) {
    const int hw = wave - 4;
    const unsigned long long epoch_raw = epoch_issue(wsp);
    __builtin_amdgcn_s_setprio(HPRIO);
    int ro = 0;
    if (ROT == 1) ro = (ti + 2 * g) & 7;
    if (ROT == 2) ro = 2 * ((ti + g) & 1);
    if (ROT == 3) ro = 4 * ((ti + g) & 1);
    if (ROT == 5) ro = ((ti + g) % 3) * 2;
    const u16* w = (g >= 8) ? wB : wA;
    const rsrc_t rsX = __builtin_amdgcn_make_buffer_rsrc((void*)x, 0, S_ * D_ * 2, 0x00020000);
    const rsrc_t rsW = __builtin_amdgcn_make_buffer_rsrc((void*)w, 0, D_ * 3 * D_ * 2, 0x00020000);
    const int rin = lane >> 3, ch = lane & 7;
    const unsigned vx = rin * 1024 + ((ch ^ (4 * (hw & 1) + (rin >> 1))) << 4);
    const unsigned csw = (ch ^ (((rin >> 1) & 1) << 2)) << 4;
    const unsigned vw = rin * 3072 + csw;
    const unsigned sx0 = (ti * 128 + 8 * hw) * 1024;
    const unsigned sw0 = 8 * hw * 3072 + gg * 128;
    auto stage = [&](int s) {
      const unsigned ks = (s + ro) & 7, slot = (s % 3) * 16384;
#pragma unroll
      for (int i = 0; i < 4; ++i) glds(vx, rsX, sx0 + ks * 128 + i * 32768, XR + slot + (hw + 4 * i) * 1024);
#pragma unroll
      for (int i = 0; i < 2; ++i) glds(vw, rsW, sw0 + ks * 196608 + i * 98304 + 1024, WR + slot + (hw + 4 * i) * 1024);
#pragma unroll
      for (int i = 0; i < 2; ++i) glds(vw, rsW, sw0 + ks * 196608 + i * 98304 + 2048, WR + slot + 8192 + (hw + 4 * i) * 1024);
    };
    auto stageq = [&](int s) {
      const unsigned ks = (s + ro) & 7;
#pragma unroll
      for (int i = 0; i < 2; ++i) glds(vw, rsW, sw0 + ks * 196608 + i * 98304, WQ + s * 8192 + (hw + 4 * i) * 1024);
    };
    ST(15);
    stage(0);
    stage(1);
    ST(2);
    WAITV(8);
    const unsigned long long tag = epoch_wait(epoch_raw);
    BAR();
    ST(3);
#pragma unroll
    for (int s = 1; s < 8; ++s) {
      if (s + 1 < 8) { stage(s + 1); WAITV(8); }
      else { stageq(0); stageq(1); stageq(2); stageq(3); WAITV(8); }
      BAR();
    }
    ST(4);
    stageq(4); stageq(5); stageq(6); stageq(7);
    const rsrc_t rsWS = __builtin_amdgcn_make_buffer_rsrc((void*)wsp, 0, WS_END, 0x00020000);
    const rsrc_t rsOA = __builtin_amdgcn_make_buffer_rsrc((void*)woA, 0, D_ * D_ * 2, 0x00020000);
    const rsrc_t rsOB = __builtin_amdgcn_make_buffer_rsrc((void*)woB, 0, D_ * D_ * 2, 0x00020000);
    const unsigned vo = rin * 1024 + csw;
    const unsigned hid = g * 4 + hw;
    u32x4 wob[16];
    const rsrc_t rsG = __builtin_amdgcn_make_buffer_rsrc((void*)gw, 0, D_ * 2, 0x00020000);
    const unsigned grow = ti * 128 + 8 * g + 2 * hw;
    const u32x4 x0 = __builtin_amdgcn_raw_buffer_load_b128(rsX, lane * 16, grow * 1024, 0);
    const u32x4 x1 = __builtin_amdgcn_raw_buffer_load_b128(rsX, lane * 16 + 1024, grow * 1024, 0);
    const u32x4 gv = __builtin_amdgcn_raw_buffer_load_b128(rsG, lane * 16, 0, 0);
    const unsigned foff = WS_FLAGC + (g * 4 + hw) * 128 + (lane & 15) * 8;
    const unsigned long long fc = ld_flag(ws, foff);
    asm volatile("" ::: "memory");
#pragma unroll
    for (int i = 0; i < 16; ++i) {
      wob[i] = __builtin_amdgcn_raw_buffer_load_b128(rsOB, vo, (hw + 4 * i) * 8192 + ct * 128, 0);
    }
    WAITV(20);
    ST(5);
    BAR();
    ST(6);
    {
      const f32x4 p0 = *(LAS f32x4*)(L + SCR + hw * 1024 + lane * 16), p1 = *(LAS f32x4*)(L + SCR + (4 + hw) * 1024 + lane * 16);
      const f32x4 z0 = *(LAS f32x4*)(L + SCR + (8 + hw) * 1024 + (lane & 3) * 16), z1 = *(LAS f32x4*)(L + SCR + (8 + hw) * 1024 + 64 + (lane & 3) * 16);
      const unsigned po = WS_PART + (hid * 16 + ti) * PART_STRIDE;
      st128<16>(nosent4(__builtin_bit_cast(u32x4, fadd4(p0, p1))), rsWS, (lane * 16) + (po));
      if (lane < 4) st128<16>(nosent4(__builtin_bit_cast(u32x4, fadd4(z0, z1))), rsWS, (1024 + lane * 16) + (po));
    }
    ST(7);
#pragma unroll
    for (int i = 0; i < 16; ++i) glds(vo, rsOA, (hw + 4 * i) * 8192 + ct * 128, WOA + (hw + 4 * i) * 1024);
    ST(8);
    {
      float a0 = 0.f, a1 = 0.f;
#pragma unroll
      for (int i = 0; i < 4; ++i) {
        const unsigned ug = gv[i], u0 = x0[i], u1 = x1[i];
        const float gl = __builtin_bit_cast(float, ug << 16), gh = __builtin_bit_cast(float, ug & 0xffff0000u);
        a0 = ffma(__builtin_bit_cast(float, u0 << 16), gl, a0); a0 = ffma(__builtin_bit_cast(float, u0 & 0xffff0000u), gh, a0);
        a1 = ffma(__builtin_bit_cast(float, u1 << 16), gl, a1); a1 = ffma(__builtin_bit_cast(float, u1 & 0xffff0000u), gh, a1);
      }
      a0 = wave_sum63(a0); a1 = wave_sum63(a1);
      const float s0 = __builtin_bit_cast(float, __builtin_amdgcn_readlane(__builtin_bit_cast(int, a0), 63));
      const float s1 = __builtin_bit_cast(float, __builtin_amdgcn_readlane(__builtin_bit_cast(int, a1), 63));
      const float lg = (lane & 1) ? s1 : s0;
      const float gval = __builtin_amdgcn_rcpf(1.0f + __builtin_amdgcn_exp2f(-1.44269504088896340736f * lg));
      if (lane < 2) __hip_atomic_store((GAS unsigned*)(ws + WS_GATE) + grow + lane, __builtin_bit_cast(unsigned, gval), __ATOMIC_RELAXED, __HIP_MEMORY_SCOPE_AGENT);
    }
    if (!__all(fc == tag)) wait_flags(ws, foff, tag);
    const unsigned pb = WS_PART + hid * 16 * PART_STRIDE;
    wait_written(ws, pb + (lane & 15) * PART_STRIDE + 1024 + 60);
    ST(9);
    {
      f32x4 a, z;
      for (unsigned spins = 0; spins < SPIN_CAP; ++spins) {
        u32x4 pv[16];
#pragma unroll
        for (int p = 0; p < 16; ++p) pv[p] = __builtin_amdgcn_raw_buffer_load_b128(rsWS, lane * 16, pb + p * PART_STRIDE, 16);
        const u32x4 zv = __builtin_amdgcn_raw_buffer_load_b128(rsWS, (lane >> 2) * PART_STRIDE + 1024 + (lane & 3) * 16, pb, 16);
        unsigned mx = umax3(zv[0], zv[1], umax3(zv[2], zv[3], 0u));
        a = __builtin_bit_cast(f32x4, pv[0]);
        mx = umax3(pv[0][0], pv[0][1], umax3(pv[0][2], pv[0][3], mx));
#pragma unroll
        for (int p = 1; p < 16; ++p) {
          a += __builtin_bit_cast(f32x4, pv[p]);
          mx = umax3(pv[p][0], pv[p][1], umax3(pv[p][2], pv[p][3], mx));
        }
        z = __builtin_bit_cast(f32x4, zv);
        if (__all(mx != SENT)) break;
      }
#pragma unroll
      for (int m = 4; m <= 32; m <<= 1) {
        z[0] += __shfl_xor(z[0], m); z[1] += __shfl_xor(z[1], m); z[2] += __shfl_xor(z[2], m); z[3] += __shfl_xor(z[3], m);
      }
      const int slot = lane & 31, jh = lane >> 5;
      u32x2 ka = {__builtin_bit_cast(unsigned, cvt2(a[0], a[1])), __builtin_bit_cast(unsigned, cvt2(a[2], a[3]))};
      *(LAS u32x2*)(L + AFR + hw * 1024 + ((slot >> 4) * 32 + (slot & 15)) * 16 + jh * 8) = ka;
      const int q4 = lane & 3;
      u32x2 kz = {__builtin_bit_cast(unsigned, cvt2(z[0], z[1])), __builtin_bit_cast(unsigned, cvt2(z[2], z[3]))};
      *(LAS u32x2*)(L + AFR + hw * 1024 + ((q4 & 1) * 32 + 16 + (lane >> 2)) * 16 + (q4 >> 1) * 8) = kz;
    }
    WAITL0();
    ST(10);
    BAR();
    ST(11);
#pragma unroll
    for (int i = 0; i < 16; ++i) *(LAS u32x4*)(L + WOB + (hw + 4 * i) * 1024 + lane * 16) = wob[i];
    WAITV(0); WAITL0();
    ST(12);
    BAR();
    {
      const int r32 = lane & 31, h = lane >> 5, g1 = (lane >> 4) & 1, q = (lane & 15) >> 2, p = lane & 3;
      const unsigned sw = ((2 * g1 + (p >> 1)) ^ (((q >> 1) & 1) << 2)) << 4;
      const int br2 = hw >> 1, sh = hw & 1;
      const unsigned st2 = 2 * rt + sh;
      const unsigned ob = WS_O + (st2 * 64 + 32 * br2) * 1024;
      for (int i = 0; i < PDELAY; ++i) __builtin_amdgcn_s_sleep(8);
      wait_flags(ws, WS_FLAGB + st2 * 128 + (lane & 15) * 8, tag);
      ST(13);
      unsigned wp0 = ((br2 ? WOB : WOA) + (4 * h + q) * 128 + sw + (p & 1) * 8) ^ (br2 << 6), wp1 = wp0 ^ 64;
      asm volatile("" : "+v"(wp0), "+v"(wp1));
      f32x16 c0 = {}, c1 = {};
      u32x4 of[32];
#pragma unroll
      for (int j = 0; j < OD; ++j) of[j] = __builtin_amdgcn_raw_buffer_load_b128(rsWS, lane * 16, ob + j * 1024, 16);
      const unsigned gvb = ld_u32(ws, WS_GATE + (st2 * 32 + r32) * 4);
      {
        bf16x8 a0[3], a1[3];
        a0[0] = ldstr(L, wp0, wp0 + 1024); a1[0] = ldstr(L, wp1, wp1 + 1024);
        a0[1] = ldstr(L, wp0 + 2048, wp0 + 2048 + 1024); a1[1] = ldstr(L, wp1 + 2048, wp1 + 2048 + 1024);
#pragma unroll
        for (int j = 0; j < 32; ++j) {
          const unsigned o = (j + 2) * 2048;
          const bf16x8 bf = __builtin_bit_cast(bf16x8, of[j]);
          SB();
          c0 = mfma(a0[j % 3], bf, c0);
          SB();
          if (j + OD < 32) of[j + OD] = __builtin_amdgcn_raw_buffer_load_b128(rsWS, lane * 16, ob + (j + OD) * 1024, 16);
          if (j + 2 < 32) a0[(j + 2) % 3] = ldstr(L, wp0 + o, wp0 + o + 1024);
          SB();
          c1 = mfma(a1[j % 3], bf, c1);
          SB();
          if (j + 2 < 32) a1[(j + 2) % 3] = ldstr(L, wp1 + o, wp1 + o + 1024);
        }
        SB();
      }
      ST(14);
      __builtin_amdgcn_s_setprio(0);
      const float gv = __builtin_bit_cast(float, gvb);
      const float gs = br2 ? 1.0f - gv : gv;
  #pragma unroll
      for (int qq = 0; qq < 4; ++qq) {
        const f32x4 v = {c1[4 * qq] * gs, c1[4 * qq + 1] * gs, c1[4 * qq + 2] * gs, c1[4 * qq + 3] * gs};
        *(LAS f32x4*)(L + EXC + hw * 4096 + qq * 1024 + lane * 16) = v;
      }
      WAITL0();
      BAR();
      if (b == 0 && hw == 0 && lane == 0) st_flag(ws, WS_EPOCH, tag);
      unsigned pk[8];
  #pragma unroll
      for (int qq = 0; qq < 4; ++qq) {
        const f32x4 v = *(LAS f32x4*)(L + EXC + (hw ^ 2) * 4096 + qq * 1024 + lane * 16);
        pk[2 * qq] = __builtin_bit_cast(unsigned, cvt2(c0[4 * qq] * gs + v[0], c0[4 * qq + 1] * gs + v[1]));
        pk[2 * qq + 1] = __builtin_bit_cast(unsigned, cvt2(c0[4 * qq + 2] * gs + v[2], c0[4 * qq + 3] * gs + v[3]));
      }
      const rsrc_t rsO = __builtin_amdgcn_make_buffer_rsrc((void*)out, 0, S_ * D_ * 2, 0x00020000);
      const unsigned orow = (st2 * 32 + r32) * 1024 + (64 * ct + 32 * br2) * 2 + h * 16;
  #pragma unroll
      for (int qq = 0; qq < 4; qq += 2) {
        const auto s0 = __builtin_amdgcn_permlane32_swap(pk[2 * qq], pk[2 * qq + 2], false, false);
        const auto s1 = __builtin_amdgcn_permlane32_swap(pk[2 * qq + 1], pk[2 * qq + 3], false, false);
        const u32x4 v = {s0[0], s1[0], s0[1], s1[1]};
        st128<0>(v, rsO, orow + qq * 16);
      }
      STR(16); STFLUSH();
    }
    return;
  }

  const unsigned long long epoch_raw = epoch_issue(wsp);
  const int wr = wave >> 1, wc = wave & 1;
  const int r32 = lane & 31, h = lane >> 5;
  const int g1 = (lane >> 4) & 1, q = (lane & 15) >> 2, p = lane & 3;
  const int own = wave, oth = wave ^ 1;
  const rsrc_t rsWS = __builtin_amdgcn_make_buffer_rsrc((void*)wsp, 0, WS_END, 0x00020000);
  unsigned ctouch = 0;
  {
    const unsigned off = (wave * 64 + lane) * 128;
    if (off < CODE_PREFETCH) { const char* a = (const char*)pc + off; asm volatile("global_load_dword %0, %1, off" : "=v"(ctouch) : "v"(a) : "memory"); }
  }
  unsigned otouch = 0;
  {
    const u32x4 sent = {SENT, SENT, SENT, SENT};
    const unsigned po = WS_PART + ((g * 4 + wave) * 16 + ti) * PART_STRIDE;
    st128<16>(sent, rsWS, lane * 16 + po);
    if (lane < 4) st128<16>(sent, rsWS, 1024 + lane * 16 + po);
  }
  const unsigned fx = (h ^ ((r32 >> 1) & 7)) << 4;
  unsigned xo0 = XR + (own * 32 + r32) * 128 + fx, xt0 = XR + (oth * 32 + r32) * 128 + fx;
  const unsigned sw = ((2 * g1 + (p >> 1)) ^ (((q >> 1) & 1) << 2)) << 4;
  const unsigned wn0 = (8 * h + q) * 128 + sw + (p & 1) * 8;
  unsigned wkv0 = WR + (wn0 ^ (wc << 6));
  asm volatile("" : "+v"(wkv0));
  unsigned xoa[4], xta[4];
#pragma unroll
  for (int k = 0; k < 4; ++k) { xoa[k] = xo0 ^ (k << 5); xta[k] = xt0 ^ (k << 5); asm volatile("" : "+v"(xoa[k]), "+v"(xta[k])); }
  const unsigned long long tag = epoch_wait(epoch_raw);

  bf16x8 xr[32];
  f32x16 kO = {}, kT = {}, vO = {}, vT = {};
  {
    BAR();
    ST(3);
    bf16x8 xt = ldsr128(L, xta[0]), wk = ldstr(L, wkv0, wkv0 + 512), wv = ldstr(L, wkv0 + 8192, wkv0 + 8192 + 512);
    xr[0] = ldsr128(L, xoa[0]);
#pragma unroll
    for (int t = 0; t < 32; ++t) {
      bf16x8 nxt, nwk, nwv;
      const int s1 = (t + 1) >> 2, k1 = (t + 1) & 3;
      const unsigned slot = (s1 % 3) * 16384;
      if (t + 1 < 32 && k1 == 0) {
        WAITL0();
        if (s1 == CLR_STAGE) { WAITV(0); asm volatile("" :: "v"(ctouch), "v"(otouch));
          if (lane == 0) st_flag(ws, WS_FLAGC + (g * 4 + wave) * 128 + ti * 8, tag); }
        BAR();
      }
      SB();
      kO = mfma(xr[t], wk, kO);
      SB();
      if (t + 1 < 32) {
        xr[t + 1] = ldsr128(L, xoa[k1] + slot);
        nwk = ldstr(L, wkv0 + slot + k1 * 2048, wkv0 + slot + k1 * 2048 + 512);
      }
      SB();
      kT = mfma(xt, wk, kT);
      SB();
      if (t + 1 < 32) {
        nxt = ldsr128(L, xta[k1] + slot);
        nwv = ldstr(L, wkv0 + slot + 8192 + k1 * 2048, wkv0 + slot + 8192 + k1 * 2048 + 512);
      }
      SB();
      vO = mfma(xr[t], wv, vO);
      vT = mfma(xt, wv, vT);
      SB();
      if (t + 1 < 32) { xt = nxt; wk = nwk; wv = nwv; }
    }
  }
  ST(4);
  {
    f32x16 kv = {};
    float zs = 0.f;
#pragma unroll
    for (int s = 0; s < 2; ++s) {
      const bf16x8 va = cvt8(vO, s), vb = cvt8(vT, s);
      const bf16x8 pa = phi8(kO, s);
      kv = mfma(pa, va, kv);
      const bf16x8 pb = phi8(kT, s);
      kv = mfma(pb, vb, kv);
      zs = sum8(pa, zs); zs = sum8(pb, zs);
    }
    {
      const unsigned zb = __builtin_bit_cast(unsigned, zs);
      const auto sw2 = __builtin_amdgcn_permlane32_swap(zb, zb, false, false);
      zs = __builtin_bit_cast(float, (unsigned)sw2[0]) + __builtin_bit_cast(float, (unsigned)sw2[1]);
    }
    const unsigned gm = g1 ? 0xFFFFFFFFu : 0u;
    f32x4 lo, hi;
#pragma unroll
    for (int j = 0; j < 4; ++j) { lo[j] = bsel(gm, kv[j], kv[8 + j]); hi[j] = bsel(gm, kv[4 + j], kv[12 + j]); }
    const int hh = 2 * wc + g1;
    const unsigned a = SCR + (4 * wr + hh) * 1024 + (h * 16 + (lane & 15)) * 16;
    *(LAS f32x4*)(L + a) = lo;
    *(LAS f32x4*)(L + a + 512) = hi;
    if (h == 0) *(LAS float*)(L + SCR + (8 + hh) * 1024 + wr * 64 + (lane & 15) * 4) = zs;
  }
  WAITL0();
  ST(5);
  BAR();
  ST(6);
  f32x16 qa = {}, qb = {};
  {
    unsigned wq0 = WQ + wn0, wq1 = WQ + (wn0 ^ 64);
    asm volatile("" : "+v"(wq0), "+v"(wq1));
    bf16x8 a0[3], a1[3];
    a0[0] = ldstr(L, wq0, wq0 + 512); a1[0] = ldstr(L, wq1, wq1 + 512);
    a0[1] = ldstr(L, wq0 + 2048, wq0 + 2048 + 512); a1[1] = ldstr(L, wq1 + 2048, wq1 + 2048 + 512);
#pragma unroll
    for (int t = 0; t < 32; ++t) {
      const unsigned o = (t + 2) * 2048;
      SB();
      qa = mfma(a0[t % 3], xr[t], qa);
      SB();
      if (t + 2 < 32) a0[(t + 2) % 3] = ldstr(L, wq0 + o, wq0 + o + 512);
      SB();
      qb = mfma(a1[t % 3], xr[t], qb);
      SB();
      if (t + 2 < 32) a1[(t + 2) % 3] = ldstr(L, wq1 + o, wq1 + o + 512);
    }
    SB();
  }
  ST(7);
  bf16x8 pq[4];
  pq[0] = phi8(qa, 0); pq[1] = phi8(qa, 1); pq[2] = phi8(qb, 0); pq[3] = phi8(qb, 1);
  WAITL0();
  ST(8);
  BAR();
  ST(9);
  {
    const unsigned st = 4 * ti + own;
    bf16x8 af[4];
#pragma unroll
    for (int hh = 0; hh < 4; ++hh) af[hh] = ldsr128(L, AFR + hh * 1024 + lane * 16);
    f32x16 d[4];
#pragma unroll
    for (int hh = 0; hh < 4; ++hh) { d[hh] = f32x16{}; d[hh] = mfma(af[hh], pq[hh], d[hh]); }
#pragma unroll
    for (int hh = 0; hh < 4; ++hh) {
      const float inv = __builtin_amdgcn_rcpf(d[hh][8] + 1e-6f);
      const bf16x8 o = pack8(d[hh][0] * inv, d[hh][1] * inv, d[hh][2] * inv, d[hh][3] * inv, d[hh][4] * inv, d[hh][5] * inv, d[hh][6] * inv, d[hh][7] * inv);
      st128<16>(__builtin_bit_cast(u32x4, o), rsWS, (lane * 16) + (WS_O + (st * 64 + 4 * g + hh) * 1024));
    }
  }
  ST(10);
  BAR();
  WAITV(0);
  if (lane == 0) st_flag(ws, WS_FLAGB + (4 * ti + own) * 128 + g * 8, tag);
  ST(11);
  STR(16); STFLUSH();
}

extern "C" void kernel_launch(void* const* d_in, const int* in_sizes, int n_in, void* d_out, int out_size, void* d_ws, size_t ws_size,
                              hipStream_t stream) {
  (void)in_sizes; (void)n_in; (void)out_size;
  const u16* x = (const u16*)d_in[0];
  const u16* wA = (const u16*)d_in[1];
  const u16* woA = (const u16*)d_in[2];
  const u16* wB = (const u16*)d_in[4];
  const u16* woB = (const u16*)d_in[5];
  const u16* gw = (const u16*)d_in[6];
  (void)hipFuncSetAttribute((const void*)gated_mix_fused, hipFuncAttributeMaxDynamicSharedMemorySize, LDS_BYTES);
  gated_mix_fused<<<256, 512, LDS_BYTES, stream>>>(x, wA, wB, woA, woB, gw, (u16*)d_out, (char*)d_ws + ((ws_size / 2) & ~(size_t)0x1FFFFF));
}
```

```cpp
#define PW 1
#define PB 1
#define PM 0
#include <hip/hip_runtime.h>
#include <hip/hip_bf16.h>
#include <stdint.h>

#define LAS __attribute__((address_space(3)))
#define GAS __attribute__((address_space(1)))
#define DI __device__ __forceinline__

typedef unsigned short u16;
typedef __bf16 bf16x8 __attribute__((ext_vector_type(8)));
typedef __bf16 bf16x4 __attribute__((ext_vector_type(4)));
typedef __bf16 bf16x2 __attribute__((ext_vector_type(2)));
typedef float f32x16 __attribute__((ext_vector_type(16)));
typedef float f32x4 __attribute__((ext_vector_type(4)));
typedef float f32x2 __attribute__((ext_vector_type(2)));
typedef unsigned u32x4 __attribute__((ext_vector_type(4)));
typedef unsigned u32x2 __attribute__((ext_vector_type(2)));
typedef __amdgpu_buffer_rsrc_t rsrc_t;

#define ROT 1
#define CODE_PREFETCH 0
#define CLR_STAGE 6
#define HPRIO 3
#define OD 10
#define TOUCH 0
#define PSLEEP 4
#define OTOUCH 0
#define WTOUCH 0
#define PDELAY 5
#define PDELAY1 0
#define CODE_BACK 0

constexpr int S_ = 2048, D_ = 512;
constexpr unsigned XR = 0;
constexpr unsigned WR = 49152;
constexpr unsigned WQ = 98304;
constexpr unsigned WOA = 0;
constexpr unsigned WOB = 98304;
constexpr unsigned SCR = 81920;
constexpr unsigned AFR = SCR + 12288;
constexpr unsigned EXC = SCR;
constexpr unsigned LDS_BYTES = 163840;
constexpr unsigned WS_EPOCH = 0;
constexpr unsigned WS_FLAGC = 4096;
constexpr unsigned WS_FLAGB = WS_FLAGC + 1024 * 128;
constexpr unsigned WS_GATE = WS_FLAGB + 1024 * 128;
constexpr unsigned WS_PART = WS_GATE + 8192;
constexpr unsigned PART_STRIDE = 1152;
constexpr unsigned WS_O = WS_PART + 1024 * PART_STRIDE;
constexpr unsigned WS_END = WS_O + 64 * 64 * 1024;
constexpr unsigned SPIN_CAP = 1u << 18;

#define ST(i) do { if ((i) == PB && (PW == 1) == (wave >= 4)) { __builtin_amdgcn_sched_barrier(0); pX = (unsigned)__builtin_amdgcn_s_memrealtime() - pT0; \
   if (PW == 0 && lane == 0) __hip_atomic_store((GAS unsigned*)(ws + (12u << 20)) + blockIdx.x * 4 + (wave & 3), pX, __ATOMIC_RELAXED, __HIP_MEMORY_SCOPE_AGENT); __builtin_amdgcn_sched_barrier(0); } } while (0)
#define STR(i) do { if ((i) == 0) pT0 = (unsigned)__builtin_amdgcn_s_memrealtime(); } while (0)
#define STFLUSH() do { if (wave >= 4) { WAITV(0); if (PB == 99) pX = (unsigned)__builtin_amdgcn_s_memrealtime() - pT0; \
   if (PW == 0) pX = __builtin_amdgcn_readfirstlane(__hip_atomic_load((GAS unsigned*)(ws + (12u << 20)) + blockIdx.x * 4 + (wave & 3), __ATOMIC_RELAXED, __HIP_MEMORY_SCOPE_AGENT)); \
   unsigned X = pX; if (X > 3000) X = 3000; const unsigned long long until = __builtin_amdgcn_s_memrealtime() + (unsigned long long)X * PM; \
   while (__builtin_amdgcn_s_memrealtime() < until) __builtin_amdgcn_s_sleep(1); } } while (0)

DI void glds(unsigned voff, rsrc_t rs, unsigned soff, unsigned ldsaddr) {
  asm volatile("s_mov_b32 m0, %0\n\ts_nop 0\n\tbuffer_load_dwordx4 %1, %2, %3 offen lds"
               :: "s"(ldsaddr), "v"(voff), "s"(rs), "s"(soff) : "memory");
}
template <int AUX> DI void st128(u32x4 v, rsrc_t rs, unsigned off) { __builtin_amdgcn_raw_buffer_store_b128(v, rs, off, 0, AUX); }
#define WAITV(n) asm volatile("s_waitcnt vmcnt(" #n ")" ::: "memory")
#define WAITL0() asm volatile("s_waitcnt lgkmcnt(0)" ::: "memory")
#define BAR() asm volatile("s_barrier" ::: "memory")

DI f32x16 mfma(bf16x8 a, bf16x8 b, f32x16 c) { return __builtin_amdgcn_mfma_f32_32x32x16_bf16(a, b, c, 0, 0, 0); }
DI bf16x8 ldsr128(LAS char* L, unsigned a) { return *(LAS bf16x8*)(L + a); }
DI bf16x8 ldstr(LAS char* L, unsigned a0, unsigned a1) {
  bf16x4 lo = __builtin_amdgcn_ds_read_tr16_b64_v4bf16((LAS bf16x4*)(L + a0));
  bf16x4 hi = __builtin_amdgcn_ds_read_tr16_b64_v4bf16((LAS bf16x4*)(L + a1));
  return __builtin_shufflevector(lo, hi, 0, 1, 2, 3, 4, 5, 6, 7);
}
DI bf16x2 cvt2(float a, float b) { f32x2 p = {a, b}; return __builtin_convertvector(p, bf16x2); }
DI bf16x8 pack8(float a0, float a1, float a2, float a3, float a4, float a5, float a6, float a7) {
  bf16x2 p0 = cvt2(a0, a1), p1 = cvt2(a2, a3), p2 = cvt2(a4, a5), p3 = cvt2(a6, a7);
  bf16x4 lo = __builtin_shufflevector(p0, p1, 0, 1, 2, 3), hi = __builtin_shufflevector(p2, p3, 0, 1, 2, 3);
  return __builtin_shufflevector(lo, hi, 0, 1, 2, 3, 4, 5, 6, 7);
}
DI unsigned phi2(float a, float b) {
  const f32x2 t = {a, b};
  const f32x2 u = t * 1.44269504088896340736f;
  const f32x2 e = {__builtin_amdgcn_fmed3f(__builtin_amdgcn_exp2f(u[0]), 0.0f, 1.0f), __builtin_amdgcn_fmed3f(__builtin_amdgcn_exp2f(u[1]), 0.0f, 1.0f)};
  const int ia = __builtin_bit_cast(int, a), ib = __builtin_bit_cast(int, b);
  const f32x2 m = {__builtin_bit_cast(float, ia > 0 ? ia : 0), __builtin_bit_cast(float, ib > 0 ? ib : 0)};
  const f32x2 r = (m + e) + 1e-6f;
  return __builtin_bit_cast(unsigned, __builtin_convertvector(r, bf16x2));
}
DI bf16x8 phi8(const f32x16& x, int s) {
  const u32x4 v = {phi2(x[8 * s], x[8 * s + 1]), phi2(x[8 * s + 2], x[8 * s + 3]), phi2(x[8 * s + 4], x[8 * s + 5]), phi2(x[8 * s + 6], x[8 * s + 7])};
  return __builtin_bit_cast(bf16x8, v);
}
DI float bsel(unsigned m, float a, float b) {
  return __builtin_bit_cast(float, (__builtin_bit_cast(unsigned, a) & ~m) | (__builtin_bit_cast(unsigned, b) & m));
}
DI float fadd(float a, float b) { float r; asm("v_add_f32 %0, %1, %2" : "=v"(r) : "v"(a), "v"(b)); return r; }
DI f32x4 fadd4(f32x4 a, f32x4 b) { return f32x4{fadd(a[0], b[0]), fadd(a[1], b[1]), fadd(a[2], b[2]), fadd(a[3], b[3])}; }
DI float ffma(float a, float b, float c) { float r; asm("v_fma_f32 %0, %1, %2, %3" : "=v"(r) : "v"(a), "v"(b), "v"(c)); return r; }
template <int CTRL, int RM> DI float dpp_add(float v) {
  return fadd(v, __builtin_bit_cast(float, __builtin_amdgcn_update_dpp(0, __builtin_bit_cast(int, v), CTRL, RM, 0xf, true)));
}
DI float wave_sum63(float v) {
  v = dpp_add<0x111, 0xf>(v); v = dpp_add<0x112, 0xf>(v); v = dpp_add<0x114, 0xf>(v); v = dpp_add<0x118, 0xf>(v);
  v = dpp_add<0x142, 0xa>(v); v = dpp_add<0x143, 0xc>(v);
  return v;
}
constexpr unsigned SENT = 0xFFFFFFFFu;
DI unsigned nosent(unsigned v) { return v < SENT ? v : SENT - 1u; }
DI u32x4 nosent4(u32x4 v) { return u32x4{nosent(v[0]), nosent(v[1]), nosent(v[2]), nosent(v[3])}; }
DI unsigned umax3(unsigned a, unsigned b, unsigned c) { unsigned m = a > b ? a : b; return m > c ? m : c; }
#define SB() __builtin_amdgcn_sched_barrier(0)
DI bf16x8 cvt8(const f32x16& x, int s) {
  return pack8(x[8 * s], x[8 * s + 1], x[8 * s + 2], x[8 * s + 3], x[8 * s + 4], x[8 * s + 5], x[8 * s + 6], x[8 * s + 7]);
}
DI float sum8(bf16x8 v, float acc) {
  const bf16x2 ones = {(__bf16)1.0f, (__bf16)1.0f};
  acc = __builtin_amdgcn_fdot2_f32_bf16(__builtin_shufflevector(v, v, 0, 1), ones, acc, false);
  acc = __builtin_amdgcn_fdot2_f32_bf16(__builtin_shufflevector(v, v, 2, 3), ones, acc, false);
  acc = __builtin_amdgcn_fdot2_f32_bf16(__builtin_shufflevector(v, v, 4, 5), ones, acc, false);
  acc = __builtin_amdgcn_fdot2_f32_bf16(__builtin_shufflevector(v, v, 6, 7), ones, acc, false);
  return acc;
}
DI unsigned long long ld_flag(GAS char* ws, unsigned off) {
  return __hip_atomic_load((GAS unsigned long long*)(ws + off), __ATOMIC_RELAXED, __HIP_MEMORY_SCOPE_AGENT);
}
DI void st_flag(GAS char* ws, unsigned off, unsigned long long v) {
  __hip_atomic_store((GAS unsigned long long*)(ws + off), v, __ATOMIC_RELAXED, __HIP_MEMORY_SCOPE_AGENT);
}
DI unsigned long long epoch_issue(const char* wsp) {
  unsigned long long e;
  asm volatile("s_load_dwordx2 %0, %1, 0x0" : "=s"(e) : "s"(wsp));
  return e;
}
DI unsigned long long epoch_wait(unsigned long long e) {
  asm volatile("s_waitcnt lgkmcnt(0)" : "+s"(e) :: "memory");
  return e + 1ull;
}
DI void wait_flags(GAS char* ws, unsigned off, unsigned long long tag) {
  for (unsigned spins = 0; spins < SPIN_CAP; ++spins) {
    const unsigned long long v = ld_flag(ws, off);
    if (__all(v == tag)) break;
    __builtin_amdgcn_s_sleep(PSLEEP);
  }
  asm volatile("" ::: "memory");
}
DI unsigned ld_u32(GAS char* ws, unsigned off) {
  return __hip_atomic_load((GAS unsigned*)(ws + off), __ATOMIC_RELAXED, __HIP_MEMORY_SCOPE_AGENT);
}
DI void wait_written(GAS char* ws, unsigned off) {
  for (unsigned spins = 0; spins < SPIN_CAP; ++spins) {
    const unsigned v = ld_u32(ws, off);
    if (__all(v != SENT)) break;
    __builtin_amdgcn_s_sleep(PSLEEP);
  }
  asm volatile("" ::: "memory");
}

__global__ __launch_bounds__(512, 2) void gated_mix_fused(const u16* __restrict__ x, const u16* __restrict__ wA, const u16* __restrict__ wB,
                                                          const u16* __restrict__ woA, const u16* __restrict__ woB, const u16* __restrict__ gw,
                                                          u16* __restrict__ out, char* __restrict__ wsp
                                                          ) {
  extern __shared__ __attribute__((aligned(16))) char lds[];
  LAS char* L = (LAS char*)lds;
  const int tid = threadIdx.x;
  const int wave = __builtin_amdgcn_readfirstlane(tid >> 6), lane = tid & 63;
  const int b = blockIdx.x, xcd = b & 7, r = b >> 3;
  const int ti = (xcd & 1) * 8 + (r & 7), g = (xcd >> 1) * 4 + (r >> 3);
  const int gg = g & 7;
  const int rt = 4 * xcd + (r & 3), ct = r >> 2;
  GAS char* ws = (GAS char*)wsp;
  asm volatile("" :: "s"(x), "s"(wA), "s"(wB), "s"(woA), "s"(woB), "s"(gw), "s"(out), "s"(wsp));
  unsigned pX = 0, pT0 = 0;
  STR(0); ST(1);

  if (wave >= 4) {
    const int hw = wave - 4;
    const unsigned long long epoch_raw = epoch_issue(wsp);
    __builtin_amdgcn_s_setprio(HPRIO);
    int ro = 0;
    if (ROT == 1) ro = (ti + 2 * g) & 7;
    if (ROT == 2) ro = 2 * ((ti + g) & 1);
    if (ROT == 3) ro = 4 * ((ti + g) & 1);
    if (ROT == 5) ro = ((ti + g) % 3) * 2;
    const u16* w = (g >= 8) ? wB : wA;
    const rsrc_t rsX = __builtin_amdgcn_make_buffer_rsrc((void*)x, 0, S_ * D_ * 2, 0x00020000);
    const rsrc_t rsW = __builtin_amdgcn_make_buffer_rsrc((void*)w, 0, D_ * 3 * D_ * 2, 0x00020000);
    const int rin = lane >> 3, ch = lane & 7;
    const unsigned vx = rin * 1024 + ((ch ^ (4 * (hw & 1) + (rin >> 1))) << 4);
    const unsigned csw = (ch ^ (((rin >> 1) & 1) << 2)) << 4;
    const unsigned vw = rin * 3072 + csw;
    const unsigned sx0 = (ti * 128 + 8 * hw) * 1024;
    const unsigned sw0 = 8 * hw * 3072 + gg * 128;
    auto stage = [&](int s) {
      const unsigned ks = (s + ro) & 7, slot = (s % 3) * 16384;
#pragma unroll
      for (int i = 0; i < 4; ++i) glds(vx, rsX, sx0 + ks * 128 + i * 32768, XR + slot + (hw + 4 * i) * 1024);
#pragma unroll
      for (int i = 0; i < 2; ++i) glds(vw, rsW, sw0 + ks * 196608 + i * 98304 + 1024, WR + slot + (hw + 4 * i) * 1024);
#pragma unroll
      for (int i = 0; i < 2; ++i) glds(vw, rsW, sw0 + ks * 196608 + i * 98304 + 2048, WR + slot + 8192 + (hw + 4 * i) * 1024);
    };
    auto stageq = [&](int s) {
      const unsigned ks = (s + ro) & 7;
#pragma unroll
      for (int i = 0; i < 2; ++i) glds(vw, rsW, sw0 + ks * 196608 + i * 98304, WQ + s * 8192 + (hw + 4 * i) * 1024);
    };
    ST(15);
    stage(0);
    stage(1);
    ST(2);
    WAITV(8);
    const unsigned long long tag = epoch_wait(epoch_raw);
    BAR();
    ST(3);
#pragma unroll
    for (int s = 1; s < 8; ++s) {
      if (s + 1 < 8) { stage(s + 1); WAITV(8); }
      else { stageq(0); stageq(1); stageq(2); stageq(3); WAITV(8); }
      BAR();
    }
    ST(4);
    stageq(4); stageq(5); stageq(6); stageq(7);
    const rsrc_t rsWS = __builtin_amdgcn_make_buffer_rsrc((void*)wsp, 0, WS_END, 0x00020000);
    const rsrc_t rsOA = __builtin_amdgcn_make_buffer_rsrc((void*)woA, 0, D_ * D_ * 2, 0x00020000);
    const rsrc_t rsOB = __builtin_amdgcn_make_buffer_rsrc((void*)woB, 0, D_ * D_ * 2, 0x00020000);
    const unsigned vo = rin * 1024 + csw;
    const unsigned hid = g * 4 + hw;
    u32x4 wob[16];
    const rsrc_t rsG = __builtin_amdgcn_make_buffer_rsrc((void*)gw, 0, D_ * 2, 0x00020000);
    const unsigned grow = ti * 128 + 8 * g + 2 * hw;
    const u32x4 x0 = __builtin_amdgcn_raw_buffer_load_b128(rsX, lane * 16, grow * 1024, 0);
    const u32x4 x1 = __builtin_amdgcn_raw_buffer_load_b128(rsX, lane * 16 + 1024, grow * 1024, 0);
    const u32x4 gv = __builtin_amdgcn_raw_buffer_load_b128(rsG, lane * 16, 0, 0);
    const unsigned foff = WS_FLAGC + (g * 4 + hw) * 128 + (lane & 15) * 8;
    const unsigned long long fc = ld_flag(ws, foff);
    asm volatile("" ::: "memory");
#pragma unroll
    for (int i = 0; i < 16; ++i) {
      wob[i] = __builtin_amdgcn_raw_buffer_load_b128(rsOB, vo, (hw + 4 * i) * 8192 + ct * 128, 0);
    }
    WAITV(20);
    ST(5);
    BAR();
    ST(6);
    {
      const f32x4 p0 = *(LAS f32x4*)(L + SCR + hw * 1024 + lane * 16), p1 = *(LAS f32x4*)(L + SCR + (4 + hw) * 1024 + lane * 16);
      const f32x4 z0 = *(LAS f32x4*)(L + SCR + (8 + hw) * 1024 + (lane & 3) * 16), z1 = *(LAS f32x4*)(L + SCR + (8 + hw) * 1024 + 64 + (lane & 3) * 16);
      const unsigned po = WS_PART + (hid * 16 + ti) * PART_STRIDE;
      st128<16>(nosent4(__builtin_bit_cast(u32x4, fadd4(p0, p1))), rsWS, (lane * 16) + (po));
      if (lane < 4) st128<16>(nosent4(__builtin_bit_cast(u32x4, fadd4(z0, z1))), rsWS, (1024 + lane * 16) + (po));
    }
    ST(7);
#pragma unroll
    for (int i = 0; i < 16; ++i) glds(vo, rsOA, (hw + 4 * i) * 8192 + ct * 128, WOA + (hw + 4 * i) * 1024);
    ST(8);
    {
      float a0 = 0.f, a1 = 0.f;
#pragma unroll
      for (int i = 0; i < 4; ++i) {
        const unsigned ug = gv[i], u0 = x0[i], u1 = x1[i];
        const float gl = __builtin_bit_cast(float, ug << 16), gh = __builtin_bit_cast(float, ug & 0xffff0000u);
        a0 = ffma(__builtin_bit_cast(float, u0 << 16), gl, a0); a0 = ffma(__builtin_bit_cast(float, u0 & 0xffff0000u), gh, a0);
        a1 = ffma(__builtin_bit_cast(float, u1 << 16), gl, a1); a1 = ffma(__builtin_bit_cast(float, u1 & 0xffff0000u), gh, a1);
      }
      a0 = wave_sum63(a0); a1 = wave_sum63(a1);
      const float s0 = __builtin_bit_cast(float, __builtin_amdgcn_readlane(__builtin_bit_cast(int, a0), 63));
      const float s1 = __builtin_bit_cast(float, __builtin_amdgcn_readlane(__builtin_bit_cast(int, a1), 63));
      const float lg = (lane & 1) ? s1 : s0;
      const float gval = __builtin_amdgcn_rcpf(1.0f + __builtin_amdgcn_exp2f(-1.44269504088896340736f * lg));
      if (lane < 2) __hip_atomic_store((GAS unsigned*)(ws + WS_GATE) + grow + lane, __builtin_bit_cast(unsigned, gval), __ATOMIC_RELAXED, __HIP_MEMORY_SCOPE_AGENT);
    }
    if (!__all(fc == tag)) wait_flags(ws, foff, tag);
    const unsigned pb = WS_PART + hid * 16 * PART_STRIDE;
    wait_written(ws, pb + (lane & 15) * PART_STRIDE + 1024 + 60);
    ST(9);
    {
      f32x4 a, z;
      for (unsigned spins = 0; spins < SPIN_CAP; ++spins) {
        u32x4 pv[16];
#pragma unroll
        for (int p = 0; p < 16; ++p) pv[p] = __builtin_amdgcn_raw_buffer_load_b128(rsWS, lane * 16, pb + p * PART_STRIDE, 16);
        const u32x4 zv = __builtin_amdgcn_raw_buffer_load_b128(rsWS, (lane >> 2) * PART_STRIDE + 1024 + (lane & 3) * 16, pb, 16);
        unsigned mx = umax3(zv[0], zv[1], umax3(zv[2], zv[3], 0u));
        a = __builtin_bit_cast(f32x4, pv[0]);
        mx = umax3(pv[0][0], pv[0][1], umax3(pv[0][2], pv[0][3], mx));
#pragma unroll
        for (int p = 1; p < 16; ++p) {
          a += __builtin_bit_cast(f32x4, pv[p]);
          mx = umax3(pv[p][0], pv[p][1], umax3(pv[p][2], pv[p][3], mx));
        }
        z = __builtin_bit_cast(f32x4, zv);
        if (__all(mx != SENT)) break;
      }
#pragma unroll
      for (int m = 4; m <= 32; m <<= 1) {
        z[0] += __shfl_xor(z[0], m); z[1] += __shfl_xor(z[1], m); z[2] += __shfl_xor(z[2], m); z[3] += __shfl_xor(z[3], m);
      }
      const int slot = lane & 31, jh = lane >> 5;
      u32x2 ka = {__builtin_bit_cast(unsigned, cvt2(a[0], a[1])), __builtin_bit_cast(unsigned, cvt2(a[2], a[3]))};
      *(LAS u32x2*)(L + AFR + hw * 1024 + ((slot >> 4) * 32 + (slot & 15)) * 16 + jh * 8) = ka;
      const int q4 = lane & 3;
      u32x2 kz = {__builtin_bit_cast(unsigned, cvt2(z[0], z[1])), __builtin_bit_cast(unsigned, cvt2(z[2], z[3]))};
      *(LAS u32x2*)(L + AFR + hw * 1024 + ((q4 & 1) * 32 + 16 + (lane >> 2)) * 16 + (q4 >> 1) * 8) = kz;
    }
    WAITL0();
    ST(10);
    BAR();
    ST(11);
#pragma unroll
    for (int i = 0; i < 16; ++i) *(LAS u32x4*)(L + WOB + (hw + 4 * i) * 1024 + lane * 16) = wob[i];
    WAITV(0); WAITL0();
    ST(12);
    BAR();
    {
      const int r32 = lane & 31, h = lane >> 5, g1 = (lane >> 4) & 1, q = (lane & 15) >> 2, p = lane & 3;
      const unsigned sw = ((2 * g1 + (p >> 1)) ^ (((q >> 1) & 1) << 2)) << 4;
      const int br2 = hw >> 1, sh = hw & 1;
      const unsigned st2 = 2 * rt + sh;
      const unsigned ob = WS_O + (st2 * 64 + 32 * br2) * 1024;
      for (int i = 0; i < PDELAY; ++i) __builtin_amdgcn_s_sleep(8);
      wait_flags(ws, WS_FLAGB + st2 * 128 + (lane & 15) * 8, tag);
      ST(13);
      unsigned wp0 = ((br2 ? WOB : WOA) + (4 * h + q) * 128 + sw + (p & 1) * 8) ^ (br2 << 6), wp1 = wp0 ^ 64;
      asm volatile("" : "+v"(wp0), "+v"(wp1));
      f32x16 c0 = {}, c1 = {};
      u32x4 of[32];
#pragma unroll
      for (int j = 0; j < OD; ++j) of[j] = __builtin_amdgcn_raw_buffer_load_b128(rsWS, lane * 16, ob + j * 1024, 16);
      const unsigned gvb = ld_u32(ws, WS_GATE + (st2 * 32 + r32) * 4);
      {
        bf16x8 a0[3], a1[3];
        a0[0] = ldstr(L, wp0, wp0 + 1024); a1[0] = ldstr(L, wp1, wp1 + 1024);
        a0[1] = ldstr(L, wp0 + 2048, wp0 + 2048 + 1024); a1[1] = ldstr(L, wp1 + 2048, wp1 + 2048 + 1024);
#pragma unroll
        for (int j = 0; j < 32; ++j) {
          const unsigned o = (j + 2) * 2048;
          const bf16x8 bf = __builtin_bit_cast(bf16x8, of[j]);
          SB();
          c0 = mfma(a0[j % 3], bf, c0);
          SB();
          if (j + OD < 32) of[j + OD] = __builtin_amdgcn_raw_buffer_load_b128(rsWS, lane * 16, ob + (j + OD) * 1024, 16);
          if (j + 2 < 32) a0[(j + 2) % 3] = ldstr(L, wp0 + o, wp0 + o + 1024);
          SB();
          c1 = mfma(a1[j % 3], bf, c1);
          SB();
          if (j + 2 < 32) a1[(j + 2) % 3] = ldstr(L, wp1 + o, wp1 + o + 1024);
        }
        SB();
      }
      ST(14);
      __builtin_amdgcn_s_setprio(0);
      const float gv = __builtin_bit_cast(float, gvb);
      const float gs = br2 ? 1.0f - gv : gv;
  #pragma unroll
      for (int qq = 0; qq < 4; ++qq) {
        const f32x4 v = {c1[4 * qq] * gs, c1[4 * qq + 1] * gs, c1[4 * qq + 2] * gs, c1[4 * qq + 3] * gs};
        *(LAS f32x4*)(L + EXC + hw * 4096 + qq * 1024 + lane * 16) = v;
      }
      WAITL0();
      BAR();
      if (b == 0 && hw == 0 && lane == 0) st_flag(ws, WS_EPOCH, tag);
      unsigned pk[8];
  #pragma unroll
      for (int qq = 0; qq < 4; ++qq) {
        const f32x4 v = *(LAS f32x4*)(L + EXC + (hw ^ 2) * 4096 + qq * 1024 + lane * 16);
        pk[2 * qq] = __builtin_bit_cast(unsigned, cvt2(c0[4 * qq] * gs + v[0], c0[4 * qq + 1] * gs + v[1]));
        pk[2 * qq + 1] = __builtin_bit_cast(unsigned, cvt2(c0[4 * qq + 2] * gs + v[2], c0[4 * qq + 3] * gs + v[3]));
      }
      const rsrc_t rsO = __builtin_amdgcn_make_buffer_rsrc((void*)out, 0, S_ * D_ * 2, 0x00020000);
      const unsigned orow = (st2 * 32 + r32) * 1024 + (64 * ct + 32 * br2) * 2 + h * 16;
  #pragma unroll
      for (int qq = 0; qq < 4; qq += 2) {
        const auto s0 = __builtin_amdgcn_permlane32_swap(pk[2 * qq], pk[2 * qq + 2], false, false);
        const auto s1 = __builtin_amdgcn_permlane32_swap(pk[2 * qq + 1], pk[2 * qq + 3], false, false);
        const u32x4 v = {s0[0], s1[0], s0[1], s1[1]};
        st128<0>(v, rsO, orow + qq * 16);
      }
      STR(16); STFLUSH();
    }
    return;
  }

  const unsigned long long epoch_raw = epoch_issue(wsp);
  const int wr = wave >> 1, wc = wave & 1;
  const int r32 = lane & 31, h = lane >> 5;
  const int g1 = (lane >> 4) & 1, q = (lane & 15) >> 2, p = lane & 3;
  const int own = wave, oth = wave ^ 1;
  const rsrc_t rsWS = __builtin_amdgcn_make_buffer_rsrc((void*)wsp, 0, WS_END, 0x00020000);
  unsigned ctouch = 0;
  unsigned otouch = 0;
  {
    const u32x4 sent = {SENT, SENT, SENT, SENT};
    const unsigned po = WS_PART + ((g * 4 + wave) * 16 + ti) * PART_STRIDE;
    st128<16>(sent, rsWS, lane * 16 + po);
    if (lane < 4) st128<16>(sent, rsWS, 1024 + lane * 16 + po);
  }
  const unsigned fx = (h ^ ((r32 >> 1) & 7)) << 4;
  unsigned xo0 = XR + (own * 32 + r32) * 128 + fx, xt0 = XR + (oth * 32 + r32) * 128 + fx;
  const unsigned sw = ((2 * g1 + (p >> 1)) ^ (((q >> 1) & 1) << 2)) << 4;
  const unsigned wn0 = (8 * h + q) * 128 + sw + (p & 1) * 8;
  unsigned wkv0 = WR + (wn0 ^ (wc << 6));
  asm volatile("" : "+v"(wkv0));
  unsigned xoa[4], xta[4];
#pragma unroll
  for (int k = 0; k < 4; ++k) { xoa[k] = xo0 ^ (k << 5); xta[k] = xt0 ^ (k << 5); asm volatile("" : "+v"(xoa[k]), "+v"(xta[k])); }
  const unsigned long long tag = epoch_wait(epoch_raw);

  bf16x8 xr[32];
  f32x16 kO = {}, kT = {}, vO = {}, vT = {};
  {
    BAR();
    ST(3);
    bf16x8 xt = ldsr128(L, xta[0]), wk = ldstr(L, wkv0, wkv0 + 512), wv = ldstr(L, wkv0 + 8192, wkv0 + 8192 + 512);
    xr[0] = ldsr128(L, xoa[0]);
#pragma unroll
    for (int t = 0; t < 32; ++t) {
      bf16x8 nxt, nwk, nwv;
      const int s1 = (t + 1) >> 2, k1 = (t + 1) & 3;
      const unsigned slot = (s1 % 3) * 16384;
      if (t + 1 < 32 && k1 == 0) {
        WAITL0();
        if (s1 == CLR_STAGE) { WAITV(0); asm volatile("" :: "v"(ctouch), "v"(otouch));
          if (lane == 0) st_flag(ws, WS_FLAGC + (g * 4 + wave) * 128 + ti * 8, tag); }
        BAR();
      }
      SB();
      kO = mfma(xr[t], wk, kO);
      SB();
      if (t + 1 < 32) {
        xr[t + 1] = ldsr128(L, xoa[k1] + slot);
        nwk = ldstr(L, wkv0 + slot + k1 * 2048, wkv0 + slot + k1 * 2048 + 512);
      }
      SB();
      kT = mfma(xt, wk, kT);
      SB();
      if (t + 1 < 32) {
        nxt = ldsr128(L, xta[k1] + slot);
        nwv = ldstr(L, wkv0 + slot + 8192 + k1 * 2048, wkv0 + slot + 8192 + k1 * 2048 + 512);
      }
      SB();
      vO = mfma(xr[t], wv, vO);
      vT = mfma(xt, wv, vT);
      SB();
      if (t + 1 < 32) { xt = nxt; wk = nwk; wv = nwv; }
    }
  }
  ST(4);
  {
    f32x16 kv = {};
    float zs = 0.f;
#pragma unroll
    for (int s = 0; s < 2; ++s) {
      const bf16x8 va = cvt8(vO, s), vb = cvt8(vT, s);
      const bf16x8 pa = phi8(kO, s);
      kv = mfma(pa, va, kv);
      const bf16x8 pb = phi8(kT, s);
      kv = mfma(pb, vb, kv);
      zs = sum8(pa, zs); zs = sum8(pb, zs);
    }
    {
      const unsigned zb = __builtin_bit_cast(unsigned, zs);
      const auto sw2 = __builtin_amdgcn_permlane32_swap(zb, zb, false, false);
      zs = __builtin_bit_cast(float, (unsigned)sw2[0]) + __builtin_bit_cast(float, (unsigned)sw2[1]);
    }
    const unsigned gm = g1 ? 0xFFFFFFFFu : 0u;
    f32x4 lo, hi;
#pragma unroll
    for (int j = 0; j < 4; ++j) { lo[j] = bsel(gm, kv[j], kv[8 + j]); hi[j] = bsel(gm, kv[4 + j], kv[12 + j]); }
    const int hh = 2 * wc + g1;
    const unsigned a = SCR + (4 * wr + hh) * 1024 + (h * 16 + (lane & 15)) * 16;
    *(LAS f32x4*)(L + a) = lo;
    *(LAS f32x4*)(L + a + 512) = hi;
    if (h == 0) *(LAS float*)(L + SCR + (8 + hh) * 1024 + wr * 64 + (lane & 15) * 4) = zs;
  }
  WAITL0();
  ST(5);
  BAR();
  ST(6);
  f32x16 qa = {}, qb = {};
  {
    unsigned wq0 = WQ + wn0, wq1 = WQ + (wn0 ^ 64);
    asm volatile("" : "+v"(wq0), "+v"(wq1));
    bf16x8 a0[3], a1[3];
    a0[0] = ldstr(L, wq0, wq0 + 512); a1[0] = ldstr(L, wq1, wq1 + 512);
    a0[1] = ldstr(L, wq0 + 2048, wq0 + 2048 + 512); a1[1] = ldstr(L, wq1 + 2048, wq1 + 2048 + 512);
#pragma unroll
    for (int t = 0; t < 32; ++t) {
      const unsigned o = (t + 2) * 2048;
      SB();
      qa = mfma(a0[t % 3], xr[t], qa);
      SB();
      if (t + 2 < 32) a0[(t + 2) % 3] = ldstr(L, wq0 + o, wq0 + o + 512);
      SB();
      qb = mfma(a1[t % 3], xr[t], qb);
      SB();
      if (t + 2 < 32) a1[(t + 2) % 3] = ldstr(L, wq1 + o, wq1 + o + 512);
    }
    SB();
  }
  ST(7);
  bf16x8 pq[4];
  pq[0] = phi8(qa, 0); pq[1] = phi8(qa, 1); pq[2] = phi8(qb, 0); pq[3] = phi8(qb, 1);
  WAITL0();
  ST(8);
  BAR();
  ST(9);
  {
    const unsigned st = 4 * ti + own;
    bf16x8 af[4];
#pragma unroll
    for (int hh = 0; hh < 4; ++hh) af[hh] = ldsr128(L, AFR + hh * 1024 + lane * 16);
    f32x16 d[4];
#pragma unroll
    for (int hh = 0; hh < 4; ++hh) { d[hh] = f32x16{}; d[hh] = mfma(af[hh], pq[hh], d[hh]); }
#pragma unroll
    for (int hh = 0; hh < 4; ++hh) {
      const float inv = __builtin_amdgcn_rcpf(d[hh][8] + 1e-6f);
      const bf16x8 o = pack8(d[hh][0] * inv, d[hh][1] * inv, d[hh][2] * inv, d[hh][3] * inv, d[hh][4] * inv, d[hh][5] * inv, d[hh][6] * inv, d[hh][7] * inv);
      st128<16>(__builtin_bit_cast(u32x4, o), rsWS, (lane * 16) + (WS_O + (st * 64 + 4 * g + hh) * 1024));
    }
  }
  ST(10);
  BAR();
  WAITV(0);
  if (lane == 0) st_flag(ws, WS_FLAGB + (4 * ti + own) * 128 + g * 8, tag);
  ST(11);
  STR(16); STFLUSH();
}

extern "C" void kernel_launch(void* const* d_in, const int* in_sizes, int n_in, void* d_out, int out_size, void* d_ws, size_t ws_size,
                              hipStream_t stream) {
  (void)in_sizes; (void)n_in; (void)out_size;
  const u16* x = (const u16*)d_in[0];
  const u16* wA = (const u16*)d_in[1];
  const u16* woA = (const u16*)d_in[2];
  const u16* wB = (const u16*)d_in[4];
  const u16* woB = (const u16*)d_in[5];
  const u16* gw = (const u16*)d_in[6];
  (void)hipFuncSetAttribute((const void*)gated_mix_fused, hipFuncAttributeMaxDynamicSharedMemorySize, LDS_BYTES);
  gated_mix_fused<<<256, 512, LDS_BYTES, stream>>>(x, wA, wB, woA, woB, gw, (u16*)d_out, (char*)d_ws + ((ws_size / 2) & ~(size_t)0x1FFFFF));
}
```

```cpp
#include <hip/hip_runtime.h>
#include <hip/hip_bf16.h>
#include <stdint.h>

#define LAS __attribute__((address_space(3)))
#define GAS __attribute__((address_space(1)))
#define DI __device__ __forceinline__

typedef unsigned short u16;
typedef __bf16 bf16x8 __attribute__((ext_vector_type(8)));
typedef __bf16 bf16x4 __attribute__((ext_vector_type(4)));
typedef __bf16 bf16x2 __attribute__((ext_vector_type(2)));
typedef float f32x16 __attribute__((ext_vector_type(16)));
typedef float f32x4 __attribute__((ext_vector_type(4)));
typedef float f32x2 __attribute__((ext_vector_type(2)));
typedef unsigned u32x4 __attribute__((ext_vector_type(4)));
typedef unsigned u32x2 __attribute__((ext_vector_type(2)));
typedef __amdgpu_buffer_rsrc_t rsrc_t;

#define ROT 6
#define CODE_PREFETCH 20480
#define CLR_STAGE 3
#define HPRIO 3
#define OD 10
#define PSLEEP 4
#define PDELAY 4
#define PDELAY1 1

constexpr int S_ = 2048, D_ = 512;
constexpr unsigned XR = 0;
constexpr unsigned WR = 49152;
constexpr unsigned WQ = 98304;
constexpr unsigned WOA = 0;
constexpr unsigned WOB = 98304;
constexpr unsigned SCR = 81920;
constexpr unsigned AFR = SCR + 12288;
constexpr unsigned EXC = SCR;
constexpr unsigned LDS_BYTES = 163840;
constexpr unsigned WS_EPOCH = 0;
constexpr unsigned WS_FLAGC = 4096;
constexpr unsigned WS_FLAGB = WS_FLAGC + 1024 * 128;
constexpr unsigned WS_GATE = WS_FLAGB + 1024 * 128;
constexpr unsigned WS_PART = WS_GATE + 8192;
constexpr unsigned PART_STRIDE = 640;
constexpr unsigned WS_O = WS_PART + 1024 * PART_STRIDE;
constexpr unsigned WS_END = WS_O + 64 * 64 * 1024;
constexpr unsigned SPIN_CAP = 1u << 18;

#define ST(i) do {} while (0)
#define STR(i) do {} while (0)
#define STFLUSH() do {} while (0)

DI void glds(unsigned voff, rsrc_t rs, unsigned soff, unsigned ldsaddr) {
  asm volatile("s_mov_b32 m0, %0\n\ts_nop 0\n\tbuffer_load_dwordx4 %1, %2, %3 offen lds"
               :: "s"(ldsaddr), "v"(voff), "s"(rs), "s"(soff) : "memory");
}
template <int AUX> DI void st128(u32x4 v, rsrc_t rs, unsigned off) { __builtin_amdgcn_raw_buffer_store_b128(v, rs, off, 0, AUX); }
#define WAITV(n) asm volatile("s_waitcnt vmcnt(" #n ")" ::: "memory")
#define WAITL0() asm volatile("s_waitcnt lgkmcnt(0)" ::: "memory")
#define BAR() asm volatile("s_barrier" ::: "memory")

DI f32x16 mfma(bf16x8 a, bf16x8 b, f32x16 c) { return __builtin_amdgcn_mfma_f32_32x32x16_bf16(a, b, c, 0, 0, 0); }
DI bf16x8 ldsr128(LAS char* L, unsigned a) { return *(LAS bf16x8*)(L + a); }
DI bf16x8 ldstr(LAS char* L, unsigned a0, unsigned a1) {
  bf16x4 lo = __builtin_amdgcn_ds_read_tr16_b64_v4bf16((LAS bf16x4*)(L + a0));
  bf16x4 hi = __builtin_amdgcn_ds_read_tr16_b64_v4bf16((LAS bf16x4*)(L + a1));
  return __builtin_shufflevector(lo, hi, 0, 1, 2, 3, 4, 5, 6, 7);
}
DI bf16x2 cvt2(float a, float b) { f32x2 p = {a, b}; return __builtin_convertvector(p, bf16x2); }
DI bf16x8 pack8(float a0, float a1, float a2, float a3, float a4, float a5, float a6, float a7) {
  bf16x2 p0 = cvt2(a0, a1), p1 = cvt2(a2, a3), p2 = cvt2(a4, a5), p3 = cvt2(a6, a7);
  bf16x4 lo = __builtin_shufflevector(p0, p1, 0, 1, 2, 3), hi = __builtin_shufflevector(p2, p3, 0, 1, 2, 3);
  return __builtin_shufflevector(lo, hi, 0, 1, 2, 3, 4, 5, 6, 7);
}
DI unsigned phi2(float a, float b) {
  const f32x2 t = {a, b};
  const f32x2 u = t * 1.44269504088896340736f;
  const f32x2 e = {__builtin_amdgcn_fmed3f(__builtin_amdgcn_exp2f(u[0]), 0.0f, 1.0f), __builtin_amdgcn_fmed3f(__builtin_amdgcn_exp2f(u[1]), 0.0f, 1.0f)};
  const int ia = __builtin_bit_cast(int, a), ib = __builtin_bit_cast(int, b);
  const f32x2 m = {__builtin_bit_cast(float, ia > 0 ? ia : 0), __builtin_bit_cast(float, ib > 0 ? ib : 0)};
  const f32x2 r = (m + e) + 1e-6f;
  return __builtin_bit_cast(unsigned, __builtin_convertvector(r, bf16x2));
}
DI bf16x8 phi8(const f32x16& x, int s) {
  const u32x4 v = {phi2(x[8 * s], x[8 * s + 1]), phi2(x[8 * s + 2], x[8 * s + 3]), phi2(x[8 * s + 4], x[8 * s + 5]), phi2(x[8 * s + 6], x[8 * s + 7])};
  return __builtin_bit_cast(bf16x8, v);
}
DI float bsel(unsigned m, float a, float b) {
  const unsigned ua = __builtin_bit_cast(unsigned, a), ub = __builtin_bit_cast(unsigned, b);
  return __builtin_bit_cast(float, (unsigned)__builtin_amdgcn_bitop3_b32(m, ub, ua, 0xCA));
}
DI float fadd(float a, float b) { float r; asm("v_add_f32 %0, %1, %2" : "=v"(r) : "v"(a), "v"(b)); return r; }
DI f32x4 fadd4(f32x4 a, f32x4 b) { return f32x4{fadd(a[0], b[0]), fadd(a[1], b[1]), fadd(a[2], b[2]), fadd(a[3], b[3])}; }
DI float ffma(float a, float b, float c) { float r; asm("v_fma_f32 %0, %1, %2, %3" : "=v"(r) : "v"(a), "v"(b), "v"(c)); return r; }
template <int CTRL, int RM> DI float dpp_add(float v) {
  return fadd(v, __builtin_bit_cast(float, __builtin_amdgcn_update_dpp(0, __builtin_bit_cast(int, v), CTRL, RM, 0xf, true)));
}
DI float wave_sum63(float v) {
  v = dpp_add<0x111, 0xf>(v); v = dpp_add<0x112, 0xf>(v); v = dpp_add<0x114, 0xf>(v); v = dpp_add<0x118, 0xf>(v);
  v = dpp_add<0x142, 0xa>(v); v = dpp_add<0x143, 0xc>(v);
  return v;
}
DI float sum_stride4(float v) {
  v += __builtin_bit_cast(float, __builtin_amdgcn_update_dpp(0, __builtin_bit_cast(int, v), 0x124, 0xf, 0xf, true));
  v += __builtin_bit_cast(float, __builtin_amdgcn_update_dpp(0, __builtin_bit_cast(int, v), 0x128, 0xf, 0xf, true));
  { const unsigned u = __builtin_bit_cast(unsigned, v); const auto t = __builtin_amdgcn_permlane16_swap(u, u, false, false);
    v = __builtin_bit_cast(float, (unsigned)t[0]) + __builtin_bit_cast(float, (unsigned)t[1]); }
  { const unsigned u = __builtin_bit_cast(unsigned, v); const auto t = __builtin_amdgcn_permlane32_swap(u, u, false, false);
    v = __builtin_bit_cast(float, (unsigned)t[0]) + __builtin_bit_cast(float, (unsigned)t[1]); }
  return v;
}
constexpr unsigned SENT = 0xFFFFFFFFu;
DI unsigned nosent(unsigned v) { return v < SENT ? v : SENT - 1u; }
DI u32x4 nosent4(u32x4 v) { return u32x4{nosent(v[0]), nosent(v[1]), nosent(v[2]), nosent(v[3])}; }
DI unsigned umax3(unsigned a, unsigned b, unsigned c) { unsigned m = a > b ? a : b; return m > c ? m : c; }
#define SB() __builtin_amdgcn_sched_barrier(0)
DI bf16x8 cvt8(const f32x16& x, int s) {
  return pack8(x[8 * s], x[8 * s + 1], x[8 * s + 2], x[8 * s + 3], x[8 * s + 4], x[8 * s + 5], x[8 * s + 6], x[8 * s + 7]);
}
DI float sum8(bf16x8 v, float acc) {
  const bf16x2 ones = {(__bf16)1.0f, (__bf16)1.0f};
  acc = __builtin_amdgcn_fdot2_f32_bf16(__builtin_shufflevector(v, v, 0, 1), ones, acc, false);
  acc = __builtin_amdgcn_fdot2_f32_bf16(__builtin_shufflevector(v, v, 2, 3), ones, acc, false);
  acc = __builtin_amdgcn_fdot2_f32_bf16(__builtin_shufflevector(v, v, 4, 5), ones, acc, false);
  acc = __builtin_amdgcn_fdot2_f32_bf16(__builtin_shufflevector(v, v, 6, 7), ones, acc, false);
  return acc;
}
DI unsigned long long ld_flag(GAS char* ws, unsigned off) {
  return __hip_atomic_load((GAS unsigned long long*)(ws + off), __ATOMIC_RELAXED, __HIP_MEMORY_SCOPE_AGENT);
}
DI void st_flag(GAS char* ws, unsigned off, unsigned long long v) {
  __hip_atomic_store((GAS unsigned long long*)(ws + off), v, __ATOMIC_RELAXED, __HIP_MEMORY_SCOPE_AGENT);
}
DI unsigned long long epoch_issue(const char* wsp) {
  unsigned long long e;
  asm volatile("s_load_dwordx2 %0, %1, 0x0" : "=s"(e) : "s"(wsp));
  return e;
}
DI unsigned long long epoch_wait(unsigned long long e) {
  asm volatile("s_waitcnt lgkmcnt(0)" : "+s"(e) :: "memory");
  return e + 1ull;
}
DI void wait_flags(GAS char* ws, unsigned off, unsigned long long tag) {
  for (unsigned spins = 0; spins < SPIN_CAP; ++spins) {
    const unsigned long long v = ld_flag(ws, off);
    if (__all(v == tag)) break;
    __builtin_amdgcn_s_sleep(PSLEEP);
  }
  asm volatile("" ::: "memory");
}
DI unsigned ld_u32(GAS char* ws, unsigned off) {
  return __hip_atomic_load((GAS unsigned*)(ws + off), __ATOMIC_RELAXED, __HIP_MEMORY_SCOPE_AGENT);
}

__global__ __launch_bounds__(512, 2) void gated_mix_fused(const u16* __restrict__ x, const u16* __restrict__ wA, const u16* __restrict__ wB,
                                                          const u16* __restrict__ woA, const u16* __restrict__ woB, const u16* __restrict__ gw,
                                                          u16* __restrict__ out, char* __restrict__ wsp
                                                          ) {
  extern __shared__ __attribute__((aligned(16))) char lds[];
  LAS char* L = (LAS char*)lds;
  const int tid = threadIdx.x;
  const int wave = __builtin_amdgcn_readfirstlane(tid >> 6), lane = tid & 63;
  const int b = blockIdx.x, xcd = b & 7, r = b >> 3;
  const int ti = (xcd & 1) * 8 + (r & 7), g = (xcd >> 1) * 4 + (r >> 3);
  const int gg = g & 7;
  const int rt = 4 * xcd + (r & 3), ct = r >> 2;
  GAS char* ws = (GAS char*)wsp;
  asm volatile("" :: "s"(x), "s"(wA), "s"(wB), "s"(woA), "s"(woB), "s"(gw), "s"(out), "s"(wsp));
  STR(0); ST(1);
  unsigned long long pc;
  asm volatile("s_getpc_b64 %0" : "=s"(pc));
  pc &= ~255ull;

  if (wave < 4) {
    const unsigned long long epoch_raw = epoch_issue(wsp);
    const int wr = wave >> 1, wc = wave & 1;
    const int r32 = lane & 31, h = lane >> 5;
    const int g1 = (lane >> 4) & 1, q = (lane & 15) >> 2, p = lane & 3;
    const int own = wave, oth = wave ^ 1;
    const rsrc_t rsWS = __builtin_amdgcn_make_buffer_rsrc((void*)wsp, 0, WS_END, 0x00020000);
    unsigned ctouch = 0;
    {
      const unsigned off = (wave * 64 + lane) * 128;
      if (off < CODE_PREFETCH) { const char* a = (const char*)pc + off; asm volatile("global_load_dword %0, %1, off" : "=v"(ctouch) : "v"(a) : "memory"); }
    }
    {
      const u32x4 sent = {SENT, SENT, SENT, SENT};
      const unsigned po = WS_PART + ((g * 4 + wave) * 16 + ti) * PART_STRIDE;
      if (lane < 36) st128<16>(sent, rsWS, lane * 16 + po);
    }
    const unsigned fx = (h ^ ((r32 >> 1) & 7)) << 4;
    unsigned xo0 = XR + (own * 32 + r32) * 128 + fx, xt0 = XR + (oth * 32 + r32) * 128 + fx;
    const unsigned sw = ((2 * g1 + (p >> 1)) ^ (((q >> 1) & 1) << 2)) << 4;
    const unsigned wn0 = (8 * h + q) * 128 + sw + (p & 1) * 8;
    unsigned wkv0 = WR + (wn0 ^ (wc << 6));
    asm volatile("" : "+v"(wkv0));
    unsigned xoa[4], xta[4];
#pragma unroll
    for (int k = 0; k < 4; ++k) { xoa[k] = xo0 ^ (k << 5); xta[k] = xt0 ^ (k << 5); asm volatile("" : "+v"(xoa[k]), "+v"(xta[k])); }
    const unsigned long long tag = epoch_wait(epoch_raw);

    bf16x8 xr[32];
    f32x16 kO = {}, kT = {}, vO = {}, vT = {};
    {
      BAR();
      ST(3);
      bf16x8 xt = ldsr128(L, xta[0]), wk = ldstr(L, wkv0, wkv0 + 512), wv = ldstr(L, wkv0 + 8192, wkv0 + 8192 + 512);
      xr[0] = ldsr128(L, xoa[0]);
#pragma unroll
      for (int t = 0; t < 32; ++t) {
        bf16x8 nxt, nwk, nwv;
        const int s1 = (t + 1) >> 2, k1 = (t + 1) & 3;
        const unsigned slot = (s1 % 3) * 16384;
        if (t + 1 < 32 && k1 == 0) {
          WAITL0();
          if (s1 == CLR_STAGE) {
            WAITV(0);
            asm volatile("" :: "v"(ctouch));
            if (lane == 0) st_flag(ws, WS_FLAGC + (g * 4 + wave) * 128 + ti * 8, tag);
          }
          BAR();
        }
        SB();
        kO = mfma(xr[t], wk, kO);
        SB();
        if (t + 1 < 32) {
          xr[t + 1] = ldsr128(L, xoa[k1] + slot);
          nwk = ldstr(L, wkv0 + slot + k1 * 2048, wkv0 + slot + k1 * 2048 + 512);
        }
        SB();
        kT = mfma(xt, wk, kT);
        SB();
        if (t + 1 < 32) {
          nxt = ldsr128(L, xta[k1] + slot);
          nwv = ldstr(L, wkv0 + slot + 8192 + k1 * 2048, wkv0 + slot + 8192 + k1 * 2048 + 512);
        }
        SB();
        vO = mfma(xr[t], wv, vO);
        vT = mfma(xt, wv, vT);
        SB();
        if (t + 1 < 32) { xt = nxt; wk = nwk; wv = nwv; }
      }
    }
    ST(4);
    {
      f32x16 kv = {};
      float zs = 0.f;
#pragma unroll
      for (int s = 0; s < 2; ++s) {
        const bf16x8 va = cvt8(vO, s), vb = cvt8(vT, s);
        const bf16x8 pa = phi8(kO, s);
        kv = mfma(pa, va, kv);
        const bf16x8 pb = phi8(kT, s);
        kv = mfma(pb, vb, kv);
        zs = sum8(pa, zs); zs = sum8(pb, zs);
      }
      {
        const unsigned zb = __builtin_bit_cast(unsigned, zs);
        const auto sw2 = __builtin_amdgcn_permlane32_swap(zb, zb, false, false);
        zs = __builtin_bit_cast(float, (unsigned)sw2[0]) + __builtin_bit_cast(float, (unsigned)sw2[1]);
      }
      const unsigned gm = g1 ? 0xFFFFFFFFu : 0u;
      f32x4 lo, hi;
#pragma unroll
      for (int j = 0; j < 4; ++j) { lo[j] = bsel(gm, kv[j], kv[8 + j]); hi[j] = bsel(gm, kv[4 + j], kv[12 + j]); }
      const int hh = 2 * wc + g1;
      const unsigned a = SCR + (4 * wr + hh) * 1024 + (h * 16 + (lane & 15)) * 16;
      *(LAS f32x4*)(L + a) = lo;
      *(LAS f32x4*)(L + a + 512) = hi;
      if (h == 0) *(LAS float*)(L + SCR + (8 + hh) * 1024 + wr * 64 + (lane & 15) * 4) = zs;
    }
    WAITL0();
    ST(5);
    BAR();
    ST(6);
    f32x16 qa = {}, qb = {};
    {
      unsigned wq0 = WQ + wn0, wq1 = WQ + (wn0 ^ 64);
      asm volatile("" : "+v"(wq0), "+v"(wq1));
      bf16x8 a0[3], a1[3];
      a0[0] = ldstr(L, wq0, wq0 + 512); a1[0] = ldstr(L, wq1, wq1 + 512);
      a0[1] = ldstr(L, wq0 + 2048, wq0 + 2048 + 512); a1[1] = ldstr(L, wq1 + 2048, wq1 + 2048 + 512);
#pragma unroll
      for (int t = 0; t < 32; ++t) {
        const unsigned o = (t + 2) * 2048;
        SB();
        qa = mfma(a0[t % 3], xr[t], qa);
        SB();
        if (t + 2 < 32) a0[(t + 2) % 3] = ldstr(L, wq0 + o, wq0 + o + 512);
        SB();
        qb = mfma(a1[t % 3], xr[t], qb);
        SB();
        if (t + 2 < 32) a1[(t + 2) % 3] = ldstr(L, wq1 + o, wq1 + o + 512);
      }
      SB();
    }
    ST(7);
    bf16x8 pq[4];
    pq[0] = phi8(qa, 0); pq[1] = phi8(qa, 1); pq[2] = phi8(qb, 0); pq[3] = phi8(qb, 1);
    asm volatile("" : "+v"(pq[0]), "+v"(pq[1]), "+v"(pq[2]), "+v"(pq[3]));
    WAITL0();
    ST(8);
    BAR();
    ST(9);
    {
      const unsigned st = 4 * ti + own;
      bf16x8 af[4];
#pragma unroll
      for (int hh = 0; hh < 4; ++hh) af[hh] = ldsr128(L, AFR + hh * 1024 + lane * 16);
      f32x16 d[4];
#pragma unroll
      for (int hh = 0; hh < 4; ++hh) { d[hh] = f32x16{}; d[hh] = mfma(af[hh], pq[hh], d[hh]); }
#pragma unroll
      for (int hh = 0; hh < 4; ++hh) {
        const float inv = __builtin_amdgcn_rcpf(d[hh][8] + 1e-6f);
        const u32x4 o = __builtin_bit_cast(u32x4, pack8(d[hh][0] * inv, d[hh][1] * inv, d[hh][2] * inv, d[hh][3] * inv, d[hh][4] * inv, d[hh][5] * inv, d[hh][6] * inv, d[hh][7] * inv));
        st128<16>(o, rsWS, lane * 16 + WS_O + (st * 64 + 4 * g + hh) * 1024);
      }
    }
    ST(10);
    BAR();
    WAITV(0);
    if (lane == 0) st_flag(ws, WS_FLAGB + (4 * ti + own) * 128 + g * 8, tag);
    ST(11);
    STR(16); STFLUSH();
    return;
  }

  const int hw = wave - 4;
  const unsigned long long epoch_raw = epoch_issue(wsp);
  __builtin_amdgcn_s_setprio(HPRIO);
  int ro = 0;
  if (ROT == 1) ro = (ti + 2 * g) & 7;
  if (ROT == 2) ro = 2 * ((ti + g) & 1);
  if (ROT == 3) ro = 4 * ((ti + g) & 1);
  if (ROT == 5) ro = ((ti + g) % 3) * 2;
  if (ROT == 6) ro = 2 * ((ti + g) & 3);
  const u16* w = (g >= 8) ? wB : wA;
  const rsrc_t rsX = __builtin_amdgcn_make_buffer_rsrc((void*)x, 0, S_ * D_ * 2, 0x00020000);
  const rsrc_t rsW = __builtin_amdgcn_make_buffer_rsrc((void*)w, 0, D_ * 3 * D_ * 2, 0x00020000);
  const int rin = lane >> 3, ch = lane & 7;
  const unsigned vx = rin * 1024 + ((ch ^ (4 * (hw & 1) + (rin >> 1))) << 4);
  const unsigned csw = (ch ^ (((rin >> 1) & 1) << 2)) << 4;
  const unsigned vw = rin * 3072 + csw;
  const unsigned sx0 = (ti * 128 + 8 * hw) * 1024;
  const unsigned sw0 = 8 * hw * 3072 + gg * 128;
  auto stage = [&](int s) {
    const unsigned ks = (s + ro) & 7, slot = (s % 3) * 16384;
#pragma unroll
    for (int i = 0; i < 4; ++i) glds(vx, rsX, sx0 + ks * 128 + i * 32768, XR + slot + (hw + 4 * i) * 1024);
#pragma unroll
    for (int i = 0; i < 2; ++i) glds(vw, rsW, sw0 + ks * 196608 + i * 98304 + 1024, WR + slot + (hw + 4 * i) * 1024);
#pragma unroll
    for (int i = 0; i < 2; ++i) glds(vw, rsW, sw0 + ks * 196608 + i * 98304 + 2048, WR + slot + 8192 + (hw + 4 * i) * 1024);
  };
  auto stageq = [&](int s) {
    const unsigned ks = (s + ro) & 7;
#pragma unroll
    for (int i = 0; i < 2; ++i) glds(vw, rsW, sw0 + ks * 196608 + i * 98304, WQ + s * 8192 + (hw + 4 * i) * 1024);
  };
  ST(15);
  unsigned wtouch = 0;
  auto wo_touch = [&]() {
    const unsigned row = r * 32 + hw * 8 + (lane >> 3);
    const char* a = (const char*)((row & 512) ? woB : woA) + (row & 511) * 1024 + (lane & 7) * 128;
    asm volatile("global_load_dword %0, %1, off" : "=v"(wtouch) : "v"(a) : "memory");
  };
  stage(0);
  stage(1);
  ST(2);
  WAITV(8);
  const unsigned long long tag = epoch_wait(epoch_raw);
  BAR();
  ST(3);
  const rsrc_t rsG = __builtin_amdgcn_make_buffer_rsrc((void*)gw, 0, D_ * 2, 0x00020000);
  const unsigned grow = ti * 128 + 8 * g + 2 * hw;
  u32x4 x0, x1, gv;
  const unsigned foff = WS_FLAGC + (g * 4 + hw) * 128 + (lane & 15) * 8;
  u32x2 fcv;
#pragma unroll
  for (int s = 1; s < 8; ++s) {
    if (s < 6) { stage(s + 1); WAITV(8); }
    else if (s == 6) {
      stage(7);
      asm volatile("buffer_load_dwordx4 %0, %4, %5, %7 offen\n\tbuffer_load_dwordx4 %1, %4, %5, %7 offen offset:1024\n\tbuffer_load_dwordx4 %2, %4, %6, 0 offen\n\t"
                   "global_load_dwordx2 %3, %8, %9 sc1"
                   : "=&v"(x0), "=&v"(x1), "=&v"(gv), "=&v"(fcv) : "v"(lane * 16), "s"(rsX), "s"(rsG), "s"(grow * 1024), "v"(foff), "s"(wsp) : "memory");
      WAITV(12);
    } else {
      stageq(0); stageq(1); stageq(2); stageq(3);
      WAITV(12);
    }
    BAR();
  }
  ST(4);
  stageq(4); stageq(5); stageq(6); stageq(7);
  const rsrc_t rsWS = __builtin_amdgcn_make_buffer_rsrc((void*)wsp, 0, WS_END, 0x00020000);
  const rsrc_t rsOA = __builtin_amdgcn_make_buffer_rsrc((void*)woA, 0, D_ * D_ * 2, 0x00020000);
  const rsrc_t rsOB = __builtin_amdgcn_make_buffer_rsrc((void*)woB, 0, D_ * D_ * 2, 0x00020000);
  const unsigned vo = rin * 1024 + csw;
  const unsigned hid = g * 4 + hw;
  wo_touch();
  asm volatile("s_waitcnt vmcnt(17)" : "+v"(x0), "+v"(x1), "+v"(gv), "+v"(fcv) :: "memory");
  if (!__all((((unsigned long long)fcv[1] << 32) | fcv[0]) == tag)) wait_flags(ws, foff, tag);
  float gval;
  {
    float a0 = 0.f, a1 = 0.f;
#pragma unroll
    for (int i = 0; i < 4; ++i) {
      const unsigned ug = gv[i], u0 = x0[i], u1 = x1[i];
      const float gl = __builtin_bit_cast(float, ug << 16), gh = __builtin_bit_cast(float, ug & 0xffff0000u);
      a0 = ffma(__builtin_bit_cast(float, u0 << 16), gl, a0); a0 = ffma(__builtin_bit_cast(float, u0 & 0xffff0000u), gh, a0);
      a1 = ffma(__builtin_bit_cast(float, u1 << 16), gl, a1); a1 = ffma(__builtin_bit_cast(float, u1 & 0xffff0000u), gh, a1);
    }
    a0 = wave_sum63(a0); a1 = wave_sum63(a1);
    const float s0 = __builtin_bit_cast(float, __builtin_amdgcn_readlane(__builtin_bit_cast(int, a0), 63));
    const float s1 = __builtin_bit_cast(float, __builtin_amdgcn_readlane(__builtin_bit_cast(int, a1), 63));
    const float lg = (lane & 1) ? s1 : s0;
    gval = __builtin_amdgcn_rcpf(1.0f + __builtin_amdgcn_exp2f(-1.44269504088896340736f * lg));
  }
  asm volatile("s_waitcnt vmcnt(1)" : "+v"(gval) :: "memory");
  ST(5);
  BAR();
  ST(6);
  {
    const f32x4 p0 = *(LAS f32x4*)(L + SCR + hw * 1024 + lane * 16), p1 = *(LAS f32x4*)(L + SCR + (4 + hw) * 1024 + lane * 16);
    const f32x4 z0 = *(LAS f32x4*)(L + SCR + (8 + hw) * 1024 + (lane & 3) * 16), z1 = *(LAS f32x4*)(L + SCR + (8 + hw) * 1024 + 64 + (lane & 3) * 16);
    const unsigned po = WS_PART + (hid * 16 + ti) * PART_STRIDE;
    const f32x4 ps = fadd4(p0, p1);
    const unsigned d0 = __builtin_bit_cast(unsigned, cvt2(ps[0], ps[1])), d1 = __builtin_bit_cast(unsigned, cvt2(ps[2], ps[3]));
    const auto e0 = __builtin_amdgcn_permlane32_swap(d0, d0, false, false), e1 = __builtin_amdgcn_permlane32_swap(d1, d1, false, false);
    const u32x4 kvp = {e0[0], e1[0], e0[1], e1[1]};
    const u32x4 zp = __builtin_bit_cast(u32x4, fadd4(z0, z1));
    u32x4 pay;
#pragma unroll
    for (int i = 0; i < 4; ++i) pay[i] = nosent(lane < 32 ? kvp[i] : zp[i]);
    if (lane < 36) st128<16>(pay, rsWS, lane * 16 + po);
    if (lane < 2) __hip_atomic_store((GAS unsigned*)(ws + WS_GATE) + grow + lane, __builtin_bit_cast(unsigned, gval), __ATOMIC_RELAXED, __HIP_MEMORY_SCOPE_AGENT);
  }
  ST(7);
#pragma unroll
  for (int i = 0; i < 16; ++i) glds(vo, rsOA, (hw + 4 * i) * 8192 + ct * 128, WOA + (hw + 4 * i) * 1024);
  asm volatile("" :: "v"(wtouch));
  ST(8);
  const unsigned pb = WS_PART + hid * 16 * PART_STRIDE;
  for (int i = 0; i < PDELAY1; ++i) __builtin_amdgcn_s_sleep(4);
  ST(9);
  {
    float a[8];
    f32x4 z;
    u32x4 pv[8], zv;
    auto gather = [&]() {
#pragma unroll
      for (int i = 0; i < 8; ++i) pv[i] = __builtin_amdgcn_raw_buffer_load_b128(rsWS, (lane >> 5) * PART_STRIDE + (lane & 31) * 16, pb + 2 * i * PART_STRIDE, 16);
      zv = __builtin_amdgcn_raw_buffer_load_b128(rsWS, (lane >> 2) * PART_STRIDE + 512 + (lane & 3) * 16, pb, 16);
    };
    auto reduce = [&]() {
      unsigned clo = 0x00003F80u, chi = 0x3F800000u;
      asm volatile("" : "+s"(clo), "+s"(chi));
      const bf16x2 lo1 = __builtin_bit_cast(bf16x2, clo), hi1 = __builtin_bit_cast(bf16x2, chi);
      unsigned mx = 0;
#pragma unroll
      for (int k = 0; k < 8; ++k) a[k] = 0.f;
#pragma unroll
      for (int i = 0; i < 8; ++i) {
#pragma unroll
        for (int k = 0; k < 4; ++k) {
          const unsigned u = pv[i][k];
          a[2 * k] = __builtin_amdgcn_fdot2_f32_bf16(__builtin_bit_cast(bf16x2, u), lo1, a[2 * k], false);
          a[2 * k + 1] = __builtin_amdgcn_fdot2_f32_bf16(__builtin_bit_cast(bf16x2, u), hi1, a[2 * k + 1], false);
        }
        mx = umax3(pv[i][0], pv[i][1], umax3(pv[i][2], pv[i][3], mx));
      }
      mx = umax3(zv[0], zv[1], umax3(zv[2], zv[3], mx));
      z = __builtin_bit_cast(f32x4, zv);
      return __all(mx != SENT);
    };
    gather();
    asm volatile("" ::: "memory");
    if (!reduce()) {
      for (unsigned spins = 0; spins < SPIN_CAP; ++spins) { gather(); if (reduce()) break; }
    }
#pragma unroll
    for (int k = 0; k < 8; ++k) {
      const unsigned u = __builtin_bit_cast(unsigned, a[k]);
      const auto t = __builtin_amdgcn_permlane32_swap(u, u, false, false);
      a[k] = __builtin_bit_cast(float, (unsigned)t[0]) + __builtin_bit_cast(float, (unsigned)t[1]);
    }
    z = f32x4{sum_stride4(z[0]), sum_stride4(z[1]), sum_stride4(z[2]), sum_stride4(z[3])};
    const int slot = lane & 31;
    if (lane < 32) *(LAS bf16x8*)(L + AFR + hw * 1024 + ((slot >> 4) * 32 + (slot & 15)) * 16) = pack8(a[0], a[1], a[2], a[3], a[4], a[5], a[6], a[7]);
    const int q4 = lane & 3;
    u32x2 kz = {__builtin_bit_cast(unsigned, cvt2(z[0], z[1])), __builtin_bit_cast(unsigned, cvt2(z[2], z[3]))};
    *(LAS u32x2*)(L + AFR + hw * 1024 + ((q4 & 1) * 32 + 16 + (lane >> 2)) * 16 + (q4 >> 1) * 8) = kz;
  }
  WAITL0();
  ST(10);
  BAR();
  ST(11);
#pragma unroll
  for (int i = 0; i < 16; ++i) glds(vo, rsOB, (hw + 4 * i) * 8192 + ct * 128, WOB + (hw + 4 * i) * 1024);
  WAITV(0);
  ST(12);
  BAR();
  {
    const int r32 = lane & 31, h = lane >> 5, g1 = (lane >> 4) & 1, q = (lane & 15) >> 2, p = lane & 3;
    const unsigned sw = ((2 * g1 + (p >> 1)) ^ (((q >> 1) & 1) << 2)) << 4;
    const int br2 = hw >> 1, sh = hw & 1;
    const unsigned st2 = 2 * rt + sh;
    const unsigned ob = WS_O + (st2 * 64 + 32 * br2) * 1024;
    for (int i = 0; i < PDELAY; ++i) __builtin_amdgcn_s_sleep(8);
    wait_flags(ws, WS_FLAGB + st2 * 128 + (lane & 15) * 8, tag);
    ST(13);
    unsigned wp0 = ((br2 ? WOB : WOA) + (4 * h + q) * 128 + sw + (p & 1) * 8) ^ (br2 << 6), wp1 = wp0 ^ 64;
    asm volatile("" : "+v"(wp0), "+v"(wp1));
    f32x16 c0 = {}, c1 = {};
    const unsigned gvb = ld_u32(ws, WS_GATE + (st2 * 32 + r32) * 4);
    {
      u32x4 of[32];
#pragma unroll
      for (int j = 0; j < OD; ++j) {
        of[j] = __builtin_amdgcn_raw_buffer_load_b128(rsWS, lane * 16, ob + j * 1024, 16);
        SB();
      }
      bf16x8 a0[3], a1[3];
      a0[0] = ldstr(L, wp0, wp0 + 1024); a1[0] = ldstr(L, wp1, wp1 + 1024);
      a0[1] = ldstr(L, wp0 + 2048, wp0 + 2048 + 1024); a1[1] = ldstr(L, wp1 + 2048, wp1 + 2048 + 1024);
#pragma unroll
      for (int j = 0; j < 32; ++j) {
        const unsigned o = (j + 2) * 2048;
        const bf16x8 bf = __builtin_bit_cast(bf16x8, of[j]);
        SB();
        c0 = mfma(a0[j % 3], bf, c0);
        SB();
        if (j + OD < 32) of[j + OD] = __builtin_amdgcn_raw_buffer_load_b128(rsWS, lane * 16, ob + (j + OD) * 1024, 16);
        if (j + 2 < 32) a0[(j + 2) % 3] = ldstr(L, wp0 + o, wp0 + o + 1024);
        SB();
        c1 = mfma(a1[j % 3], bf, c1);
        SB();
        if (j + 2 < 32) a1[(j + 2) % 3] = ldstr(L, wp1 + o, wp1 + o + 1024);
      }
      SB();
    }
    ST(14);
    __builtin_amdgcn_s_setprio(0);
    const float gv = __builtin_bit_cast(float, gvb);
    const float gs = br2 ? 1.0f - gv : gv;
#pragma unroll
    for (int qq = 0; qq < 4; ++qq) {
      const f32x4 v = {c1[4 * qq] * gs, c1[4 * qq + 1] * gs, c1[4 * qq + 2] * gs, c1[4 * qq + 3] * gs};
      *(LAS f32x4*)(L + EXC + hw * 4096 + qq * 1024 + lane * 16) = v;
    }
    WAITL0();
    BAR();
    if (b == 0 && hw == 0 && lane == 0) st_flag(ws, WS_EPOCH, tag);
    unsigned pk[8];
#pragma unroll
    for (int qq = 0; qq < 4; ++qq) {
      const f32x4 v = *(LAS f32x4*)(L + EXC + (hw ^ 2) * 4096 + qq * 1024 + lane * 16);
      pk[2 * qq] = __builtin_bit_cast(unsigned, cvt2(c0[4 * qq] * gs + v[0], c0[4 * qq + 1] * gs + v[1]));
      pk[2 * qq + 1] = __builtin_bit_cast(unsigned, cvt2(c0[4 * qq + 2] * gs + v[2], c0[4 * qq + 3] * gs + v[3]));
    }
    const rsrc_t rsO = __builtin_amdgcn_make_buffer_rsrc((void*)out, 0, S_ * D_ * 2, 0x00020000);
    const unsigned orow = (st2 * 32 + r32) * 1024 + (64 * ct + 32 * br2) * 2 + h * 16;
#pragma unroll
    for (int qq = 0; qq < 4; qq += 2) {
      const auto s0 = __builtin_amdgcn_permlane32_swap(pk[2 * qq], pk[2 * qq + 2], false, false);
      const auto s1 = __builtin_amdgcn_permlane32_swap(pk[2 * qq + 1], pk[2 * qq + 3], false, false);
      const u32x4 v = {s0[0], s1[0], s0[1], s1[1]};
      st128<0>(v, rsO, orow + qq * 16);
    }
    STR(16); STFLUSH();
  }
}

extern "C" void kernel_launch(void* const* d_in, const int* in_sizes, int n_in, void* d_out, int out_size, void* d_ws, size_t ws_size,
                              hipStream_t stream) {
  (void)in_sizes; (void)n_in; (void)out_size;
  const u16* x = (const u16*)d_in[0];
  const u16* wA = (const u16*)d_in[1];
  const u16* woA = (const u16*)d_in[2];
  const u16* wB = (const u16*)d_in[4];
  const u16* woB = (const u16*)d_in[5];
  const u16* gw = (const u16*)d_in[6];
  (void)hipFuncSetAttribute((const void*)gated_mix_fused, hipFuncAttributeMaxDynamicSharedMemorySize, LDS_BYTES);
  gated_mix_fused<<<256, 512, LDS_BYTES, stream>>>(x, wA, wB, woA, woB, gw, (u16*)d_out, (char*)d_ws + ((ws_size / 2) & ~(size_t)0x1FFFFF));
}
```

```cpp
#include <hip/hip_runtime.h>
#include <hip/hip_bf16.h>
#include <stdint.h>

#define LAS __attribute__((address_space(3)))
#define GAS __attribute__((address_space(1)))
#define DI __device__ __forceinline__

typedef unsigned short u16;
typedef __bf16 bf16x8 __attribute__((ext_vector_type(8)));
typedef __bf16 bf16x4 __attribute__((ext_vector_type(4)));
typedef __bf16 bf16x2 __attribute__((ext_vector_type(2)));
typedef float f32x16 __attribute__((ext_vector_type(16)));
typedef float f32x4 __attribute__((ext_vector_type(4)));
typedef float f32x2 __attribute__((ext_vector_type(2)));
typedef unsigned u32x4 __attribute__((ext_vector_type(4)));
typedef unsigned u32x2 __attribute__((ext_vector_type(2)));
typedef __amdgpu_buffer_rsrc_t rsrc_t;

#define ROT 6
#define CODE_PREFETCH 20480
#define CLR_STAGE 3
#define HPRIO 3
#define OD 10
#define PSLEEP 4
#define PDELAY 4
#define P2MAP 0
#define PDELAY1 1

constexpr int S_ = 2048, D_ = 512;
constexpr unsigned XR = 0;
constexpr unsigned WR = 49152;
constexpr unsigned WQ = 98304;
constexpr unsigned WOA = 0;
constexpr unsigned WOB = 98304;
constexpr unsigned SCR = 81920;
constexpr unsigned AFR = SCR + 12288;
constexpr unsigned EXC = SCR;
constexpr unsigned LDS_BYTES = 163840;
constexpr unsigned WS_EPOCH = 0;
constexpr unsigned WS_FLAGC = 4096;
constexpr unsigned WS_FLAGB = WS_FLAGC + 1024 * 128;
constexpr unsigned WS_GATE = WS_FLAGB + 1024 * 128;
constexpr unsigned WS_PART = WS_GATE + 8192;
constexpr unsigned PART_STRIDE = 640;
constexpr unsigned WS_O = WS_PART + 1024 * PART_STRIDE;
constexpr unsigned WS_END = WS_O + 64 * 64 * 1024;
constexpr unsigned SPIN_CAP = 1u << 18;

DI void glds(unsigned voff, rsrc_t rs, unsigned soff, unsigned ldsaddr) {
  asm volatile("s_mov_b32 m0, %0\n\ts_nop 0\n\tbuffer_load_dwordx4 %1, %2, %3 offen lds"
               :: "s"(ldsaddr), "v"(voff), "s"(rs), "s"(soff) : "memory");
}
template <int AUX> DI void st128(u32x4 v, rsrc_t rs, unsigned off) { __builtin_amdgcn_raw_buffer_store_b128(v, rs, off, 0, AUX); }
#define WAITV(n) asm volatile("s_waitcnt vmcnt(" #n ")" ::: "memory")
#define WAITL0() asm volatile("s_waitcnt lgkmcnt(0)" ::: "memory")
#define BAR() asm volatile("s_barrier" ::: "memory")

DI f32x16 mfma(bf16x8 a, bf16x8 b, f32x16 c) { return __builtin_amdgcn_mfma_f32_32x32x16_bf16(a, b, c, 0, 0, 0); }
DI bf16x8 ldsr128(LAS char* L, unsigned a) { return *(LAS bf16x8*)(L + a); }
DI bf16x8 ldstr(LAS char* L, unsigned a0, unsigned a1) {
  bf16x4 lo = __builtin_amdgcn_ds_read_tr16_b64_v4bf16((LAS bf16x4*)(L + a0));
  bf16x4 hi = __builtin_amdgcn_ds_read_tr16_b64_v4bf16((LAS bf16x4*)(L + a1));
  return __builtin_shufflevector(lo, hi, 0, 1, 2, 3, 4, 5, 6, 7);
}
DI bf16x2 cvt2(float a, float b) { f32x2 p = {a, b}; return __builtin_convertvector(p, bf16x2); }
DI bf16x8 pack8(float a0, float a1, float a2, float a3, float a4, float a5, float a6, float a7) {
  bf16x2 p0 = cvt2(a0, a1), p1 = cvt2(a2, a3), p2 = cvt2(a4, a5), p3 = cvt2(a6, a7);
  bf16x4 lo = __builtin_shufflevector(p0, p1, 0, 1, 2, 3), hi = __builtin_shufflevector(p2, p3, 0, 1, 2, 3);
  return __builtin_shufflevector(lo, hi, 0, 1, 2, 3, 4, 5, 6, 7);
}
DI unsigned phi2(float a, float b) {
  const f32x2 t = {a, b};
  const f32x2 u = t * 1.44269504088896340736f;
  const f32x2 e = {__builtin_amdgcn_fmed3f(__builtin_amdgcn_exp2f(u[0]), 0.0f, 1.0f), __builtin_amdgcn_fmed3f(__builtin_amdgcn_exp2f(u[1]), 0.0f, 1.0f)};
  const int ia = __builtin_bit_cast(int, a), ib = __builtin_bit_cast(int, b);
  const f32x2 m = {__builtin_bit_cast(float, ia > 0 ? ia : 0), __builtin_bit_cast(float, ib > 0 ? ib : 0)};
  const f32x2 r = (m + e) + 1e-6f;
  return __builtin_bit_cast(unsigned, __builtin_convertvector(r, bf16x2));
}
DI bf16x8 phi8(const f32x16& x, int s) {
  const u32x4 v = {phi2(x[8 * s], x[8 * s + 1]), phi2(x[8 * s + 2], x[8 * s + 3]), phi2(x[8 * s + 4], x[8 * s + 5]), phi2(x[8 * s + 6], x[8 * s + 7])};
  return __builtin_bit_cast(bf16x8, v);
}
DI float bsel(unsigned m, float a, float b) {
  const unsigned ua = __builtin_bit_cast(unsigned, a), ub = __builtin_bit_cast(unsigned, b);
  return __builtin_bit_cast(float, (unsigned)__builtin_amdgcn_bitop3_b32(m, ub, ua, 0xCA));
}
DI float fadd(float a, float b) { float r; asm("v_add_f32 %0, %1, %2" : "=v"(r) : "v"(a), "v"(b)); return r; }
DI f32x4 fadd4(f32x4 a, f32x4 b) { return f32x4{fadd(a[0], b[0]), fadd(a[1], b[1]), fadd(a[2], b[2]), fadd(a[3], b[3])}; }
DI float ffma(float a, float b, float c) { float r; asm("v_fma_f32 %0, %1, %2, %3" : "=v"(r) : "v"(a), "v"(b), "v"(c)); return r; }
template <int CTRL, int RM> DI float dpp_add(float v) {
  return fadd(v, __builtin_bit_cast(float, __builtin_amdgcn_update_dpp(0, __builtin_bit_cast(int, v), CTRL, RM, 0xf, true)));
}
DI float wave_sum63(float v) {
  v = dpp_add<0x111, 0xf>(v); v = dpp_add<0x112, 0xf>(v); v = dpp_add<0x114, 0xf>(v); v = dpp_add<0x118, 0xf>(v);
  v = dpp_add<0x142, 0xa>(v); v = dpp_add<0x143, 0xc>(v);
  return v;
}
DI float sum_stride4(float v) {
  v += __builtin_bit_cast(float, __builtin_amdgcn_update_dpp(0, __builtin_bit_cast(int, v), 0x124, 0xf, 0xf, true));
  v += __builtin_bit_cast(float, __builtin_amdgcn_update_dpp(0, __builtin_bit_cast(int, v), 0x128, 0xf, 0xf, true));
  { const unsigned u = __builtin_bit_cast(unsigned, v); const auto t = __builtin_amdgcn_permlane16_swap(u, u, false, false);
    v = __builtin_bit_cast(float, (unsigned)t[0]) + __builtin_bit_cast(float, (unsigned)t[1]); }
  { const unsigned u = __builtin_bit_cast(unsigned, v); const auto t = __builtin_amdgcn_permlane32_swap(u, u, false, false);
    v = __builtin_bit_cast(float, (unsigned)t[0]) + __builtin_bit_cast(float, (unsigned)t[1]); }
  return v;
}
constexpr unsigned SENT = 0xFFFFFFFFu;
DI unsigned nosent(unsigned v) { return v < SENT ? v : SENT - 1u; }
DI u32x4 nosent4(u32x4 v) { return u32x4{nosent(v[0]), nosent(v[1]), nosent(v[2]), nosent(v[3])}; }
DI unsigned umax3(unsigned a, unsigned b, unsigned c) { unsigned m = a > b ? a : b; return m > c ? m : c; }
#define SB() __builtin_amdgcn_sched_barrier(0)
DI bf16x8 cvt8(const f32x16& x, int s) {
  return pack8(x[8 * s], x[8 * s + 1], x[8 * s + 2], x[8 * s + 3], x[8 * s + 4], x[8 * s + 5], x[8 * s + 6], x[8 * s + 7]);
}
DI float sum8(bf16x8 v, float acc) {
  const bf16x2 ones = {(__bf16)1.0f, (__bf16)1.0f};
  acc = __builtin_amdgcn_fdot2_f32_bf16(__builtin_shufflevector(v, v, 0, 1), ones, acc, false);
  acc = __builtin_amdgcn_fdot2_f32_bf16(__builtin_shufflevector(v, v, 2, 3), ones, acc, false);
  acc = __builtin_amdgcn_fdot2_f32_bf16(__builtin_shufflevector(v, v, 4, 5), ones, acc, false);
  acc = __builtin_amdgcn_fdot2_f32_bf16(__builtin_shufflevector(v, v, 6, 7), ones, acc, false);
  return acc;
}
DI unsigned long long ld_flag(GAS char* ws, unsigned off) {
  return __hip_atomic_load((GAS unsigned long long*)(ws + off), __ATOMIC_RELAXED, __HIP_MEMORY_SCOPE_AGENT);
}
DI void st_flag(GAS char* ws, unsigned off, unsigned long long v) {
  __hip_atomic_store((GAS unsigned long long*)(ws + off), v, __ATOMIC_RELAXED, __HIP_MEMORY_SCOPE_AGENT);
}
DI unsigned long long epoch_issue(const char* wsp) {
  unsigned long long e;
  asm volatile("s_load_dwordx2 %0, %1, 0x0" : "=s"(e) : "s"(wsp));
  return e;
}
DI unsigned long long epoch_wait(unsigned long long e) {
  asm volatile("s_waitcnt lgkmcnt(0)" : "+s"(e) :: "memory");
  return e + 1ull;
}
DI void wait_flags(GAS char* ws, unsigned off, unsigned long long tag) {
  for (unsigned spins = 0; spins < SPIN_CAP; ++spins) {
    const unsigned long long v = ld_flag(ws, off);
    if (__all(v == tag)) break;
    __builtin_amdgcn_s_sleep(PSLEEP);
  }
  asm volatile("" ::: "memory");
}
DI unsigned ld_u32(GAS char* ws, unsigned off) {
  return __hip_atomic_load((GAS unsigned*)(ws + off), __ATOMIC_RELAXED, __HIP_MEMORY_SCOPE_AGENT);
}

__global__ __launch_bounds__(512, 2) void gated_mix_fused(const u16* __restrict__ x, const u16* __restrict__ wA, const u16* __restrict__ wB,
                                                          const u16* __restrict__ woA, const u16* __restrict__ woB, const u16* __restrict__ gw,
                                                          u16* __restrict__ out, char* __restrict__ wsp) {
  extern __shared__ __attribute__((aligned(16))) char lds[];
  LAS char* L = (LAS char*)lds;
  const int tid = threadIdx.x;
  const int wave = __builtin_amdgcn_readfirstlane(tid >> 6), lane = tid & 63;
  const int b = blockIdx.x, xcd = b & 7, r = b >> 3;
  const int ti = (xcd & 1) * 8 + (r & 7), g = (xcd >> 1) * 4 + (r >> 3);
  const int gg = g & 7;
  const int rt = P2MAP ? 8 * (xcd >> 1) + (r & 7) : 4 * xcd + (r & 3), ct = P2MAP ? 4 * (xcd & 1) + (r >> 3) : r >> 2;
  GAS char* ws = (GAS char*)wsp;
  asm volatile("" :: "s"(x), "s"(wA), "s"(wB), "s"(woA), "s"(woB), "s"(gw), "s"(out), "s"(wsp));
  unsigned long long pc;
  asm volatile("s_getpc_b64 %0" : "=s"(pc));

  if (wave < 4) {
    const unsigned long long epoch_raw = epoch_issue(wsp);
    const int wr = wave >> 1, wc = wave & 1;
    const int r32 = lane & 31, h = lane >> 5;
    const int g1 = (lane >> 4) & 1, q = (lane & 15) >> 2, p = lane & 3;
    const int own = wave, oth = wave ^ 1;
    const rsrc_t rsWS = __builtin_amdgcn_make_buffer_rsrc((void*)wsp, 0, WS_END, 0x00020000);
    unsigned ctouch = 0;
    {
      const unsigned off = (wave * 64 + lane) * 128;
      if (off < CODE_PREFETCH) { const char* a = (const char*)pc + off; asm volatile("global_load_dword %0, %1, off" : "=v"(ctouch) : "v"(a) : "memory"); }
    }
    {
      const u32x4 sent = {SENT, SENT, SENT, SENT};
      const unsigned po = WS_PART + ((g * 4 + wave) * 16 + ti) * PART_STRIDE;
      if (lane < 36) st128<16>(sent, rsWS, lane * 16 + po);
    }
    const unsigned fx = (h ^ ((r32 >> 1) & 7)) << 4;
    unsigned xo0 = XR + (own * 32 + r32) * 128 + fx, xt0 = XR + (oth * 32 + r32) * 128 + fx;
    const unsigned sw = ((2 * g1 + (p >> 1)) ^ (((q >> 1) & 1) << 2)) << 4;
    const unsigned wn0 = (8 * h + q) * 128 + sw + (p & 1) * 8;
    unsigned wkv0 = WR + (wn0 ^ (wc << 6));
    asm volatile("" : "+v"(wkv0));
    unsigned xoa[4], xta[4];
#pragma unroll
    for (int k = 0; k < 4; ++k) { xoa[k] = xo0 ^ (k << 5); xta[k] = xt0 ^ (k << 5); asm volatile("" : "+v"(xoa[k]), "+v"(xta[k])); }
    const unsigned long long tag = epoch_wait(epoch_raw);

    bf16x8 xr[32];
    f32x16 kO = {}, kT = {}, vO = {}, vT = {};
    {
      BAR();
      bf16x8 xt = ldsr128(L, xta[0]), wk = ldstr(L, wkv0, wkv0 + 512), wv = ldstr(L, wkv0 + 8192, wkv0 + 8192 + 512);
      xr[0] = ldsr128(L, xoa[0]);
#pragma unroll
      for (int t = 0; t < 32; ++t) {
        bf16x8 nxt, nwk, nwv;
        const int s1 = (t + 1) >> 2, k1 = (t + 1) & 3;
        const unsigned slot = (s1 % 3) * 16384;
        if (t + 1 < 32 && k1 == 0) {
          WAITL0();
          if (s1 == CLR_STAGE) {
            WAITV(0);
            asm volatile("" :: "v"(ctouch));
            if (lane == 0) st_flag(ws, WS_FLAGC + (g * 4 + wave) * 128 + ti * 8, tag);
          }
          BAR();
        }
        SB();
        kO = mfma(xr[t], wk, kO);
        SB();
        if (t + 1 < 32) {
          xr[t + 1] = ldsr128(L, xoa[k1] + slot);
          nwk = ldstr(L, wkv0 + slot + k1 * 2048, wkv0 + slot + k1 * 2048 + 512);
        }
        SB();
        kT = mfma(xt, wk, kT);
        SB();
        if (t + 1 < 32) {
          nxt = ldsr128(L, xta[k1] + slot);
          nwv = ldstr(L, wkv0 + slot + 8192 + k1 * 2048, wkv0 + slot + 8192 + k1 * 2048 + 512);
        }
        SB();
        vO = mfma(xr[t], wv, vO);
        vT = mfma(xt, wv, vT);
        SB();
        if (t + 1 < 32) { xt = nxt; wk = nwk; wv = nwv; }
      }
    }
    {
      const bf16x8 pa0 = phi8(kO, 0), pa1 = phi8(kO, 1), pb0 = phi8(kT, 0), pb1 = phi8(kT, 1);
      const bf16x8 va0 = cvt8(vO, 0), va1 = cvt8(vO, 1), vb0 = cvt8(vT, 0), vb1 = cvt8(vT, 1);
      float zs = sum8(pb1, sum8(pb0, sum8(pa1, sum8(pa0, 0.f))));
      SB();
      f32x16 kv = {};
      kv = mfma(pa0, va0, kv); kv = mfma(pb0, vb0, kv); kv = mfma(pa1, va1, kv); kv = mfma(pb1, vb1, kv);
      SB();
      {
        const unsigned zb = __builtin_bit_cast(unsigned, zs);
        const auto sw2 = __builtin_amdgcn_permlane32_swap(zb, zb, false, false);
        zs = __builtin_bit_cast(float, (unsigned)sw2[0]) + __builtin_bit_cast(float, (unsigned)sw2[1]);
      }
      const unsigned gm = g1 ? 0xFFFFFFFFu : 0u;
      f32x4 lo, hi;
#pragma unroll
      for (int j = 0; j < 4; ++j) { lo[j] = bsel(gm, kv[j], kv[8 + j]); hi[j] = bsel(gm, kv[4 + j], kv[12 + j]); }
      const int hh = 2 * wc + g1;
      const unsigned a = SCR + (4 * wr + hh) * 1024 + (h * 16 + (lane & 15)) * 16;
      *(LAS f32x4*)(L + a) = lo;
      *(LAS f32x4*)(L + a + 512) = hi;
      if (h == 0) *(LAS float*)(L + SCR + (8 + hh) * 1024 + wr * 64 + (lane & 15) * 4) = zs;
    }
    WAITL0();
    BAR();
    f32x16 qa = {}, qb = {};
    {
      unsigned wq0 = WQ + wn0, wq1 = WQ + (wn0 ^ 64);
      asm volatile("" : "+v"(wq0), "+v"(wq1));
      bf16x8 a0[3], a1[3];
      a0[0] = ldstr(L, wq0, wq0 + 512); a1[0] = ldstr(L, wq1, wq1 + 512);
      a0[1] = ldstr(L, wq0 + 2048, wq0 + 2048 + 512); a1[1] = ldstr(L, wq1 + 2048, wq1 + 2048 + 512);
#pragma unroll
      for (int t = 0; t < 32; ++t) {
        const unsigned o = (t + 2) * 2048;
        SB();
        qa = mfma(a0[t % 3], xr[t], qa);
        SB();
        if (t + 2 < 32) a0[(t + 2) % 3] = ldstr(L, wq0 + o, wq0 + o + 512);
        SB();
        qb = mfma(a1[t % 3], xr[t], qb);
        SB();
        if (t + 2 < 32) a1[(t + 2) % 3] = ldstr(L, wq1 + o, wq1 + o + 512);
      }
      SB();
    }
    bf16x8 pq[4];
    pq[0] = phi8(qa, 0); pq[1] = phi8(qa, 1); pq[2] = phi8(qb, 0); pq[3] = phi8(qb, 1);
    asm volatile("" : "+v"(pq[0]), "+v"(pq[1]), "+v"(pq[2]), "+v"(pq[3]));
    WAITL0();
    BAR();
    {
      const unsigned st = 4 * ti + own;
      bf16x8 af[4];
#pragma unroll
      for (int hh = 0; hh < 4; ++hh) af[hh] = ldsr128(L, AFR + hh * 1024 + lane * 16);
      SB();
      f32x16 d[4];
#pragma unroll
      for (int hh = 0; hh < 4; ++hh) { d[hh] = f32x16{}; d[hh] = mfma(af[hh], pq[hh], d[hh]); }
      SB();
#pragma unroll
      for (int hh = 0; hh < 4; ++hh) {
        const float inv = __builtin_amdgcn_rcpf(d[hh][8] + 1e-6f);
        const u32x4 o = __builtin_bit_cast(u32x4, pack8(d[hh][0] * inv, d[hh][1] * inv, d[hh][2] * inv, d[hh][3] * inv, d[hh][4] * inv, d[hh][5] * inv, d[hh][6] * inv, d[hh][7] * inv));
        st128<16>(o, rsWS, lane * 16 + WS_O + (st * 64 + 4 * g + hh) * 1024);
      }
    }
    BAR();
    WAITV(0);
    if (lane == 0) st_flag(ws, WS_FLAGB + (4 * ti + own) * 128 + g * 8, tag);
    return;
  }

  const int hw = wave - 4;
  const unsigned long long epoch_raw = epoch_issue(wsp);
  __builtin_amdgcn_s_setprio(HPRIO);
  int ro = 0;
  if (ROT == 1) ro = (ti + 2 * g) & 7;
  if (ROT == 2) ro = 2 * ((ti + g) & 1);
  if (ROT == 3) ro = 4 * ((ti + g) & 1);
  if (ROT == 5) ro = ((ti + g) % 3) * 2;
  if (ROT == 6) ro = 2 * ((ti + g) & 3);
  const u16* w = (g >= 8) ? wB : wA;
  const rsrc_t rsX = __builtin_amdgcn_make_buffer_rsrc((void*)x, 0, S_ * D_ * 2, 0x00020000);
  const rsrc_t rsW = __builtin_amdgcn_make_buffer_rsrc((void*)w, 0, D_ * 3 * D_ * 2, 0x00020000);
  const int rin = lane >> 3, ch = lane & 7;
  const unsigned vx = rin * 1024 + ((ch ^ (4 * (hw & 1) + (rin >> 1))) << 4);
  const unsigned csw = (ch ^ (((rin >> 1) & 1) << 2)) << 4;
  const unsigned vw = rin * 3072 + csw;
  const unsigned sx0 = (ti * 128 + 8 * hw) * 1024;
  const unsigned sw0 = 8 * hw * 3072 + gg * 128;
  auto stage = [&](int s) {
    const unsigned ks = (s + ro) & 7, slot = (s % 3) * 16384;
#pragma unroll
    for (int i = 0; i < 4; ++i) glds(vx, rsX, sx0 + ks * 128 + i * 32768, XR + slot + (hw + 4 * i) * 1024);
#pragma unroll
    for (int i = 0; i < 2; ++i) glds(vw, rsW, sw0 + ks * 196608 + i * 98304 + 1024, WR + slot + (hw + 4 * i) * 1024);
#pragma unroll
    for (int i = 0; i < 2; ++i) glds(vw, rsW, sw0 + ks * 196608 + i * 98304 + 2048, WR + slot + 8192 + (hw + 4 * i) * 1024);
  };
  auto stageq = [&](int s) {
    const unsigned ks = (s + ro) & 7;
#pragma unroll
    for (int i = 0; i < 2; ++i) glds(vw, rsW, sw0 + ks * 196608 + i * 98304, WQ + s * 8192 + (hw + 4 * i) * 1024);
  };
  unsigned wtouch = 0;
  auto wo_touch = [&]() {
    if (P2MAP) {
      const unsigned row = r * 32 + hw * 8 + ((lane & 31) >> 2);
      const char* a = (const char*)((row & 512) ? woB : woA) + (row & 511) * 1024 + (xcd & 1) * 512 + (lane & 3) * 128;
      if (lane < 32) asm volatile("global_load_dword %0, %1, off" : "=v"(wtouch) : "v"(a) : "memory");
    } else {
      const unsigned row = r * 32 + hw * 8 + (lane >> 3);
      const char* a = (const char*)((row & 512) ? woB : woA) + (row & 511) * 1024 + (lane & 7) * 128;
      asm volatile("global_load_dword %0, %1, off" : "=v"(wtouch) : "v"(a) : "memory");
    }
  };
  stage(0);
  stage(1);
  WAITV(8);
  const unsigned long long tag = epoch_wait(epoch_raw);
  BAR();
  const rsrc_t rsG = __builtin_amdgcn_make_buffer_rsrc((void*)gw, 0, D_ * 2, 0x00020000);
  const unsigned grow = ti * 128 + 8 * g + 2 * hw;
  u32x4 x0, x1, gv;
  const unsigned foff = WS_FLAGC + (g * 4 + hw) * 128 + (lane & 15) * 8;
  u32x2 fcv;
#pragma unroll
  for (int s = 1; s < 8; ++s) {
    if (s < 6) { stage(s + 1); WAITV(8); }
    else if (s == 6) {
      stage(7);
      asm volatile("buffer_load_dwordx4 %0, %4, %5, %7 offen\n\tbuffer_load_dwordx4 %1, %4, %5, %7 offen offset:1024\n\tbuffer_load_dwordx4 %2, %4, %6, 0 offen\n\t"
                   "global_load_dwordx2 %3, %8, %9 sc1"
                   : "=&v"(x0), "=&v"(x1), "=&v"(gv), "=&v"(fcv) : "v"(lane * 16), "s"(rsX), "s"(rsG), "s"(grow * 1024), "v"(foff), "s"(wsp) : "memory");
      WAITV(12);
    } else {
      stageq(0); stageq(1); stageq(2); stageq(3);
      WAITV(12);
    }
    BAR();
  }
  stageq(4); stageq(5); stageq(6); stageq(7);
  const rsrc_t rsWS = __builtin_amdgcn_make_buffer_rsrc((void*)wsp, 0, WS_END, 0x00020000);
  const rsrc_t rsOA = __builtin_amdgcn_make_buffer_rsrc((void*)woA, 0, D_ * D_ * 2, 0x00020000);
  const rsrc_t rsOB = __builtin_amdgcn_make_buffer_rsrc((void*)woB, 0, D_ * D_ * 2, 0x00020000);
  const unsigned vo = rin * 1024 + csw;
  const unsigned hid = g * 4 + hw;
  wo_touch();
  asm volatile("s_waitcnt vmcnt(17)" : "+v"(x0), "+v"(x1), "+v"(gv), "+v"(fcv) :: "memory");
  if (!__all((((unsigned long long)fcv[1] << 32) | fcv[0]) == tag)) wait_flags(ws, foff, tag);
  float gval;
  {
    float a0 = 0.f, a1 = 0.f;
#pragma unroll
    for (int i = 0; i < 4; ++i) {
      const unsigned ug = gv[i], u0 = x0[i], u1 = x1[i];
      const float gl = __builtin_bit_cast(float, ug << 16), gh = __builtin_bit_cast(float, ug & 0xffff0000u);
      a0 = ffma(__builtin_bit_cast(float, u0 << 16), gl, a0); a0 = ffma(__builtin_bit_cast(float, u0 & 0xffff0000u), gh, a0);
      a1 = ffma(__builtin_bit_cast(float, u1 << 16), gl, a1); a1 = ffma(__builtin_bit_cast(float, u1 & 0xffff0000u), gh, a1);
    }
    a0 = wave_sum63(a0); a1 = wave_sum63(a1);
    const float s0 = __builtin_bit_cast(float, __builtin_amdgcn_readlane(__builtin_bit_cast(int, a0), 63));
    const float s1 = __builtin_bit_cast(float, __builtin_amdgcn_readlane(__builtin_bit_cast(int, a1), 63));
    const float lg = (lane & 1) ? s1 : s0;
    gval = __builtin_amdgcn_rcpf(1.0f + __builtin_amdgcn_exp2f(-1.44269504088896340736f * lg));
  }
  asm volatile("s_waitcnt vmcnt(1)" : "+v"(gval) :: "memory");
  BAR();
  {
    const f32x4 p0 = *(LAS f32x4*)(L + SCR + hw * 1024 + lane * 16), p1 = *(LAS f32x4*)(L + SCR + (4 + hw) * 1024 + lane * 16);
    const f32x4 z0 = *(LAS f32x4*)(L + SCR + (8 + hw) * 1024 + (lane & 3) * 16), z1 = *(LAS f32x4*)(L + SCR + (8 + hw) * 1024 + 64 + (lane & 3) * 16);
    const unsigned po = WS_PART + (hid * 16 + ti) * PART_STRIDE;
    const f32x4 ps = fadd4(p0, p1);
    const unsigned d0 = __builtin_bit_cast(unsigned, cvt2(ps[0], ps[1])), d1 = __builtin_bit_cast(unsigned, cvt2(ps[2], ps[3]));
    const auto e0 = __builtin_amdgcn_permlane32_swap(d0, d0, false, false), e1 = __builtin_amdgcn_permlane32_swap(d1, d1, false, false);
    const u32x4 kvp = {e0[0], e1[0], e0[1], e1[1]};
    const u32x4 zp = __builtin_bit_cast(u32x4, fadd4(z0, z1));
    u32x4 pay;
#pragma unroll
    for (int i = 0; i < 4; ++i) pay[i] = nosent(lane < 32 ? kvp[i] : zp[i]);
    if (lane < 36) st128<16>(pay, rsWS, lane * 16 + po);
    if (lane < 2) __hip_atomic_store((GAS unsigned*)(ws + WS_GATE) + grow + lane, __builtin_bit_cast(unsigned, gval), __ATOMIC_RELAXED, __HIP_MEMORY_SCOPE_AGENT);
  }
#pragma unroll
  for (int i = 0; i < 16; ++i) glds(vo, rsOA, (hw + 4 * i) * 8192 + ct * 128, WOA + (hw + 4 * i) * 1024);
  asm volatile("" :: "v"(wtouch));
  const unsigned pb = WS_PART + hid * 16 * PART_STRIDE;
  for (int i = 0; i < PDELAY1; ++i) __builtin_amdgcn_s_sleep(4);
  {
    float a[8];
    f32x4 z;
    u32x4 pv[8], zv;
    auto gather = [&]() {
#pragma unroll
      for (int i = 0; i < 8; ++i) pv[i] = __builtin_amdgcn_raw_buffer_load_b128(rsWS, (lane >> 5) * PART_STRIDE + (lane & 31) * 16, pb + 2 * i * PART_STRIDE, 16);
      zv = __builtin_amdgcn_raw_buffer_load_b128(rsWS, (lane >> 2) * PART_STRIDE + 512 + (lane & 3) * 16, pb, 16);
    };
    auto reduce = [&]() {
      unsigned clo = 0x00003F80u, chi = 0x3F800000u;
      asm volatile("" : "+s"(clo), "+s"(chi));
      const bf16x2 lo1 = __builtin_bit_cast(bf16x2, clo), hi1 = __builtin_bit_cast(bf16x2, chi);
      unsigned mx = 0;
#pragma unroll
      for (int k = 0; k < 8; ++k) a[k] = 0.f;
#pragma unroll
      for (int i = 0; i < 8; ++i) {
#pragma unroll
        for (int k = 0; k < 4; ++k) {
          const unsigned u = pv[i][k];
          a[2 * k] = __builtin_amdgcn_fdot2_f32_bf16(__builtin_bit_cast(bf16x2, u), lo1, a[2 * k], false);
          a[2 * k + 1] = __builtin_amdgcn_fdot2_f32_bf16(__builtin_bit_cast(bf16x2, u), hi1, a[2 * k + 1], false);
        }
        mx = umax3(pv[i][0], pv[i][1], umax3(pv[i][2], pv[i][3], mx));
      }
      mx = umax3(zv[0], zv[1], umax3(zv[2], zv[3], mx));
      z = __builtin_bit_cast(f32x4, zv);
      return __all(mx != SENT);
    };
    gather();
    asm volatile("" ::: "memory");
    if (!reduce()) {
      for (unsigned spins = 0; spins < SPIN_CAP; ++spins) { gather(); if (reduce()) break; }
    }
#pragma unroll
    for (int k = 0; k < 8; ++k) {
      const unsigned u = __builtin_bit_cast(unsigned, a[k]);
      const auto t = __builtin_amdgcn_permlane32_swap(u, u, false, false);
      a[k] = __builtin_bit_cast(float, (unsigned)t[0]) + __builtin_bit_cast(float, (unsigned)t[1]);
    }
    z = f32x4{sum_stride4(z[0]), sum_stride4(z[1]), sum_stride4(z[2]), sum_stride4(z[3])};
    const int slot = lane & 31;
    if (lane < 32) *(LAS bf16x8*)(L + AFR + hw * 1024 + ((slot >> 4) * 32 + (slot & 15)) * 16) = pack8(a[0], a[1], a[2], a[3], a[4], a[5], a[6], a[7]);
    const int q4 = lane & 3;
    u32x2 kz = {__builtin_bit_cast(unsigned, cvt2(z[0], z[1])), __builtin_bit_cast(unsigned, cvt2(z[2], z[3]))};
    *(LAS u32x2*)(L + AFR + hw * 1024 + ((q4 & 1) * 32 + 16 + (lane >> 2)) * 16 + (q4 >> 1) * 8) = kz;
  }
  WAITV(0);
  WAITL0();
  BAR();
#pragma unroll
  for (int i = 0; i < 16; ++i) glds(vo, rsOB, (hw + 4 * i) * 8192 + ct * 128, WOB + (hw + 4 * i) * 1024);
  WAITV(0);
  BAR();
  {
    const int r32 = lane & 31, h = lane >> 5, g1 = (lane >> 4) & 1, q = (lane & 15) >> 2, p = lane & 3;
    const unsigned sw = ((2 * g1 + (p >> 1)) ^ (((q >> 1) & 1) << 2)) << 4;
    const int br2 = hw >> 1, sh = hw & 1;
    const unsigned st2 = 2 * rt + sh;
    const unsigned ob = WS_O + (st2 * 64 + 32 * br2) * 1024;
    for (int i = 0; i < PDELAY; ++i) __builtin_amdgcn_s_sleep(8);
    wait_flags(ws, WS_FLAGB + st2 * 128 + (lane & 15) * 8, tag);
    unsigned wp0 = ((br2 ? WOB : WOA) + (4 * h + q) * 128 + sw + (p & 1) * 8) ^ (br2 << 6), wp1 = wp0 ^ 64;
    asm volatile("" : "+v"(wp0), "+v"(wp1));
    f32x16 c0 = {}, c1 = {};
    const unsigned gvb = ld_u32(ws, WS_GATE + (st2 * 32 + r32) * 4);
    {
      u32x4 of[32];
#pragma unroll
      for (int j = 0; j < OD; ++j) {
        of[j] = __builtin_amdgcn_raw_buffer_load_b128(rsWS, lane * 16, ob + j * 1024, 16);
        SB();
      }
      bf16x8 a0[3], a1[3];
      a0[0] = ldstr(L, wp0, wp0 + 1024); a1[0] = ldstr(L, wp1, wp1 + 1024);
      a0[1] = ldstr(L, wp0 + 2048, wp0 + 2048 + 1024); a1[1] = ldstr(L, wp1 + 2048, wp1 + 2048 + 1024);
#pragma unroll
      for (int j = 0; j < 32; ++j) {
        const unsigned o = (j + 2) * 2048;
        const bf16x8 bf = __builtin_bit_cast(bf16x8, of[j]);
        SB();
        c0 = mfma(a0[j % 3], bf, c0);
        SB();
        if (j + OD < 32) of[j + OD] = __builtin_amdgcn_raw_buffer_load_b128(rsWS, lane * 16, ob + (j + OD) * 1024, 16);
        if (j + 2 < 32) a0[(j + 2) % 3] = ldstr(L, wp0 + o, wp0 + o + 1024);
        SB();
        c1 = mfma(a1[j % 3], bf, c1);
        SB();
        if (j + 2 < 32) a1[(j + 2) % 3] = ldstr(L, wp1 + o, wp1 + o + 1024);
      }
      SB();
    }
    __builtin_amdgcn_s_setprio(0);
    const float gv = __builtin_bit_cast(float, gvb);
    const float gs = br2 ? 1.0f - gv : gv;
#pragma unroll
    for (int qq = 0; qq < 4; ++qq) {
      const f32x4 v = {c1[4 * qq] * gs, c1[4 * qq + 1] * gs, c1[4 * qq + 2] * gs, c1[4 * qq + 3] * gs};
      *(LAS f32x4*)(L + EXC + hw * 4096 + qq * 1024 + lane * 16) = v;
    }
    WAITL0();
    BAR();
    if (b == 0 && hw == 0 && lane == 0) st_flag(ws, WS_EPOCH, tag);
    unsigned pk[8];
#pragma unroll
    for (int qq = 0; qq < 4; ++qq) {
      const f32x4 v = *(LAS f32x4*)(L + EXC + (hw ^ 2) * 4096 + qq * 1024 + lane * 16);
      pk[2 * qq] = __builtin_bit_cast(unsigned, cvt2(c0[4 * qq] * gs + v[0], c0[4 * qq + 1] * gs + v[1]));
      pk[2 * qq + 1] = __builtin_bit_cast(unsigned, cvt2(c0[4 * qq + 2] * gs + v[2], c0[4 * qq + 3] * gs + v[3]));
    }
    const rsrc_t rsO = __builtin_amdgcn_make_buffer_rsrc((void*)out, 0, S_ * D_ * 2, 0x00020000);
    const unsigned orow = (st2 * 32 + r32) * 1024 + (64 * ct + 32 * br2) * 2 + h * 16;
#pragma unroll
    for (int qq = 0; qq < 4; qq += 2) {
      const auto s0 = __builtin_amdgcn_permlane32_swap(pk[2 * qq], pk[2 * qq + 2], false, false);
      const auto s1 = __builtin_amdgcn_permlane32_swap(pk[2 * qq + 1], pk[2 * qq + 3], false, false);
      const u32x4 v = {s0[0], s1[0], s0[1], s1[1]};
      st128<0>(v, rsO, orow + qq * 16);
    }
  }
}

extern "C" void kernel_launch(void* const* d_in, const int* in_sizes, int n_in, void* d_out, int out_size, void* d_ws, size_t ws_size,
                              hipStream_t stream) {
  (void)in_sizes; (void)n_in; (void)out_size;
  const u16* x = (const u16*)d_in[0];
  const u16* wA = (const u16*)d_in[1];
  const u16* woA = (const u16*)d_in[2];
  const u16* wB = (const u16*)d_in[4];
  const u16* woB = (const u16*)d_in[5];
  const u16* gw = (const u16*)d_in[6];
  (void)hipFuncSetAttribute((const void*)gated_mix_fused, hipFuncAttributeMaxDynamicSharedMemorySize, LDS_BYTES);
  gated_mix_fused<<<256, 512, LDS_BYTES, stream>>>(x, wA, wB, woA, woB, gw, (u16*)d_out, (char*)d_ws + ((ws_size / 2) & ~(size_t)0x1FFFFF));
}
```

```cpp
#define PW 1
#define PB 1
#define PM 0
#include <hip/hip_runtime.h>
#include <hip/hip_bf16.h>
#include <stdint.h>

#define LAS __attribute__((address_space(3)))
#define GAS __attribute__((address_space(1)))
#define DI __device__ __forceinline__

typedef unsigned short u16;
typedef __bf16 bf16x8 __attribute__((ext_vector_type(8)));
typedef __bf16 bf16x4 __attribute__((ext_vector_type(4)));
typedef __bf16 bf16x2 __attribute__((ext_vector_type(2)));
typedef float f32x16 __attribute__((ext_vector_type(16)));
typedef float f32x4 __attribute__((ext_vector_type(4)));
typedef float f32x2 __attribute__((ext_vector_type(2)));
typedef unsigned u32x4 __attribute__((ext_vector_type(4)));
typedef unsigned u32x2 __attribute__((ext_vector_type(2)));
typedef __amdgpu_buffer_rsrc_t rsrc_t;

#define ROT 6
#define CODE_PREFETCH 20480
#define CLR_STAGE 3
#define HPRIO 3
#define OD 10
#define PSLEEP 4
#define PDELAY 4
#define P2MAP 0
#define PDELAY1 1

constexpr int S_ = 2048, D_ = 512;
constexpr unsigned XR = 0;
constexpr unsigned WR = 49152;
constexpr unsigned WQ = 98304;
constexpr unsigned WOA = 0;
constexpr unsigned WOB = 98304;
constexpr unsigned SCR = 81920;
constexpr unsigned AFR = SCR + 12288;
constexpr unsigned EXC = SCR;
constexpr unsigned LDS_BYTES = 163840;
constexpr unsigned WS_EPOCH = 0;
constexpr unsigned WS_FLAGC = 4096;
constexpr unsigned WS_FLAGB = WS_FLAGC + 1024 * 128;
constexpr unsigned WS_GATE = WS_FLAGB + 1024 * 128;
constexpr unsigned WS_PART = WS_GATE + 8192;
constexpr unsigned PART_STRIDE = 640;
constexpr unsigned WS_O = WS_PART + 1024 * PART_STRIDE;
constexpr unsigned WS_END = WS_O + 64 * 64 * 1024;
constexpr unsigned SPIN_CAP = 1u << 18;

#define ST(i) do { if ((i) == PB && (PW == 1) == (wave >= 4)) { __builtin_amdgcn_sched_barrier(0); pX = (unsigned)__builtin_amdgcn_s_memrealtime() - pT0; \
   if (PW == 0 && lane == 0) __hip_atomic_store((GAS unsigned*)(ws + (12u << 20)) + blockIdx.x * 4 + (wave & 3), pX, __ATOMIC_RELAXED, __HIP_MEMORY_SCOPE_AGENT); __builtin_amdgcn_sched_barrier(0); } } while (0)
#define STR(i) do { if ((i) == 0) pT0 = (unsigned)__builtin_amdgcn_s_memrealtime(); } while (0)
#define STFLUSH() do { if (wave >= 4) { WAITV(0); if (PB == 99) pX = (unsigned)__builtin_amdgcn_s_memrealtime() - pT0; \
   if (PW == 0) pX = __builtin_amdgcn_readfirstlane(__hip_atomic_load((GAS unsigned*)(ws + (12u << 20)) + blockIdx.x * 4 + (wave & 3), __ATOMIC_RELAXED, __HIP_MEMORY_SCOPE_AGENT)); \
   unsigned X = pX; if (X > 3000) X = 3000; const unsigned long long until = __builtin_amdgcn_s_memrealtime() + (unsigned long long)X * PM; \
   while (__builtin_amdgcn_s_memrealtime() < until) __builtin_amdgcn_s_sleep(1); } } while (0)

DI void glds(unsigned voff, rsrc_t rs, unsigned soff, unsigned ldsaddr) {
  asm volatile("s_mov_b32 m0, %0\n\ts_nop 0\n\tbuffer_load_dwordx4 %1, %2, %3 offen lds"
               :: "s"(ldsaddr), "v"(voff), "s"(rs), "s"(soff) : "memory");
}
template <int AUX> DI void st128(u32x4 v, rsrc_t rs, unsigned off) { __builtin_amdgcn_raw_buffer_store_b128(v, rs, off, 0, AUX); }
#define WAITV(n) asm volatile("s_waitcnt vmcnt(" #n ")" ::: "memory")
#define WAITL0() asm volatile("s_waitcnt lgkmcnt(0)" ::: "memory")
#define BAR() asm volatile("s_barrier" ::: "memory")

DI f32x16 mfma(bf16x8 a, bf16x8 b, f32x16 c) { return __builtin_amdgcn_mfma_f32_32x32x16_bf16(a, b, c, 0, 0, 0); }
DI bf16x8 ldsr128(LAS char* L, unsigned a) { return *(LAS bf16x8*)(L + a); }
DI bf16x8 ldstr(LAS char* L, unsigned a0, unsigned a1) {
  bf16x4 lo = __builtin_amdgcn_ds_read_tr16_b64_v4bf16((LAS bf16x4*)(L + a0));
  bf16x4 hi = __builtin_amdgcn_ds_read_tr16_b64_v4bf16((LAS bf16x4*)(L + a1));
  return __builtin_shufflevector(lo, hi, 0, 1, 2, 3, 4, 5, 6, 7);
}
DI bf16x2 cvt2(float a, float b) { f32x2 p = {a, b}; return __builtin_convertvector(p, bf16x2); }
DI bf16x8 pack8(float a0, float a1, float a2, float a3, float a4, float a5, float a6, float a7) {
  bf16x2 p0 = cvt2(a0, a1), p1 = cvt2(a2, a3), p2 = cvt2(a4, a5), p3 = cvt2(a6, a7);
  bf16x4 lo = __builtin_shufflevector(p0, p1, 0, 1, 2, 3), hi = __builtin_shufflevector(p2, p3, 0, 1, 2, 3);
  return __builtin_shufflevector(lo, hi, 0, 1, 2, 3, 4, 5, 6, 7);
}
DI unsigned phi2(float a, float b) {
  const f32x2 t = {a, b};
  const f32x2 u = t * 1.44269504088896340736f;
  const f32x2 e = {__builtin_amdgcn_fmed3f(__builtin_amdgcn_exp2f(u[0]), 0.0f, 1.0f), __builtin_amdgcn_fmed3f(__builtin_amdgcn_exp2f(u[1]), 0.0f, 1.0f)};
  const int ia = __builtin_bit_cast(int, a), ib = __builtin_bit_cast(int, b);
  const f32x2 m = {__builtin_bit_cast(float, ia > 0 ? ia : 0), __builtin_bit_cast(float, ib > 0 ? ib : 0)};
  const f32x2 r = (m + e) + 1e-6f;
  return __builtin_bit_cast(unsigned, __builtin_convertvector(r, bf16x2));
}
DI bf16x8 phi8(const f32x16& x, int s) {
  const u32x4 v = {phi2(x[8 * s], x[8 * s + 1]), phi2(x[8 * s + 2], x[8 * s + 3]), phi2(x[8 * s + 4], x[8 * s + 5]), phi2(x[8 * s + 6], x[8 * s + 7])};
  return __builtin_bit_cast(bf16x8, v);
}
DI float bsel(unsigned m, float a, float b) {
  const unsigned ua = __builtin_bit_cast(unsigned, a), ub = __builtin_bit_cast(unsigned, b);
  return __builtin_bit_cast(float, (unsigned)__builtin_amdgcn_bitop3_b32(m, ub, ua, 0xCA));
}
DI float fadd(float a, float b) { float r; asm("v_add_f32 %0, %1, %2" : "=v"(r) : "v"(a), "v"(b)); return r; }
DI f32x4 fadd4(f32x4 a, f32x4 b) { return f32x4{fadd(a[0], b[0]), fadd(a[1], b[1]), fadd(a[2], b[2]), fadd(a[3], b[3])}; }
DI float ffma(float a, float b, float c) { float r; asm("v_fma_f32 %0, %1, %2, %3" : "=v"(r) : "v"(a), "v"(b), "v"(c)); return r; }
template <int CTRL, int RM> DI float dpp_add(float v) {
  return fadd(v, __builtin_bit_cast(float, __builtin_amdgcn_update_dpp(0, __builtin_bit_cast(int, v), CTRL, RM, 0xf, true)));
}
DI float wave_sum63(float v) {
  v = dpp_add<0x111, 0xf>(v); v = dpp_add<0x112, 0xf>(v); v = dpp_add<0x114, 0xf>(v); v = dpp_add<0x118, 0xf>(v);
  v = dpp_add<0x142, 0xa>(v); v = dpp_add<0x143, 0xc>(v);
  return v;
}
DI float sum_stride4(float v) {
  v += __builtin_bit_cast(float, __builtin_amdgcn_update_dpp(0, __builtin_bit_cast(int, v), 0x124, 0xf, 0xf, true));
  v += __builtin_bit_cast(float, __builtin_amdgcn_update_dpp(0, __builtin_bit_cast(int, v), 0x128, 0xf, 0xf, true));
  { const unsigned u = __builtin_bit_cast(unsigned, v); const auto t = __builtin_amdgcn_permlane16_swap(u, u, false, false);
    v = __builtin_bit_cast(float, (unsigned)t[0]) + __builtin_bit_cast(float, (unsigned)t[1]); }
  { const unsigned u = __builtin_bit_cast(unsigned, v); const auto t = __builtin_amdgcn_permlane32_swap(u, u, false, false);
    v = __builtin_bit_cast(float, (unsigned)t[0]) + __builtin_bit_cast(float, (unsigned)t[1]); }
  return v;
}
constexpr unsigned SENT = 0xFFFFFFFFu;
DI unsigned nosent(unsigned v) { return v < SENT ? v : SENT - 1u; }
DI u32x4 nosent4(u32x4 v) { return u32x4{nosent(v[0]), nosent(v[1]), nosent(v[2]), nosent(v[3])}; }
DI unsigned umax3(unsigned a, unsigned b, unsigned c) { unsigned m = a > b ? a : b; return m > c ? m : c; }
#define SB() __builtin_amdgcn_sched_barrier(0)
DI bf16x8 cvt8(const f32x16& x, int s) {
  return pack8(x[8 * s], x[8 * s + 1], x[8 * s + 2], x[8 * s + 3], x[8 * s + 4], x[8 * s + 5], x[8 * s + 6], x[8 * s + 7]);
}
DI float sum8(bf16x8 v, float acc) {
  const bf16x2 ones = {(__bf16)1.0f, (__bf16)1.0f};
  acc = __builtin_amdgcn_fdot2_f32_bf16(__builtin_shufflevector(v, v, 0, 1), ones, acc, false);
  acc = __builtin_amdgcn_fdot2_f32_bf16(__builtin_shufflevector(v, v, 2, 3), ones, acc, false);
  acc = __builtin_amdgcn_fdot2_f32_bf16(__builtin_shufflevector(v, v, 4, 5), ones, acc, false);
  acc = __builtin_amdgcn_fdot2_f32_bf16(__builtin_shufflevector(v, v, 6, 7), ones, acc, false);
  return acc;
}
DI unsigned long long ld_flag(GAS char* ws, unsigned off) {
  return __hip_atomic_load((GAS unsigned long long*)(ws + off), __ATOMIC_RELAXED, __HIP_MEMORY_SCOPE_AGENT);
}
DI void st_flag(GAS char* ws, unsigned off, unsigned long long v) {
  __hip_atomic_store((GAS unsigned long long*)(ws + off), v, __ATOMIC_RELAXED, __HIP_MEMORY_SCOPE_AGENT);
}
DI unsigned long long epoch_issue(const char* wsp) {
  unsigned long long e;
  asm volatile("s_load_dwordx2 %0, %1, 0x0" : "=s"(e) : "s"(wsp));
  return e;
}
DI unsigned long long epoch_wait(unsigned long long e) {
  asm volatile("s_waitcnt lgkmcnt(0)" : "+s"(e) :: "memory");
  return e + 1ull;
}
DI void wait_flags(GAS char* ws, unsigned off, unsigned long long tag) {
  for (unsigned spins = 0; spins < SPIN_CAP; ++spins) {
    const unsigned long long v = ld_flag(ws, off);
    if (__all(v == tag)) break;
    __builtin_amdgcn_s_sleep(PSLEEP);
  }
  asm volatile("" ::: "memory");
}
DI unsigned ld_u32(GAS char* ws, unsigned off) {
  return __hip_atomic_load((GAS unsigned*)(ws + off), __ATOMIC_RELAXED, __HIP_MEMORY_SCOPE_AGENT);
}

__global__ __launch_bounds__(512, 2) void gated_mix_fused(const u16* __restrict__ x, const u16* __restrict__ wA, const u16* __restrict__ wB,
                                                          const u16* __restrict__ woA, const u16* __restrict__ woB, const u16* __restrict__ gw,
                                                          u16* __restrict__ out, char* __restrict__ wsp
                                                          ) {
  extern __shared__ __attribute__((aligned(16))) char lds[];
  LAS char* L = (LAS char*)lds;
  const int tid = threadIdx.x;
  const int wave = __builtin_amdgcn_readfirstlane(tid >> 6), lane = tid & 63;
  const int b = blockIdx.x, xcd = b & 7, r = b >> 3;
  const int ti = (xcd & 1) * 8 + (r & 7), g = (xcd >> 1) * 4 + (r >> 3);
  const int gg = g & 7;
  const int rt = P2MAP ? 8 * (xcd >> 1) + (r & 7) : 4 * xcd + (r & 3), ct = P2MAP ? 4 * (xcd & 1) + (r >> 3) : r >> 2;
  GAS char* ws = (GAS char*)wsp;
  asm volatile("" :: "s"(x), "s"(wA), "s"(wB), "s"(woA), "s"(woB), "s"(gw), "s"(out), "s"(wsp));
  unsigned pX = 0, pT0 = 0;
  STR(0); ST(1);
  unsigned long long pc;
  asm volatile("s_getpc_b64 %0" : "=s"(pc));

  if (wave < 4) {
    const unsigned long long epoch_raw = epoch_issue(wsp);
    const int wr = wave >> 1, wc = wave & 1;
    const int r32 = lane & 31, h = lane >> 5;
    const int g1 = (lane >> 4) & 1, q = (lane & 15) >> 2, p = lane & 3;
    const int own = wave, oth = wave ^ 1;
    const rsrc_t rsWS = __builtin_amdgcn_make_buffer_rsrc((void*)wsp, 0, WS_END, 0x00020000);
    unsigned ctouch = 0;
    {
      const unsigned off = (wave * 64 + lane) * 128;
      if (off < CODE_PREFETCH) { const char* a = (const char*)pc + off; asm volatile("global_load_dword %0, %1, off" : "=v"(ctouch) : "v"(a) : "memory"); }
    }
    {
      const u32x4 sent = {SENT, SENT, SENT, SENT};
      const unsigned po = WS_PART + ((g * 4 + wave) * 16 + ti) * PART_STRIDE;
      if (lane < 36) st128<16>(sent, rsWS, lane * 16 + po);
    }
    const unsigned fx = (h ^ ((r32 >> 1) & 7)) << 4;
    unsigned xo0 = XR + (own * 32 + r32) * 128 + fx, xt0 = XR + (oth * 32 + r32) * 128 + fx;
    const unsigned sw = ((2 * g1 + (p >> 1)) ^ (((q >> 1) & 1) << 2)) << 4;
    const unsigned wn0 = (8 * h + q) * 128 + sw + (p & 1) * 8;
    unsigned wkv0 = WR + (wn0 ^ (wc << 6));
    asm volatile("" : "+v"(wkv0));
    unsigned xoa[4], xta[4];
#pragma unroll
    for (int k = 0; k < 4; ++k) { xoa[k] = xo0 ^ (k << 5); xta[k] = xt0 ^ (k << 5); asm volatile("" : "+v"(xoa[k]), "+v"(xta[k])); }
    const unsigned long long tag = epoch_wait(epoch_raw);

    bf16x8 xr[32];
    f32x16 kO = {}, kT = {}, vO = {}, vT = {};
    {
      BAR();
      ST(3);
      bf16x8 xt = ldsr128(L, xta[0]), wk = ldstr(L, wkv0, wkv0 + 512), wv = ldstr(L, wkv0 + 8192, wkv0 + 8192 + 512);
      xr[0] = ldsr128(L, xoa[0]);
#pragma unroll
      for (int t = 0; t < 32; ++t) {
        bf16x8 nxt, nwk, nwv;
        const int s1 = (t + 1) >> 2, k1 = (t + 1) & 3;
        const unsigned slot = (s1 % 3) * 16384;
        if (t + 1 < 32 && k1 == 0) {
          WAITL0();
          if (s1 == CLR_STAGE) {
            WAITV(0);
            asm volatile("" :: "v"(ctouch));
            if (lane == 0) st_flag(ws, WS_FLAGC + (g * 4 + wave) * 128 + ti * 8, tag);
          }
          BAR();
        }
        SB();
        kO = mfma(xr[t], wk, kO);
        SB();
        if (t + 1 < 32) {
          xr[t + 1] = ldsr128(L, xoa[k1] + slot);
          nwk = ldstr(L, wkv0 + slot + k1 * 2048, wkv0 + slot + k1 * 2048 + 512);
        }
        SB();
        kT = mfma(xt, wk, kT);
        SB();
        if (t + 1 < 32) {
          nxt = ldsr128(L, xta[k1] + slot);
          nwv = ldstr(L, wkv0 + slot + 8192 + k1 * 2048, wkv0 + slot + 8192 + k1 * 2048 + 512);
        }
        SB();
        vO = mfma(xr[t], wv, vO);
        vT = mfma(xt, wv, vT);
        SB();
        if (t + 1 < 32) { xt = nxt; wk = nwk; wv = nwv; }
      }
    }
    ST(4);
    {
      const bf16x8 pa0 = phi8(kO, 0), pa1 = phi8(kO, 1), pb0 = phi8(kT, 0), pb1 = phi8(kT, 1);
      const bf16x8 va0 = cvt8(vO, 0), va1 = cvt8(vO, 1), vb0 = cvt8(vT, 0), vb1 = cvt8(vT, 1);
      float zs = sum8(pb1, sum8(pb0, sum8(pa1, sum8(pa0, 0.f))));
      SB();
      f32x16 kv = {};
      kv = mfma(pa0, va0, kv); kv = mfma(pb0, vb0, kv); kv = mfma(pa1, va1, kv); kv = mfma(pb1, vb1, kv);
      SB();
      {
        const unsigned zb = __builtin_bit_cast(unsigned, zs);
        const auto sw2 = __builtin_amdgcn_permlane32_swap(zb, zb, false, false);
        zs = __builtin_bit_cast(float, (unsigned)sw2[0]) + __builtin_bit_cast(float, (unsigned)sw2[1]);
      }
      const unsigned gm = g1 ? 0xFFFFFFFFu : 0u;
      f32x4 lo, hi;
#pragma unroll
      for (int j = 0; j < 4; ++j) { lo[j] = bsel(gm, kv[j], kv[8 + j]); hi[j] = bsel(gm, kv[4 + j], kv[12 + j]); }
      const int hh = 2 * wc + g1;
      const unsigned a = SCR + (4 * wr + hh) * 1024 + (h * 16 + (lane & 15)) * 16;
      *(LAS f32x4*)(L + a) = lo;
      *(LAS f32x4*)(L + a + 512) = hi;
      if (h == 0) *(LAS float*)(L + SCR + (8 + hh) * 1024 + wr * 64 + (lane & 15) * 4) = zs;
    }
    WAITL0();
    ST(5);
    BAR();
    ST(6);
    f32x16 qa = {}, qb = {};
    {
      unsigned wq0 = WQ + wn0, wq1 = WQ + (wn0 ^ 64);
      asm volatile("" : "+v"(wq0), "+v"(wq1));
      bf16x8 a0[3], a1[3];
      a0[0] = ldstr(L, wq0, wq0 + 512); a1[0] = ldstr(L, wq1, wq1 + 512);
      a0[1] = ldstr(L, wq0 + 2048, wq0 + 2048 + 512); a1[1] = ldstr(L, wq1 + 2048, wq1 + 2048 + 512);
#pragma unroll
      for (int t = 0; t < 32; ++t) {
        const unsigned o = (t + 2) * 2048;
        SB();
        qa = mfma(a0[t % 3], xr[t], qa);
        SB();
        if (t + 2 < 32) a0[(t + 2) % 3] = ldstr(L, wq0 + o, wq0 + o + 512);
        SB();
        qb = mfma(a1[t % 3], xr[t], qb);
        SB();
        if (t + 2 < 32) a1[(t + 2) % 3] = ldstr(L, wq1 + o, wq1 + o + 512);
      }
      SB();
    }
    ST(7);
    bf16x8 pq[4];
    pq[0] = phi8(qa, 0); pq[1] = phi8(qa, 1); pq[2] = phi8(qb, 0); pq[3] = phi8(qb, 1);
    asm volatile("" : "+v"(pq[0]), "+v"(pq[1]), "+v"(pq[2]), "+v"(pq[3]));
    WAITL0();
    ST(8);
    BAR();
    ST(9);
    {
      const unsigned st = 4 * ti + own;
      bf16x8 af[4];
#pragma unroll
      for (int hh = 0; hh < 4; ++hh) af[hh] = ldsr128(L, AFR + hh * 1024 + lane * 16);
      SB();
      f32x16 d[4];
#pragma unroll
      for (int hh = 0; hh < 4; ++hh) { d[hh] = f32x16{}; d[hh] = mfma(af[hh], pq[hh], d[hh]); }
      SB();
#pragma unroll
      for (int hh = 0; hh < 4; ++hh) {
        const float inv = __builtin_amdgcn_rcpf(d[hh][8] + 1e-6f);
        const u32x4 o = __builtin_bit_cast(u32x4, pack8(d[hh][0] * inv, d[hh][1] * inv, d[hh][2] * inv, d[hh][3] * inv, d[hh][4] * inv, d[hh][5] * inv, d[hh][6] * inv, d[hh][7] * inv));
        st128<16>(o, rsWS, lane * 16 + WS_O + (st * 64 + 4 * g + hh) * 1024);
      }
    }
    ST(10);
    BAR();
    WAITV(0);
    if (lane == 0) st_flag(ws, WS_FLAGB + (4 * ti + own) * 128 + g * 8, tag);
    ST(11);
    STR(16); STFLUSH();
    return;
  }

  const int hw = wave - 4;
  const unsigned long long epoch_raw = epoch_issue(wsp);
  __builtin_amdgcn_s_setprio(HPRIO);
  int ro = 0;
  if (ROT == 1) ro = (ti + 2 * g) & 7;
  if (ROT == 2) ro = 2 * ((ti + g) & 1);
  if (ROT == 3) ro = 4 * ((ti + g) & 1);
  if (ROT == 5) ro = ((ti + g) % 3) * 2;
  if (ROT == 6) ro = 2 * ((ti + g) & 3);
  const u16* w = (g >= 8) ? wB : wA;
  const rsrc_t rsX = __builtin_amdgcn_make_buffer_rsrc((void*)x, 0, S_ * D_ * 2, 0x00020000);
  const rsrc_t rsW = __builtin_amdgcn_make_buffer_rsrc((void*)w, 0, D_ * 3 * D_ * 2, 0x00020000);
  const int rin = lane >> 3, ch = lane & 7;
  const unsigned vx = rin * 1024 + ((ch ^ (4 * (hw & 1) + (rin >> 1))) << 4);
  const unsigned csw = (ch ^ (((rin >> 1) & 1) << 2)) << 4;
  const unsigned vw = rin * 3072 + csw;
  const unsigned sx0 = (ti * 128 + 8 * hw) * 1024;
  const unsigned sw0 = 8 * hw * 3072 + gg * 128;
  auto stage = [&](int s) {
    const unsigned ks = (s + ro) & 7, slot = (s % 3) * 16384;
#pragma unroll
    for (int i = 0; i < 4; ++i) glds(vx, rsX, sx0 + ks * 128 + i * 32768, XR + slot + (hw + 4 * i) * 1024);
#pragma unroll
    for (int i = 0; i < 2; ++i) glds(vw, rsW, sw0 + ks * 196608 + i * 98304 + 1024, WR + slot + (hw + 4 * i) * 1024);
#pragma unroll
    for (int i = 0; i < 2; ++i) glds(vw, rsW, sw0 + ks * 196608 + i * 98304 + 2048, WR + slot + 8192 + (hw + 4 * i) * 1024);
  };
  auto stageq = [&](int s) {
    const unsigned ks = (s + ro) & 7;
#pragma unroll
    for (int i = 0; i < 2; ++i) glds(vw, rsW, sw0 + ks * 196608 + i * 98304, WQ + s * 8192 + (hw + 4 * i) * 1024);
  };
  ST(15);
  unsigned wtouch = 0;
  auto wo_touch = [&]() {
    if (P2MAP) {
      const unsigned row = r * 32 + hw * 8 + ((lane & 31) >> 2);
      const char* a = (const char*)((row & 512) ? woB : woA) + (row & 511) * 1024 + (xcd & 1) * 512 + (lane & 3) * 128;
      if (lane < 32) asm volatile("global_load_dword %0, %1, off" : "=v"(wtouch) : "v"(a) : "memory");
    } else {
      const unsigned row = r * 32 + hw * 8 + (lane >> 3);
      const char* a = (const char*)((row & 512) ? woB : woA) + (row & 511) * 1024 + (lane & 7) * 128;
      asm volatile("global_load_dword %0, %1, off" : "=v"(wtouch) : "v"(a) : "memory");
    }
  };
  stage(0);
  stage(1);
  ST(2);
  WAITV(8);
  const unsigned long long tag = epoch_wait(epoch_raw);
  BAR();
  ST(3);
  const rsrc_t rsG = __builtin_amdgcn_make_buffer_rsrc((void*)gw, 0, D_ * 2, 0x00020000);
  const unsigned grow = ti * 128 + 8 * g + 2 * hw;
  u32x4 x0, x1, gv;
  const unsigned foff = WS_FLAGC + (g * 4 + hw) * 128 + (lane & 15) * 8;
  u32x2 fcv;
#pragma unroll
  for (int s = 1; s < 8; ++s) {
    if (s < 6) { stage(s + 1); WAITV(8); }
    else if (s == 6) {
      stage(7);
      asm volatile("buffer_load_dwordx4 %0, %4, %5, %7 offen\n\tbuffer_load_dwordx4 %1, %4, %5, %7 offen offset:1024\n\tbuffer_load_dwordx4 %2, %4, %6, 0 offen\n\t"
                   "global_load_dwordx2 %3, %8, %9 sc1"
                   : "=&v"(x0), "=&v"(x1), "=&v"(gv), "=&v"(fcv) : "v"(lane * 16), "s"(rsX), "s"(rsG), "s"(grow * 1024), "v"(foff), "s"(wsp) : "memory");
      WAITV(12);
    } else {
      stageq(0); stageq(1); stageq(2); stageq(3);
      WAITV(12);
    }
    BAR();
  }
  ST(4);
  stageq(4); stageq(5); stageq(6); stageq(7);
  const rsrc_t rsWS = __builtin_amdgcn_make_buffer_rsrc((void*)wsp, 0, WS_END, 0x00020000);
  const rsrc_t rsOA = __builtin_amdgcn_make_buffer_rsrc((void*)woA, 0, D_ * D_ * 2, 0x00020000);
  const rsrc_t rsOB = __builtin_amdgcn_make_buffer_rsrc((void*)woB, 0, D_ * D_ * 2, 0x00020000);
  const unsigned vo = rin * 1024 + csw;
  const unsigned hid = g * 4 + hw;
  wo_touch();
  asm volatile("s_waitcnt vmcnt(17)" : "+v"(x0), "+v"(x1), "+v"(gv), "+v"(fcv) :: "memory");
  if (!__all((((unsigned long long)fcv[1] << 32) | fcv[0]) == tag)) wait_flags(ws, foff, tag);
  float gval;
  {
    float a0 = 0.f, a1 = 0.f;
#pragma unroll
    for (int i = 0; i < 4; ++i) {
      const unsigned ug = gv[i], u0 = x0[i], u1 = x1[i];
      const float gl = __builtin_bit_cast(float, ug << 16), gh = __builtin_bit_cast(float, ug & 0xffff0000u);
      a0 = ffma(__builtin_bit_cast(float, u0 << 16), gl, a0); a0 = ffma(__builtin_bit_cast(float, u0 & 0xffff0000u), gh, a0);
      a1 = ffma(__builtin_bit_cast(float, u1 << 16), gl, a1); a1 = ffma(__builtin_bit_cast(float, u1 & 0xffff0000u), gh, a1);
    }
    a0 = wave_sum63(a0); a1 = wave_sum63(a1);
    const float s0 = __builtin_bit_cast(float, __builtin_amdgcn_readlane(__builtin_bit_cast(int, a0), 63));
    const float s1 = __builtin_bit_cast(float, __builtin_amdgcn_readlane(__builtin_bit_cast(int, a1), 63));
    const float lg = (lane & 1) ? s1 : s0;
    gval = __builtin_amdgcn_rcpf(1.0f + __builtin_amdgcn_exp2f(-1.44269504088896340736f * lg));
  }
  asm volatile("s_waitcnt vmcnt(1)" : "+v"(gval) :: "memory");
  ST(5);
  BAR();
  ST(6);
  {
    const f32x4 p0 = *(LAS f32x4*)(L + SCR + hw * 1024 + lane * 16), p1 = *(LAS f32x4*)(L + SCR + (4 + hw) * 1024 + lane * 16);
    const f32x4 z0 = *(LAS f32x4*)(L + SCR + (8 + hw) * 1024 + (lane & 3) * 16), z1 = *(LAS f32x4*)(L + SCR + (8 + hw) * 1024 + 64 + (lane & 3) * 16);
    const unsigned po = WS_PART + (hid * 16 + ti) * PART_STRIDE;
    const f32x4 ps = fadd4(p0, p1);
    const unsigned d0 = __builtin_bit_cast(unsigned, cvt2(ps[0], ps[1])), d1 = __builtin_bit_cast(unsigned, cvt2(ps[2], ps[3]));
    const auto e0 = __builtin_amdgcn_permlane32_swap(d0, d0, false, false), e1 = __builtin_amdgcn_permlane32_swap(d1, d1, false, false);
    const u32x4 kvp = {e0[0], e1[0], e0[1], e1[1]};
    const u32x4 zp = __builtin_bit_cast(u32x4, fadd4(z0, z1));
    u32x4 pay;
#pragma unroll
    for (int i = 0; i < 4; ++i) pay[i] = nosent(lane < 32 ? kvp[i] : zp[i]);
    if (lane < 36) st128<16>(pay, rsWS, lane * 16 + po);
    if (lane < 2) __hip_atomic_store((GAS unsigned*)(ws + WS_GATE) + grow + lane, __builtin_bit_cast(unsigned, gval), __ATOMIC_RELAXED, __HIP_MEMORY_SCOPE_AGENT);
  }
  ST(7);
#pragma unroll
  for (int i = 0; i < 16; ++i) glds(vo, rsOA, (hw + 4 * i) * 8192 + ct * 128, WOA + (hw + 4 * i) * 1024);
  asm volatile("" :: "v"(wtouch));
  ST(8);
  const unsigned pb = WS_PART + hid * 16 * PART_STRIDE;
  for (int i = 0; i < PDELAY1; ++i) __builtin_amdgcn_s_sleep(4);
  ST(9);
  {
    float a[8];
    f32x4 z;
    u32x4 pv[8], zv;
    auto gather = [&]() {
#pragma unroll
      for (int i = 0; i < 8; ++i) pv[i] = __builtin_amdgcn_raw_buffer_load_b128(rsWS, (lane >> 5) * PART_STRIDE + (lane & 31) * 16, pb + 2 * i * PART_STRIDE, 16);
      zv = __builtin_amdgcn_raw_buffer_load_b128(rsWS, (lane >> 2) * PART_STRIDE + 512 + (lane & 3) * 16, pb, 16);
    };
    auto reduce = [&]() {
      unsigned clo = 0x00003F80u, chi = 0x3F800000u;
      asm volatile("" : "+s"(clo), "+s"(chi));
      const bf16x2 lo1 = __builtin_bit_cast(bf16x2, clo), hi1 = __builtin_bit_cast(bf16x2, chi);
      unsigned mx = 0;
#pragma unroll
      for (int k = 0; k < 8; ++k) a[k] = 0.f;
#pragma unroll
      for (int i = 0; i < 8; ++i) {
#pragma unroll
        for (int k = 0; k < 4; ++k) {
          const unsigned u = pv[i][k];
          a[2 * k] = __builtin_amdgcn_fdot2_f32_bf16(__builtin_bit_cast(bf16x2, u), lo1, a[2 * k], false);
          a[2 * k + 1] = __builtin_amdgcn_fdot2_f32_bf16(__builtin_bit_cast(bf16x2, u), hi1, a[2 * k + 1], false);
        }
        mx = umax3(pv[i][0], pv[i][1], umax3(pv[i][2], pv[i][3], mx));
      }
      mx = umax3(zv[0], zv[1], umax3(zv[2], zv[3], mx));
      z = __builtin_bit_cast(f32x4, zv);
      return __all(mx != SENT);
    };
    gather();
    asm volatile("" ::: "memory");
    if (!reduce()) {
      for (unsigned spins = 0; spins < SPIN_CAP; ++spins) { gather(); if (reduce()) break; }
    }
#pragma unroll
    for (int k = 0; k < 8; ++k) {
      const unsigned u = __builtin_bit_cast(unsigned, a[k]);
      const auto t = __builtin_amdgcn_permlane32_swap(u, u, false, false);
      a[k] = __builtin_bit_cast(float, (unsigned)t[0]) + __builtin_bit_cast(float, (unsigned)t[1]);
    }
    z = f32x4{sum_stride4(z[0]), sum_stride4(z[1]), sum_stride4(z[2]), sum_stride4(z[3])};
    const int slot = lane & 31;
    if (lane < 32) *(LAS bf16x8*)(L + AFR + hw * 1024 + ((slot >> 4) * 32 + (slot & 15)) * 16) = pack8(a[0], a[1], a[2], a[3], a[4], a[5], a[6], a[7]);
    const int q4 = lane & 3;
    u32x2 kz = {__builtin_bit_cast(unsigned, cvt2(z[0], z[1])), __builtin_bit_cast(unsigned, cvt2(z[2], z[3]))};
    *(LAS u32x2*)(L + AFR + hw * 1024 + ((q4 & 1) * 32 + 16 + (lane >> 2)) * 16 + (q4 >> 1) * 8) = kz;
  }
  WAITV(0);
  WAITL0();
  ST(10);
  BAR();
  ST(11);
#pragma unroll
  for (int i = 0; i < 16; ++i) glds(vo, rsOB, (hw + 4 * i) * 8192 + ct * 128, WOB + (hw + 4 * i) * 1024);
  WAITV(0);
  ST(12);
  BAR();
  {
    const int r32 = lane & 31, h = lane >> 5, g1 = (lane >> 4) & 1, q = (lane & 15) >> 2, p = lane & 3;
    const unsigned sw = ((2 * g1 + (p >> 1)) ^ (((q >> 1) & 1) << 2)) << 4;
    const int br2 = hw >> 1, sh = hw & 1;
    const unsigned st2 = 2 * rt + sh;
    const unsigned ob = WS_O + (st2 * 64 + 32 * br2) * 1024;
    for (int i = 0; i < PDELAY; ++i) __builtin_amdgcn_s_sleep(8);
    wait_flags(ws, WS_FLAGB + st2 * 128 + (lane & 15) * 8, tag);
    ST(13);
    unsigned wp0 = ((br2 ? WOB : WOA) + (4 * h + q) * 128 + sw + (p & 1) * 8) ^ (br2 << 6), wp1 = wp0 ^ 64;
    asm volatile("" : "+v"(wp0), "+v"(wp1));
    f32x16 c0 = {}, c1 = {};
    const unsigned gvb = ld_u32(ws, WS_GATE + (st2 * 32 + r32) * 4);
    {
      u32x4 of[32];
#pragma unroll
      for (int j = 0; j < OD; ++j) {
        of[j] = __builtin_amdgcn_raw_buffer_load_b128(rsWS, lane * 16, ob + j * 1024, 16);
        SB();
      }
      bf16x8 a0[3], a1[3];
      a0[0] = ldstr(L, wp0, wp0 + 1024); a1[0] = ldstr(L, wp1, wp1 + 1024);
      a0[1] = ldstr(L, wp0 + 2048, wp0 + 2048 + 1024); a1[1] = ldstr(L, wp1 + 2048, wp1 + 2048 + 1024);
#pragma unroll
      for (int j = 0; j < 32; ++j) {
        const unsigned o = (j + 2) * 2048;
        const bf16x8 bf = __builtin_bit_cast(bf16x8, of[j]);
        SB();
        c0 = mfma(a0[j % 3], bf, c0);
        SB();
        if (j + OD < 32) of[j + OD] = __builtin_amdgcn_raw_buffer_load_b128(rsWS, lane * 16, ob + (j + OD) * 1024, 16);
        if (j + 2 < 32) a0[(j + 2) % 3] = ldstr(L, wp0 + o, wp0 + o + 1024);
        SB();
        c1 = mfma(a1[j % 3], bf, c1);
        SB();
        if (j + 2 < 32) a1[(j + 2) % 3] = ldstr(L, wp1 + o, wp1 + o + 1024);
      }
      SB();
    }
    ST(14);
    __builtin_amdgcn_s_setprio(0);
    const float gv = __builtin_bit_cast(float, gvb);
    const float gs = br2 ? 1.0f - gv : gv;
#pragma unroll
    for (int qq = 0; qq < 4; ++qq) {
      const f32x4 v = {c1[4 * qq] * gs, c1[4 * qq + 1] * gs, c1[4 * qq + 2] * gs, c1[4 * qq + 3] * gs};
      *(LAS f32x4*)(L + EXC + hw * 4096 + qq * 1024 + lane * 16) = v;
    }
    WAITL0();
    BAR();
    if (b == 0 && hw == 0 && lane == 0) st_flag(ws, WS_EPOCH, tag);
    unsigned pk[8];
#pragma unroll
    for (int qq = 0; qq < 4; ++qq) {
      const f32x4 v = *(LAS f32x4*)(L + EXC + (hw ^ 2) * 4096 + qq * 1024 + lane * 16);
      pk[2 * qq] = __builtin_bit_cast(unsigned, cvt2(c0[4 * qq] * gs + v[0], c0[4 * qq + 1] * gs + v[1]));
      pk[2 * qq + 1] = __builtin_bit_cast(unsigned, cvt2(c0[4 * qq + 2] * gs + v[2], c0[4 * qq + 3] * gs + v[3]));
    }
    const rsrc_t rsO = __builtin_amdgcn_make_buffer_rsrc((void*)out, 0, S_ * D_ * 2, 0x00020000);
    const unsigned orow = (st2 * 32 + r32) * 1024 + (64 * ct + 32 * br2) * 2 + h * 16;
#pragma unroll
    for (int qq = 0; qq < 4; qq += 2) {
      const auto s0 = __builtin_amdgcn_permlane32_swap(pk[2 * qq], pk[2 * qq + 2], false, false);
      const auto s1 = __builtin_amdgcn_permlane32_swap(pk[2 * qq + 1], pk[2 * qq + 3], false, false);
      const u32x4 v = {s0[0], s1[0], s0[1], s1[1]};
      st128<0>(v, rsO, orow + qq * 16);
    }
    STR(16); STFLUSH();
  }
}

extern "C" void kernel_launch(void* const* d_in, const int* in_sizes, int n_in, void* d_out, int out_size, void* d_ws, size_t ws_size,
                              hipStream_t stream) {
  (void)in_sizes; (void)n_in; (void)out_size;
  const u16* x = (const u16*)d_in[0];
  const u16* wA = (const u16*)d_in[1];
  const u16* woA = (const u16*)d_in[2];
  const u16* wB = (const u16*)d_in[4];
  const u16* woB = (const u16*)d_in[5];
  const u16* gw = (const u16*)d_in[6];
  (void)hipFuncSetAttribute((const void*)gated_mix_fused, hipFuncAttributeMaxDynamicSharedMemorySize, LDS_BYTES);
  gated_mix_fused<<<256, 512, LDS_BYTES, stream>>>(x, wA, wB, woA, woB, gw, (u16*)d_out, (char*)d_ws + ((ws_size / 2) & ~(size_t)0x1FFFFF));
}
```

```cpp
#include <hip/hip_runtime.h>
#include <hip/hip_bf16.h>
#include <stdint.h>

#define LAS __attribute__((address_space(3)))
#define GAS __attribute__((address_space(1)))
#define DI __device__ __forceinline__

typedef unsigned short u16;
typedef __bf16 bf16x8 __attribute__((ext_vector_type(8)));
typedef __bf16 bf16x4 __attribute__((ext_vector_type(4)));
typedef __bf16 bf16x2 __attribute__((ext_vector_type(2)));
typedef float f32x16 __attribute__((ext_vector_type(16)));
typedef float f32x4 __attribute__((ext_vector_type(4)));
typedef float f32x2 __attribute__((ext_vector_type(2)));
typedef unsigned u32x4 __attribute__((ext_vector_type(4)));
typedef unsigned u32x2 __attribute__((ext_vector_type(2)));
typedef __amdgpu_buffer_rsrc_t rsrc_t;

#define ROT 6
#define CODE_PREFETCH 20480
#define CLR_STAGE 3
#define HPRIO 3
#define OD 10
#define PSLEEP 4
#define PDELAY 3
#define P2MAP 0
#define BWLATE 1
#define PBACK 8
#define FPACK 1
#define PDELAY1 1

constexpr int S_ = 2048, D_ = 512;
constexpr unsigned XR = 0;
constexpr unsigned WR = 49152;
constexpr unsigned WQ = 98304;
constexpr unsigned WOA = 0;
constexpr unsigned WOB = 98304;
constexpr unsigned SCR = 81920;
constexpr unsigned AFR = SCR + 12288;
constexpr unsigned EXC = SCR;
constexpr unsigned LDS_BYTES = 163840;
constexpr unsigned WS_EPOCH = 0;
constexpr unsigned WS_FLAGC = 4096;
constexpr unsigned WS_FLAGB = WS_FLAGC + 1024 * 128;
constexpr unsigned WS_GATE = WS_FLAGB + 1024 * 128;
constexpr unsigned WS_PART = WS_GATE + 8192;
constexpr unsigned PART_STRIDE = 640;
constexpr unsigned WS_O = WS_PART + 1024 * PART_STRIDE;
constexpr unsigned WS_END = WS_O + 64 * 64 * 1024;
constexpr unsigned SPIN_CAP = 1u << 18;

DI void glds(unsigned voff, rsrc_t rs, unsigned soff, unsigned ldsaddr) {
  asm volatile("s_mov_b32 m0, %0\n\ts_nop 0\n\tbuffer_load_dwordx4 %1, %2, %3 offen lds"
               :: "s"(ldsaddr), "v"(voff), "s"(rs), "s"(soff) : "memory");
}
template <int AUX> DI void st128(u32x4 v, rsrc_t rs, unsigned off) { __builtin_amdgcn_raw_buffer_store_b128(v, rs, off, 0, AUX); }
#define WAITV(n) asm volatile("s_waitcnt vmcnt(" #n ")" ::: "memory")
#define WAITL0() asm volatile("s_waitcnt lgkmcnt(0)" ::: "memory")
#define BAR() asm volatile("s_barrier" ::: "memory")

DI f32x16 mfma(bf16x8 a, bf16x8 b, f32x16 c) { return __builtin_amdgcn_mfma_f32_32x32x16_bf16(a, b, c, 0, 0, 0); }
DI bf16x8 ldsr128(LAS char* L, unsigned a) { return *(LAS bf16x8*)(L + a); }
DI bf16x8 ldstr(LAS char* L, unsigned a0, unsigned a1) {
  bf16x4 lo = __builtin_amdgcn_ds_read_tr16_b64_v4bf16((LAS bf16x4*)(L + a0));
  bf16x4 hi = __builtin_amdgcn_ds_read_tr16_b64_v4bf16((LAS bf16x4*)(L + a1));
  return __builtin_shufflevector(lo, hi, 0, 1, 2, 3, 4, 5, 6, 7);
}
DI bf16x2 cvt2(float a, float b) { f32x2 p = {a, b}; return __builtin_convertvector(p, bf16x2); }
DI bf16x8 pack8(float a0, float a1, float a2, float a3, float a4, float a5, float a6, float a7) {
  bf16x2 p0 = cvt2(a0, a1), p1 = cvt2(a2, a3), p2 = cvt2(a4, a5), p3 = cvt2(a6, a7);
  bf16x4 lo = __builtin_shufflevector(p0, p1, 0, 1, 2, 3), hi = __builtin_shufflevector(p2, p3, 0, 1, 2, 3);
  return __builtin_shufflevector(lo, hi, 0, 1, 2, 3, 4, 5, 6, 7);
}
DI unsigned phi2(float a, float b) {
  const f32x2 t = {a, b};
  const f32x2 u = t * 1.44269504088896340736f;
  const f32x2 e = {__builtin_amdgcn_fmed3f(__builtin_amdgcn_exp2f(u[0]), 0.0f, 1.0f), __builtin_amdgcn_fmed3f(__builtin_amdgcn_exp2f(u[1]), 0.0f, 1.0f)};
  const int ia = __builtin_bit_cast(int, a), ib = __builtin_bit_cast(int, b);
  const f32x2 m = {__builtin_bit_cast(float, ia > 0 ? ia : 0), __builtin_bit_cast(float, ib > 0 ? ib : 0)};
  const f32x2 r = (m + e) + 1e-6f;
  return __builtin_bit_cast(unsigned, __builtin_convertvector(r, bf16x2));
}
DI bf16x8 phi8(const f32x16& x, int s) {
  const u32x4 v = {phi2(x[8 * s], x[8 * s + 1]), phi2(x[8 * s + 2], x[8 * s + 3]), phi2(x[8 * s + 4], x[8 * s + 5]), phi2(x[8 * s + 6], x[8 * s + 7])};
  return __builtin_bit_cast(bf16x8, v);
}
DI float bsel(unsigned m, float a, float b) {
  const unsigned ua = __builtin_bit_cast(unsigned, a), ub = __builtin_bit_cast(unsigned, b);
  return __builtin_bit_cast(float, (unsigned)__builtin_amdgcn_bitop3_b32(m, ub, ua, 0xCA));
}
DI float fadd(float a, float b) { float r; asm("v_add_f32 %0, %1, %2" : "=v"(r) : "v"(a), "v"(b)); return r; }
DI f32x4 fadd4(f32x4 a, f32x4 b) { return f32x4{fadd(a[0], b[0]), fadd(a[1], b[1]), fadd(a[2], b[2]), fadd(a[3], b[3])}; }
DI float ffma(float a, float b, float c) { float r; asm("v_fma_f32 %0, %1, %2, %3" : "=v"(r) : "v"(a), "v"(b), "v"(c)); return r; }
template <int CTRL, int RM> DI float dpp_add(float v) {
  return fadd(v, __builtin_bit_cast(float, __builtin_amdgcn_update_dpp(0, __builtin_bit_cast(int, v), CTRL, RM, 0xf, true)));
}
DI float wave_sum63(float v) {
  v = dpp_add<0x111, 0xf>(v); v = dpp_add<0x112, 0xf>(v); v = dpp_add<0x114, 0xf>(v); v = dpp_add<0x118, 0xf>(v);
  v = dpp_add<0x142, 0xa>(v); v = dpp_add<0x143, 0xc>(v);
  return v;
}
DI float sum_stride4(float v) {
  v += __builtin_bit_cast(float, __builtin_amdgcn_update_dpp(0, __builtin_bit_cast(int, v), 0x124, 0xf, 0xf, true));
  v += __builtin_bit_cast(float, __builtin_amdgcn_update_dpp(0, __builtin_bit_cast(int, v), 0x128, 0xf, 0xf, true));
  { const unsigned u = __builtin_bit_cast(unsigned, v); const auto t = __builtin_amdgcn_permlane16_swap(u, u, false, false);
    v = __builtin_bit_cast(float, (unsigned)t[0]) + __builtin_bit_cast(float, (unsigned)t[1]); }
  { const unsigned u = __builtin_bit_cast(unsigned, v); const auto t = __builtin_amdgcn_permlane32_swap(u, u, false, false);
    v = __builtin_bit_cast(float, (unsigned)t[0]) + __builtin_bit_cast(float, (unsigned)t[1]); }
  return v;
}
constexpr unsigned SENT = 0xFFFFFFFFu;
DI unsigned nosent(unsigned v) { return v < SENT ? v : SENT - 1u; }
DI u32x4 nosent4(u32x4 v) { return u32x4{nosent(v[0]), nosent(v[1]), nosent(v[2]), nosent(v[3])}; }
DI unsigned umax3(unsigned a, unsigned b, unsigned c) { unsigned m = a > b ? a : b; return m > c ? m : c; }
#define SB() __builtin_amdgcn_sched_barrier(0)
DI bf16x8 cvt8(const f32x16& x, int s) {
  return pack8(x[8 * s], x[8 * s + 1], x[8 * s + 2], x[8 * s + 3], x[8 * s + 4], x[8 * s + 5], x[8 * s + 6], x[8 * s + 7]);
}
DI float sum8(bf16x8 v, float acc) {
  const bf16x2 ones = {(__bf16)1.0f, (__bf16)1.0f};
  acc = __builtin_amdgcn_fdot2_f32_bf16(__builtin_shufflevector(v, v, 0, 1), ones, acc, false);
  acc = __builtin_amdgcn_fdot2_f32_bf16(__builtin_shufflevector(v, v, 2, 3), ones, acc, false);
  acc = __builtin_amdgcn_fdot2_f32_bf16(__builtin_shufflevector(v, v, 4, 5), ones, acc, false);
  acc = __builtin_amdgcn_fdot2_f32_bf16(__builtin_shufflevector(v, v, 6, 7), ones, acc, false);
  return acc;
}
DI unsigned long long ld_flag(GAS char* ws, unsigned off) {
  return __hip_atomic_load((GAS unsigned long long*)(ws + off), __ATOMIC_RELAXED, __HIP_MEMORY_SCOPE_AGENT);
}
DI void st_flag(GAS char* ws, unsigned off, unsigned long long v) {
  __hip_atomic_store((GAS unsigned long long*)(ws + off), v, __ATOMIC_RELAXED, __HIP_MEMORY_SCOPE_AGENT);
}
DI unsigned long long epoch_issue(const char* wsp) {
  unsigned long long e;
  asm volatile("s_load_dwordx2 %0, %1, 0x0" : "=s"(e) : "s"(wsp));
  return e;
}
DI unsigned long long epoch_wait(unsigned long long e) {
  asm volatile("s_waitcnt lgkmcnt(0)" : "+s"(e) :: "memory");
  return e + 1ull;
}
DI void wait_flags(GAS char* ws, unsigned off, unsigned long long tag) {
  for (unsigned spins = 0; spins < SPIN_CAP; ++spins) {
    const unsigned long long v = ld_flag(ws, off);
    if (__all(v == tag)) break;
    __builtin_amdgcn_s_sleep(PSLEEP);
  }
  asm volatile("" ::: "memory");
}
DI unsigned ld_u32(GAS char* ws, unsigned off) {
  return __hip_atomic_load((GAS unsigned*)(ws + off), __ATOMIC_RELAXED, __HIP_MEMORY_SCOPE_AGENT);
}

__global__ __launch_bounds__(512, 2) void gated_mix_fused(const u16* __restrict__ x, const u16* __restrict__ wA, const u16* __restrict__ wB,
                                                          const u16* __restrict__ woA, const u16* __restrict__ woB, const u16* __restrict__ gw,
                                                          u16* __restrict__ out, char* __restrict__ wsp) {
  extern __shared__ __attribute__((aligned(16))) char lds[];
  LAS char* L = (LAS char*)lds;
  const int tid = threadIdx.x;
  const int wave = __builtin_amdgcn_readfirstlane(tid >> 6), lane = tid & 63;
  const int b = blockIdx.x, xcd = b & 7, r = b >> 3;
  const int ti = (xcd & 1) * 8 + (r & 7), g = (xcd >> 1) * 4 + (r >> 3);
  const int gg = g & 7;
  const int rt = P2MAP ? 8 * (xcd >> 1) + (r & 7) : 4 * xcd + (r & 3), ct = P2MAP ? 4 * (xcd & 1) + (r >> 3) : r >> 2;
  GAS char* ws = (GAS char*)wsp;
  asm volatile("" :: "s"(x), "s"(wA), "s"(wB), "s"(woA), "s"(woB), "s"(gw), "s"(out), "s"(wsp));
  unsigned long long pc;
  asm volatile("s_getpc_b64 %0" : "=s"(pc));

  if (wave < 4) {
    const unsigned long long epoch_raw = epoch_issue(wsp);
    const int wr = wave >> 1, wc = wave & 1;
    const int r32 = lane & 31, h = lane >> 5;
    const int g1 = (lane >> 4) & 1, q = (lane & 15) >> 2, p = lane & 3;
    const int own = wave, oth = wave ^ 1;
    const rsrc_t rsWS = __builtin_amdgcn_make_buffer_rsrc((void*)wsp, 0, WS_END, 0x00020000);
    unsigned ctouch = 0;
    {
      const unsigned off = (wave * 64 + lane) * 128;
      if (off < CODE_PREFETCH) { const char* a = (const char*)pc + off; asm volatile("global_load_dword %0, %1, off" : "=v"(ctouch) : "v"(a) : "memory"); }
    }
    {
      const u32x4 sent = {SENT, SENT, SENT, SENT};
      const unsigned po = WS_PART + ((g * 4 + wave) * 16 + ti) * PART_STRIDE;
      if (lane < 36) st128<16>(sent, rsWS, lane * 16 + po);
    }
    const unsigned fx = (h ^ ((r32 >> 1) & 7)) << 4;
    unsigned xo0 = XR + (own * 32 + r32) * 128 + fx, xt0 = XR + (oth * 32 + r32) * 128 + fx;
    const unsigned sw = ((2 * g1 + (p >> 1)) ^ (((q >> 1) & 1) << 2)) << 4;
    const unsigned wn0 = (8 * h + q) * 128 + sw + (p & 1) * 8;
    unsigned wkv0 = WR + (wn0 ^ (wc << 6));
    asm volatile("" : "+v"(wkv0));
    unsigned xoa[4], xta[4];
#pragma unroll
    for (int k = 0; k < 4; ++k) { xoa[k] = xo0 ^ (k << 5); xta[k] = xt0 ^ (k << 5); asm volatile("" : "+v"(xoa[k]), "+v"(xta[k])); }
    const unsigned long long tag = epoch_wait(epoch_raw);

    bf16x8 xr[32];
    f32x16 kO = {}, kT = {}, vO = {}, vT = {};
    {
      BAR();
      bf16x8 xt = ldsr128(L, xta[0]), wk = ldstr(L, wkv0, wkv0 + 512), wv = ldstr(L, wkv0 + 8192, wkv0 + 8192 + 512);
      xr[0] = ldsr128(L, xoa[0]);
#pragma unroll
      for (int t = 0; t < 32; ++t) {
        bf16x8 nxt, nwk, nwv;
        const int s1 = (t + 1) >> 2, k1 = (t + 1) & 3;
        const unsigned slot = (s1 % 3) * 16384;
        if (t + 1 < 32 && k1 == 0) {
          WAITL0();
          if (s1 == CLR_STAGE) {
            WAITV(0);
            asm volatile("" :: "v"(ctouch));
            if (lane == 0) st_flag(ws, WS_FLAGC + (g * 4 + wave) * 128 + ti * 8, tag);
          }
          BAR();
        }
        SB();
        kO = mfma(xr[t], wk, kO);
        SB();
        if (t + 1 < 32) {
          xr[t + 1] = ldsr128(L, xoa[k1] + slot);
          nwk = ldstr(L, wkv0 + slot + k1 * 2048, wkv0 + slot + k1 * 2048 + 512);
        }
        SB();
        kT = mfma(xt, wk, kT);
        SB();
        if (t + 1 < 32) {
          nxt = ldsr128(L, xta[k1] + slot);
          nwv = ldstr(L, wkv0 + slot + 8192 + k1 * 2048, wkv0 + slot + 8192 + k1 * 2048 + 512);
        }
        SB();
        vO = mfma(xr[t], wv, vO);
        vT = mfma(xt, wv, vT);
        SB();
        if (t + 1 < 32) { xt = nxt; wk = nwk; wv = nwv; }
      }
    }
    {
      const bf16x8 pa0 = phi8(kO, 0), pa1 = phi8(kO, 1), pb0 = phi8(kT, 0), pb1 = phi8(kT, 1);
      const bf16x8 va0 = cvt8(vO, 0), va1 = cvt8(vO, 1), vb0 = cvt8(vT, 0), vb1 = cvt8(vT, 1);
      float zs = sum8(pb1, sum8(pb0, sum8(pa1, sum8(pa0, 0.f))));
      SB();
      f32x16 kv = {};
      kv = mfma(pa0, va0, kv); kv = mfma(pb0, vb0, kv); kv = mfma(pa1, va1, kv); kv = mfma(pb1, vb1, kv);
      SB();
      {
        const unsigned zb = __builtin_bit_cast(unsigned, zs);
        const auto sw2 = __builtin_amdgcn_permlane32_swap(zb, zb, false, false);
        zs = __builtin_bit_cast(float, (unsigned)sw2[0]) + __builtin_bit_cast(float, (unsigned)sw2[1]);
      }
      const unsigned gm = g1 ? 0xFFFFFFFFu : 0u;
      f32x4 lo, hi;
#pragma unroll
      for (int j = 0; j < 4; ++j) { lo[j] = bsel(gm, kv[j], kv[8 + j]); hi[j] = bsel(gm, kv[4 + j], kv[12 + j]); }
      const int hh = 2 * wc + g1;
      const unsigned a = SCR + (4 * wr + hh) * 1024 + (h * 16 + (lane & 15)) * 16;
      *(LAS f32x4*)(L + a) = lo;
      *(LAS f32x4*)(L + a + 512) = hi;
      if (h == 0) *(LAS float*)(L + SCR + (8 + hh) * 1024 + wr * 64 + (lane & 15) * 4) = zs;
    }
    WAITL0();
    BAR();
    f32x16 qa = {}, qb = {};
    {
      unsigned wq0 = WQ + wn0, wq1 = WQ + (wn0 ^ 64);
      asm volatile("" : "+v"(wq0), "+v"(wq1));
      bf16x8 a0[3], a1[3];
      a0[0] = ldstr(L, wq0, wq0 + 512); a1[0] = ldstr(L, wq1, wq1 + 512);
      a0[1] = ldstr(L, wq0 + 2048, wq0 + 2048 + 512); a1[1] = ldstr(L, wq1 + 2048, wq1 + 2048 + 512);
#pragma unroll
      for (int t = 0; t < 32; ++t) {
        const unsigned o = (t + 2) * 2048;
        SB();
        qa = mfma(a0[t % 3], xr[t], qa);
        SB();
        if (t + 2 < 32) a0[(t + 2) % 3] = ldstr(L, wq0 + o, wq0 + o + 512);
        SB();
        qb = mfma(a1[t % 3], xr[t], qb);
        SB();
        if (t + 2 < 32) a1[(t + 2) % 3] = ldstr(L, wq1 + o, wq1 + o + 512);
      }
      SB();
    }
    bf16x8 pq[4];
    pq[0] = phi8(qa, 0); pq[1] = phi8(qa, 1); pq[2] = phi8(qb, 0); pq[3] = phi8(qb, 1);
    asm volatile("" : "+v"(pq[0]), "+v"(pq[1]), "+v"(pq[2]), "+v"(pq[3]));
    WAITL0();
    BAR();
    {
      const unsigned st = 4 * ti + own;
      bf16x8 af[4];
#pragma unroll
      for (int hh = 0; hh < 4; ++hh) af[hh] = ldsr128(L, AFR + hh * 1024 + lane * 16);
      SB();
      f32x16 d[4];
#pragma unroll
      for (int hh = 0; hh < 4; ++hh) { d[hh] = f32x16{}; d[hh] = mfma(af[hh], pq[hh], d[hh]); }
      SB();
#pragma unroll
      for (int hh = 0; hh < 4; ++hh) {
        const float inv = __builtin_amdgcn_rcpf(d[hh][8] + 1e-6f);
        const u32x4 o = __builtin_bit_cast(u32x4, pack8(d[hh][0] * inv, d[hh][1] * inv, d[hh][2] * inv, d[hh][3] * inv, d[hh][4] * inv, d[hh][5] * inv, d[hh][6] * inv, d[hh][7] * inv));
        st128<16>(o, rsWS, lane * 16 + WS_O + (st * 64 + 4 * g + hh) * 1024);
      }
    }
    if (!BWLATE) BAR();
    WAITV(0);
    if (lane == 0) st_flag(ws, WS_FLAGB + (FPACK ? (4 * ti + own) * 128 + g * 8 : ((4 * ti + own) * 16 + g) * 128), tag);
    if (BWLATE) BAR();
    return;
  }

  const int hw = wave - 4;
  const unsigned long long epoch_raw = epoch_issue(wsp);
  __builtin_amdgcn_s_setprio(HPRIO);
  int ro = 0;
  if (ROT == 1) ro = (ti + 2 * g) & 7;
  if (ROT == 2) ro = 2 * ((ti + g) & 1);
  if (ROT == 3) ro = 4 * ((ti + g) & 1);
  if (ROT == 5) ro = ((ti + g) % 3) * 2;
  if (ROT == 6) ro = 2 * ((ti + g) & 3);
  const u16* w = (g >= 8) ? wB : wA;
  const rsrc_t rsX = __builtin_amdgcn_make_buffer_rsrc((void*)x, 0, S_ * D_ * 2, 0x00020000);
  const rsrc_t rsW = __builtin_amdgcn_make_buffer_rsrc((void*)w, 0, D_ * 3 * D_ * 2, 0x00020000);
  const int rin = lane >> 3, ch = lane & 7;
  const unsigned vx = rin * 1024 + ((ch ^ (4 * (hw & 1) + (rin >> 1))) << 4);
  const unsigned csw = (ch ^ (((rin >> 1) & 1) << 2)) << 4;
  const unsigned vw = rin * 3072 + csw;
  const unsigned sx0 = (ti * 128 + 8 * hw) * 1024;
  const unsigned sw0 = 8 * hw * 3072 + gg * 128;
  auto stage = [&](int s) {
    const unsigned ks = (s + ro) & 7, slot = (s % 3) * 16384;
#pragma unroll
    for (int i = 0; i < 4; ++i) glds(vx, rsX, sx0 + ks * 128 + i * 32768, XR + slot + (hw + 4 * i) * 1024);
#pragma unroll
    for (int i = 0; i < 2; ++i) glds(vw, rsW, sw0 + ks * 196608 + i * 98304 + 1024, WR + slot + (hw + 4 * i) * 1024);
#pragma unroll
    for (int i = 0; i < 2; ++i) glds(vw, rsW, sw0 + ks * 196608 + i * 98304 + 2048, WR + slot + 8192 + (hw + 4 * i) * 1024);
  };
  auto stageq = [&](int s) {
    const unsigned ks = (s + ro) & 7;
#pragma unroll
    for (int i = 0; i < 2; ++i) glds(vw, rsW, sw0 + ks * 196608 + i * 98304, WQ + s * 8192 + (hw + 4 * i) * 1024);
  };
  unsigned wtouch = 0;
  auto wo_touch = [&]() {
    if (P2MAP) {
      const unsigned row = r * 32 + hw * 8 + ((lane & 31) >> 2);
      const char* a = (const char*)((row & 512) ? woB : woA) + (row & 511) * 1024 + (xcd & 1) * 512 + (lane & 3) * 128;
      if (lane < 32) asm volatile("global_load_dword %0, %1, off" : "=v"(wtouch) : "v"(a) : "memory");
    } else {
      const unsigned row = r * 32 + hw * 8 + (lane >> 3);
      const char* a = (const char*)((row & 512) ? woB : woA) + (row & 511) * 1024 + (lane & 7) * 128;
      asm volatile("global_load_dword %0, %1, off" : "=v"(wtouch) : "v"(a) : "memory");
    }
  };
  stage(0);
  stage(1);
  WAITV(8);
  const unsigned long long tag = epoch_wait(epoch_raw);
  BAR();
  const rsrc_t rsG = __builtin_amdgcn_make_buffer_rsrc((void*)gw, 0, D_ * 2, 0x00020000);
  const unsigned grow = ti * 128 + 8 * g + 2 * hw;
  u32x4 x0, x1, gv;
  const unsigned foff = WS_FLAGC + (g * 4 + hw) * 128 + (lane & 15) * 8;
  u32x2 fcv;
#pragma unroll
  for (int s = 1; s < 8; ++s) {
    if (s < 6) { stage(s + 1); WAITV(8); }
    else if (s == 6) {
      stage(7);
      asm volatile("buffer_load_dwordx4 %0, %4, %5, %7 offen\n\tbuffer_load_dwordx4 %1, %4, %5, %7 offen offset:1024\n\tbuffer_load_dwordx4 %2, %4, %6, 0 offen\n\t"
                   "global_load_dwordx2 %3, %8, %9 sc1"
                   : "=&v"(x0), "=&v"(x1), "=&v"(gv), "=&v"(fcv) : "v"(lane * 16), "s"(rsX), "s"(rsG), "s"(grow * 1024), "v"(foff), "s"(wsp) : "memory");
      WAITV(12);
    } else {
      stageq(0); stageq(1); stageq(2); stageq(3);
      WAITV(12);
    }
    BAR();
  }
  stageq(4); stageq(5); stageq(6); stageq(7);
  const rsrc_t rsWS = __builtin_amdgcn_make_buffer_rsrc((void*)wsp, 0, WS_END, 0x00020000);
  const rsrc_t rsOA = __builtin_amdgcn_make_buffer_rsrc((void*)woA, 0, D_ * D_ * 2, 0x00020000);
  const rsrc_t rsOB = __builtin_amdgcn_make_buffer_rsrc((void*)woB, 0, D_ * D_ * 2, 0x00020000);
  const unsigned vo = rin * 1024 + csw;
  const unsigned hid = g * 4 + hw;
  wo_touch();
  asm volatile("s_waitcnt vmcnt(17)" : "+v"(x0), "+v"(x1), "+v"(gv), "+v"(fcv) :: "memory");
  if (!__all((((unsigned long long)fcv[1] << 32) | fcv[0]) == tag)) wait_flags(ws, foff, tag);
  float gval;
  {
    float a0 = 0.f, a1 = 0.f;
#pragma unroll
    for (int i = 0; i < 4; ++i) {
      const unsigned ug = gv[i], u0 = x0[i], u1 = x1[i];
      const float gl = __builtin_bit_cast(float, ug << 16), gh = __builtin_bit_cast(float, ug & 0xffff0000u);
      a0 = ffma(__builtin_bit_cast(float, u0 << 16), gl, a0); a0 = ffma(__builtin_bit_cast(float, u0 & 0xffff0000u), gh, a0);
      a1 = ffma(__builtin_bit_cast(float, u1 << 16), gl, a1); a1 = ffma(__builtin_bit_cast(float, u1 & 0xffff0000u), gh, a1);
    }
    a0 = wave_sum63(a0); a1 = wave_sum63(a1);
    const float s0 = __builtin_bit_cast(float, __builtin_amdgcn_readlane(__builtin_bit_cast(int, a0), 63));
    const float s1 = __builtin_bit_cast(float, __builtin_amdgcn_readlane(__builtin_bit_cast(int, a1), 63));
    const float lg = (lane & 1) ? s1 : s0;
    gval = __builtin_amdgcn_rcpf(1.0f + __builtin_amdgcn_exp2f(-1.44269504088896340736f * lg));
  }
  asm volatile("s_waitcnt vmcnt(1)" : "+v"(gval) :: "memory");
  BAR();
  {
    const f32x4 p0 = *(LAS f32x4*)(L + SCR + hw * 1024 + lane * 16), p1 = *(LAS f32x4*)(L + SCR + (4 + hw) * 1024 + lane * 16);
    const f32x4 z0 = *(LAS f32x4*)(L + SCR + (8 + hw) * 1024 + (lane & 3) * 16), z1 = *(LAS f32x4*)(L + SCR + (8 + hw) * 1024 + 64 + (lane & 3) * 16);
    const unsigned po = WS_PART + (hid * 16 + ti) * PART_STRIDE;
    const f32x4 ps = fadd4(p0, p1);
    const unsigned d0 = __builtin_bit_cast(unsigned, cvt2(ps[0], ps[1])), d1 = __builtin_bit_cast(unsigned, cvt2(ps[2], ps[3]));
    const auto e0 = __builtin_amdgcn_permlane32_swap(d0, d0, false, false), e1 = __builtin_amdgcn_permlane32_swap(d1, d1, false, false);
    const u32x4 kvp = {e0[0], e1[0], e0[1], e1[1]};
    const u32x4 zp = __builtin_bit_cast(u32x4, fadd4(z0, z1));
    u32x4 pay;
#pragma unroll
    for (int i = 0; i < 4; ++i) pay[i] = nosent(lane < 32 ? kvp[i] : zp[i]);
    if (lane < 36) st128<16>(pay, rsWS, lane * 16 + po);
    if (lane < 2) __hip_atomic_store((GAS unsigned*)(ws + WS_GATE) + grow + lane, __builtin_bit_cast(unsigned, gval), __ATOMIC_RELAXED, __HIP_MEMORY_SCOPE_AGENT);
  }
#pragma unroll
  for (int i = 0; i < 16; ++i) glds(vo, rsOA, (hw + 4 * i) * 8192 + ct * 128, WOA + (hw + 4 * i) * 1024);
  asm volatile("" :: "v"(wtouch));
  const unsigned pb = WS_PART + hid * 16 * PART_STRIDE;
  for (int i = 0; i < PDELAY1; ++i) __builtin_amdgcn_s_sleep(4);
  {
    float a[8];
    f32x4 z;
    u32x4 pv[8], zv;
    auto gather = [&]() {
#pragma unroll
      for (int i = 0; i < 8; ++i) pv[i] = __builtin_amdgcn_raw_buffer_load_b128(rsWS, (lane >> 5) * PART_STRIDE + (lane & 31) * 16, pb + 2 * i * PART_STRIDE, 16);
      zv = __builtin_amdgcn_raw_buffer_load_b128(rsWS, (lane >> 2) * PART_STRIDE + 512 + (lane & 3) * 16, pb, 16);
    };
    auto reduce = [&]() {
      unsigned clo = 0x00003F80u, chi = 0x3F800000u;
      asm volatile("" : "+s"(clo), "+s"(chi));
      const bf16x2 lo1 = __builtin_bit_cast(bf16x2, clo), hi1 = __builtin_bit_cast(bf16x2, chi);
      unsigned mx = 0;
#pragma unroll
      for (int k = 0; k < 8; ++k) a[k] = 0.f;
#pragma unroll
      for (int i = 0; i < 8; ++i) {
#pragma unroll
        for (int k = 0; k < 4; ++k) {
          const unsigned u = pv[i][k];
          a[2 * k] = __builtin_amdgcn_fdot2_f32_bf16(__builtin_bit_cast(bf16x2, u), lo1, a[2 * k], false);
          a[2 * k + 1] = __builtin_amdgcn_fdot2_f32_bf16(__builtin_bit_cast(bf16x2, u), hi1, a[2 * k + 1], false);
        }
        mx = umax3(pv[i][0], pv[i][1], umax3(pv[i][2], pv[i][3], mx));
      }
      mx = umax3(zv[0], zv[1], umax3(zv[2], zv[3], mx));
      z = __builtin_bit_cast(f32x4, zv);
      return __all(mx != SENT);
    };
    gather();
    asm volatile("" ::: "memory");
    if (!reduce()) {
      for (unsigned spins = 0; spins < SPIN_CAP; ++spins) { gather(); if (reduce()) break; }
    }
#pragma unroll
    for (int k = 0; k < 8; ++k) {
      const unsigned u = __builtin_bit_cast(unsigned, a[k]);
      const auto t = __builtin_amdgcn_permlane32_swap(u, u, false, false);
      a[k] = __builtin_bit_cast(float, (unsigned)t[0]) + __builtin_bit_cast(float, (unsigned)t[1]);
    }
    z = f32x4{sum_stride4(z[0]), sum_stride4(z[1]), sum_stride4(z[2]), sum_stride4(z[3])};
    const int slot = lane & 31;
    if (lane < 32) *(LAS bf16x8*)(L + AFR + hw * 1024 + ((slot >> 4) * 32 + (slot & 15)) * 16) = pack8(a[0], a[1], a[2], a[3], a[4], a[5], a[6], a[7]);
    const int q4 = lane & 3;
    u32x2 kz = {__builtin_bit_cast(unsigned, cvt2(z[0], z[1])), __builtin_bit_cast(unsigned, cvt2(z[2], z[3]))};
    *(LAS u32x2*)(L + AFR + hw * 1024 + ((q4 & 1) * 32 + 16 + (lane >> 2)) * 16 + (q4 >> 1) * 8) = kz;
  }
  WAITV(0);
  WAITL0();
  BAR();
#pragma unroll
  for (int i = 0; i < 16; ++i) glds(vo, rsOB, (hw + 4 * i) * 8192 + ct * 128, WOB + (hw + 4 * i) * 1024);
  WAITV(0);
  BAR();
  {
    const int r32 = lane & 31, h = lane >> 5, g1 = (lane >> 4) & 1, q = (lane & 15) >> 2, p = lane & 3;
    const unsigned sw = ((2 * g1 + (p >> 1)) ^ (((q >> 1) & 1) << 2)) << 4;
    const int br2 = hw >> 1, sh = hw & 1;
    const unsigned st2 = 2 * rt + sh;
    const unsigned ob = WS_O + (st2 * 64 + 32 * br2) * 1024;
    for (int i = 0; i < PDELAY; ++i) __builtin_amdgcn_s_sleep(8);
    {
      const unsigned foffb = WS_FLAGB + (FPACK ? st2 * 128 + (lane & 15) * 8 : (st2 * 16 + (lane & 15)) * 128);
      for (unsigned spins = 0; spins < SPIN_CAP; ++spins) {
        if (__all(ld_flag(ws, foffb) == tag)) break;
        __builtin_amdgcn_s_sleep(PBACK);
      }
      asm volatile("" ::: "memory");
    }
    unsigned wp0 = ((br2 ? WOB : WOA) + (4 * h + q) * 128 + sw + (p & 1) * 8) ^ (br2 << 6), wp1 = wp0 ^ 64;
    asm volatile("" : "+v"(wp0), "+v"(wp1));
    f32x16 c0 = {}, c1 = {};
    const unsigned gvb = ld_u32(ws, WS_GATE + (st2 * 32 + r32) * 4);
    {
      u32x4 of[32];
#pragma unroll
      for (int j = 0; j < OD; ++j) {
        of[j] = __builtin_amdgcn_raw_buffer_load_b128(rsWS, lane * 16, ob + j * 1024, 16);
        SB();
      }
      bf16x8 a0[3], a1[3];
      a0[0] = ldstr(L, wp0, wp0 + 1024); a1[0] = ldstr(L, wp1, wp1 + 1024);
      a0[1] = ldstr(L, wp0 + 2048, wp0 + 2048 + 1024); a1[1] = ldstr(L, wp1 + 2048, wp1 + 2048 + 1024);
#pragma unroll
      for (int j = 0; j < 32; ++j) {
        const unsigned o = (j + 2) * 2048;
        const bf16x8 bf = __builtin_bit_cast(bf16x8, of[j]);
        SB();
        c0 = mfma(a0[j % 3], bf, c0);
        SB();
        if (j + OD < 32) of[j + OD] = __builtin_amdgcn_raw_buffer_load_b128(rsWS, lane * 16, ob + (j + OD) * 1024, 16);
        if (j + 2 < 32) a0[(j + 2) % 3] = ldstr(L, wp0 + o, wp0 + o + 1024);
        SB();
        c1 = mfma(a1[j % 3], bf, c1);
        SB();
        if (j + 2 < 32) a1[(j + 2) % 3] = ldstr(L, wp1 + o, wp1 + o + 1024);
      }
      SB();
    }
    __builtin_amdgcn_s_setprio(0);
    const float gv = __builtin_bit_cast(float, gvb);
    const float gs = br2 ? 1.0f - gv : gv;
#pragma unroll
    for (int qq = 0; qq < 4; ++qq) {
      const f32x4 v = {c1[4 * qq] * gs, c1[4 * qq + 1] * gs, c1[4 * qq + 2] * gs, c1[4 * qq + 3] * gs};
      *(LAS f32x4*)(L + EXC + hw * 4096 + qq * 1024 + lane * 16) = v;
    }
    WAITL0();
    BAR();
    if (b == 0 && hw == 0 && lane == 0) st_flag(ws, WS_EPOCH, tag);
    unsigned pk[8];
#pragma unroll
    for (int qq = 0; qq < 4; ++qq) {
      const f32x4 v = *(LAS f32x4*)(L + EXC + (hw ^ 2) * 4096 + qq * 1024 + lane * 16);
      pk[2 * qq] = __builtin_bit_cast(unsigned, cvt2(c0[4 * qq] * gs + v[0], c0[4 * qq + 1] * gs + v[1]));
      pk[2 * qq + 1] = __builtin_bit_cast(unsigned, cvt2(c0[4 * qq + 2] * gs + v[2], c0[4 * qq + 3] * gs + v[3]));
    }
    const rsrc_t rsO = __builtin_amdgcn_make_buffer_rsrc((void*)out, 0, S_ * D_ * 2, 0x00020000);
    const unsigned orow = (st2 * 32 + r32) * 1024 + (64 * ct + 32 * br2) * 2 + h * 16;
#pragma unroll
    for (int qq = 0; qq < 4; qq += 2) {
      const auto s0 = __builtin_amdgcn_permlane32_swap(pk[2 * qq], pk[2 * qq + 2], false, false);
      const auto s1 = __builtin_amdgcn_permlane32_swap(pk[2 * qq + 1], pk[2 * qq + 3], false, false);
      const u32x4 v = {s0[0], s1[0], s0[1], s1[1]};
      st128<0>(v, rsO, orow + qq * 16);
    }
  }
}

extern "C" void kernel_launch(void* const* d_in, const int* in_sizes, int n_in, void* d_out, int out_size, void* d_ws, size_t ws_size,
                              hipStream_t stream) {
  (void)in_sizes; (void)n_in; (void)out_size;
  const u16* x = (const u16*)d_in[0];
  const u16* wA = (const u16*)d_in[1];
  const u16* woA = (const u16*)d_in[2];
  const u16* wB = (const u16*)d_in[4];
  const u16* woB = (const u16*)d_in[5];
  const u16* gw = (const u16*)d_in[6];
  (void)hipFuncSetAttribute((const void*)gated_mix_fused, hipFuncAttributeMaxDynamicSharedMemorySize, LDS_BYTES);
  gated_mix_fused<<<256, 512, LDS_BYTES, stream>>>(x, wA, wB, woA, woB, gw, (u16*)d_out, (char*)d_ws + ((ws_size / 2) & ~(size_t)0x1FFFFF));
}
```

```cpp
#include <hip/hip_runtime.h>
#include <hip/hip_bf16.h>
#include <stdint.h>

#define LAS __attribute__((address_space(3)))
#define GAS __attribute__((address_space(1)))
#define DI __device__ __forceinline__

typedef unsigned short u16;
typedef __bf16 bf16x8 __attribute__((ext_vector_type(8)));
typedef __bf16 bf16x4 __attribute__((ext_vector_type(4)));
typedef __bf16 bf16x2 __attribute__((ext_vector_type(2)));
typedef float f32x16 __attribute__((ext_vector_type(16)));
typedef float f32x4 __attribute__((ext_vector_type(4)));
typedef float f32x2 __attribute__((ext_vector_type(2)));
typedef unsigned u32x4 __attribute__((ext_vector_type(4)));
typedef unsigned u32x2 __attribute__((ext_vector_type(2)));
typedef __amdgpu_buffer_rsrc_t rsrc_t;

constexpr int CODE_PREFETCH = 20480;
constexpr int CLR_STAGE = 3;
constexpr int HPRIO = 3;
constexpr int OD = 10;
constexpr int PSLEEP = 4;
constexpr int PDELAY1 = 1;
constexpr int PDELAY = 3;
constexpr int PBACK = 8;

constexpr int S_ = 2048, D_ = 512;
constexpr unsigned XR = 0;
constexpr unsigned WR = 49152;
constexpr unsigned WQ = 98304;
constexpr unsigned WOA = 0;
constexpr unsigned WOB = 98304;
constexpr unsigned SCR = 81920;
constexpr unsigned AFR = SCR + 12288;
constexpr unsigned EXC = SCR;
constexpr unsigned LDS_BYTES = 163840;
constexpr unsigned WS_EPOCH = 0;
constexpr unsigned WS_FLAGC = 4096;
constexpr unsigned WS_FLAGB = WS_FLAGC + 64 * 128;
constexpr unsigned WS_GATE = WS_FLAGB + 64 * 128;
constexpr unsigned WS_PART = WS_GATE + 8192;
constexpr unsigned PART_STRIDE = 640;
constexpr unsigned WS_O = WS_PART + 1024 * PART_STRIDE;
constexpr unsigned WS_END = WS_O + 64 * 64 * 1024;
constexpr unsigned SPIN_CAP = 1u << 18;

DI void glds(unsigned voff, rsrc_t rs, unsigned soff, unsigned ldsaddr) {
  asm volatile("s_mov_b32 m0, %0\n\ts_nop 0\n\tbuffer_load_dwordx4 %1, %2, %3 offen lds"
               :: "s"(ldsaddr), "v"(voff), "s"(rs), "s"(soff) : "memory");
}
template <int AUX> DI void st128(u32x4 v, rsrc_t rs, unsigned off) { __builtin_amdgcn_raw_buffer_store_b128(v, rs, off, 0, AUX); }
#define WAITV(n) asm volatile("s_waitcnt vmcnt(" #n ")" ::: "memory")
#define WAITL0() asm volatile("s_waitcnt lgkmcnt(0)" ::: "memory")
#define BAR() asm volatile("s_barrier" ::: "memory")

DI f32x16 mfma(bf16x8 a, bf16x8 b, f32x16 c) { return __builtin_amdgcn_mfma_f32_32x32x16_bf16(a, b, c, 0, 0, 0); }
DI bf16x8 ldsr128(LAS char* L, unsigned a) { return *(LAS bf16x8*)(L + a); }
DI bf16x8 ldstr(LAS char* L, unsigned a0, unsigned a1) {
  bf16x4 lo = __builtin_amdgcn_ds_read_tr16_b64_v4bf16((LAS bf16x4*)(L + a0));
  bf16x4 hi = __builtin_amdgcn_ds_read_tr16_b64_v4bf16((LAS bf16x4*)(L + a1));
  return __builtin_shufflevector(lo, hi, 0, 1, 2, 3, 4, 5, 6, 7);
}
DI bf16x2 cvt2(float a, float b) { f32x2 p = {a, b}; return __builtin_convertvector(p, bf16x2); }
DI bf16x8 pack8(float a0, float a1, float a2, float a3, float a4, float a5, float a6, float a7) {
  bf16x2 p0 = cvt2(a0, a1), p1 = cvt2(a2, a3), p2 = cvt2(a4, a5), p3 = cvt2(a6, a7);
  bf16x4 lo = __builtin_shufflevector(p0, p1, 0, 1, 2, 3), hi = __builtin_shufflevector(p2, p3, 0, 1, 2, 3);
  return __builtin_shufflevector(lo, hi, 0, 1, 2, 3, 4, 5, 6, 7);
}
DI unsigned phi2(float a, float b) {
  const f32x2 t = {a, b};
  const f32x2 u = t * 1.44269504088896340736f;
  const f32x2 e = {__builtin_amdgcn_fmed3f(__builtin_amdgcn_exp2f(u[0]), 0.0f, 1.0f), __builtin_amdgcn_fmed3f(__builtin_amdgcn_exp2f(u[1]), 0.0f, 1.0f)};
  const int ia = __builtin_bit_cast(int, a), ib = __builtin_bit_cast(int, b);
  const f32x2 m = {__builtin_bit_cast(float, ia > 0 ? ia : 0), __builtin_bit_cast(float, ib > 0 ? ib : 0)};
  const f32x2 r = (m + e) + 1e-6f;
  return __builtin_bit_cast(unsigned, __builtin_convertvector(r, bf16x2));
}
DI bf16x8 phi8(const f32x16& x, int s) {
  const u32x4 v = {phi2(x[8 * s], x[8 * s + 1]), phi2(x[8 * s + 2], x[8 * s + 3]), phi2(x[8 * s + 4], x[8 * s + 5]), phi2(x[8 * s + 6], x[8 * s + 7])};
  return __builtin_bit_cast(bf16x8, v);
}
DI float bsel(unsigned m, float a, float b) {
  const unsigned ua = __builtin_bit_cast(unsigned, a), ub = __builtin_bit_cast(unsigned, b);
  return __builtin_bit_cast(float, (unsigned)__builtin_amdgcn_bitop3_b32(m, ub, ua, 0xCA));
}
DI float fadd(float a, float b) { float r; asm("v_add_f32 %0, %1, %2" : "=v"(r) : "v"(a), "v"(b)); return r; }
DI f32x4 fadd4(f32x4 a, f32x4 b) { return f32x4{fadd(a[0], b[0]), fadd(a[1], b[1]), fadd(a[2], b[2]), fadd(a[3], b[3])}; }
DI float ffma(float a, float b, float c) { float r; asm("v_fma_f32 %0, %1, %2, %3" : "=v"(r) : "v"(a), "v"(b), "v"(c)); return r; }
template <int CTRL, int RM> DI float dpp_add(float v) {
  return fadd(v, __builtin_bit_cast(float, __builtin_amdgcn_update_dpp(0, __builtin_bit_cast(int, v), CTRL, RM, 0xf, true)));
}
DI float wave_sum63(float v) {
  v = dpp_add<0x111, 0xf>(v); v = dpp_add<0x112, 0xf>(v); v = dpp_add<0x114, 0xf>(v); v = dpp_add<0x118, 0xf>(v);
  v = dpp_add<0x142, 0xa>(v); v = dpp_add<0x143, 0xc>(v);
  return v;
}
DI float sum_stride4(float v) {
  v += __builtin_bit_cast(float, __builtin_amdgcn_update_dpp(0, __builtin_bit_cast(int, v), 0x124, 0xf, 0xf, true));
  v += __builtin_bit_cast(float, __builtin_amdgcn_update_dpp(0, __builtin_bit_cast(int, v), 0x128, 0xf, 0xf, true));
  { const unsigned u = __builtin_bit_cast(unsigned, v); const auto t = __builtin_amdgcn_permlane16_swap(u, u, false, false);
    v = __builtin_bit_cast(float, (unsigned)t[0]) + __builtin_bit_cast(float, (unsigned)t[1]); }
  { const unsigned u = __builtin_bit_cast(unsigned, v); const auto t = __builtin_amdgcn_permlane32_swap(u, u, false, false);
    v = __builtin_bit_cast(float, (unsigned)t[0]) + __builtin_bit_cast(float, (unsigned)t[1]); }
  return v;
}
constexpr unsigned SENT = 0xFFFFFFFFu;
DI unsigned nosent(unsigned v) { return v < SENT ? v : SENT - 1u; }
DI u32x4 nosent4(u32x4 v) { return u32x4{nosent(v[0]), nosent(v[1]), nosent(v[2]), nosent(v[3])}; }
DI unsigned umax3(unsigned a, unsigned b, unsigned c) { unsigned m = a > b ? a : b; return m > c ? m : c; }
#define SB() __builtin_amdgcn_sched_barrier(0)
DI bf16x8 cvt8(const f32x16& x, int s) {
  return pack8(x[8 * s], x[8 * s + 1], x[8 * s + 2], x[8 * s + 3], x[8 * s + 4], x[8 * s + 5], x[8 * s + 6], x[8 * s + 7]);
}
DI float sum8(bf16x8 v, float acc) {
  const bf16x2 ones = {(__bf16)1.0f, (__bf16)1.0f};
  acc = __builtin_amdgcn_fdot2_f32_bf16(__builtin_shufflevector(v, v, 0, 1), ones, acc, false);
  acc = __builtin_amdgcn_fdot2_f32_bf16(__builtin_shufflevector(v, v, 2, 3), ones, acc, false);
  acc = __builtin_amdgcn_fdot2_f32_bf16(__builtin_shufflevector(v, v, 4, 5), ones, acc, false);
  acc = __builtin_amdgcn_fdot2_f32_bf16(__builtin_shufflevector(v, v, 6, 7), ones, acc, false);
  return acc;
}
DI unsigned long long ld_flag(GAS char* ws, unsigned off) {
  return __hip_atomic_load((GAS unsigned long long*)(ws + off), __ATOMIC_RELAXED, __HIP_MEMORY_SCOPE_AGENT);
}
DI void st_flag(GAS char* ws, unsigned off, unsigned long long v) {
  __hip_atomic_store((GAS unsigned long long*)(ws + off), v, __ATOMIC_RELAXED, __HIP_MEMORY_SCOPE_AGENT);
}
DI unsigned long long epoch_issue(const char* wsp) {
  unsigned long long e;
  asm volatile("s_load_dwordx2 %0, %1, 0x0" : "=s"(e) : "s"(wsp));
  return e;
}
DI unsigned long long epoch_wait(unsigned long long e) {
  asm volatile("s_waitcnt lgkmcnt(0)" : "+s"(e) :: "memory");
  return e + 1ull;
}
DI void wait_flags(GAS char* ws, unsigned off, unsigned long long tag) {
  for (unsigned spins = 0; spins < SPIN_CAP; ++spins) {
    const unsigned long long v = ld_flag(ws, off);
    if (__all(v == tag)) break;
    __builtin_amdgcn_s_sleep(PSLEEP);
  }
  asm volatile("" ::: "memory");
}
DI unsigned ld_u32(GAS char* ws, unsigned off) {
  return __hip_atomic_load((GAS unsigned*)(ws + off), __ATOMIC_RELAXED, __HIP_MEMORY_SCOPE_AGENT);
}

__global__ __launch_bounds__(512, 2) void gated_mix_fused(const u16* __restrict__ x, const u16* __restrict__ wA, const u16* __restrict__ wB,
                                                          const u16* __restrict__ woA, const u16* __restrict__ woB, const u16* __restrict__ gw,
                                                          u16* __restrict__ out, char* __restrict__ wsp) {
  extern __shared__ __attribute__((aligned(16))) char lds[];
  LAS char* L = (LAS char*)lds;
  const int tid = threadIdx.x;
  const int wave = __builtin_amdgcn_readfirstlane(tid >> 6), lane = tid & 63;
  const int b = blockIdx.x, xcd = b & 7, r = b >> 3;
  const int ti = (xcd & 1) * 8 + (r & 7), g = (xcd >> 1) * 4 + (r >> 3);
  const int gg = g & 7;
  const int rt = 4 * xcd + (r & 3), ct = r >> 2;
  GAS char* ws = (GAS char*)wsp;
  asm volatile("" :: "s"(x), "s"(wA), "s"(wB), "s"(woA), "s"(woB), "s"(gw), "s"(out), "s"(wsp));
  unsigned long long pc;
  asm volatile("s_getpc_b64 %0" : "=s"(pc));

  if (wave < 4) {
    const unsigned long long epoch_raw = epoch_issue(wsp);
    const int wr = wave >> 1, wc = wave & 1;
    const int r32 = lane & 31, h = lane >> 5;
    const int g1 = (lane >> 4) & 1, q = (lane & 15) >> 2, p = lane & 3;
    const int own = wave, oth = wave ^ 1;
    const rsrc_t rsWS = __builtin_amdgcn_make_buffer_rsrc((void*)wsp, 0, WS_END, 0x00020000);
    unsigned ctouch = 0;
    {
      const unsigned off = (wave * 64 + lane) * 128;
      if (off < CODE_PREFETCH) { const char* a = (const char*)pc + off; asm volatile("global_load_dword %0, %1, off" : "=v"(ctouch) : "v"(a) : "memory"); }
    }
    {
      const u32x4 sent = {SENT, SENT, SENT, SENT};
      const unsigned po = WS_PART + ((g * 4 + wave) * 16 + ti) * PART_STRIDE;
      if (lane < 36) st128<16>(sent, rsWS, lane * 16 + po);
    }
    const unsigned fx = (h ^ ((r32 >> 1) & 7)) << 4;
    unsigned xo0 = XR + (own * 32 + r32) * 128 + fx, xt0 = XR + (oth * 32 + r32) * 128 + fx;
    const unsigned sw = ((2 * g1 + (p >> 1)) ^ (((q >> 1) & 1) << 2)) << 4;
    const unsigned wn0 = (8 * h + q) * 128 + sw + (p & 1) * 8;
    unsigned wkv0 = WR + (wn0 ^ (wc << 6));
    asm volatile("" : "+v"(wkv0));
    unsigned xoa[4], xta[4];
#pragma unroll
    for (int k = 0; k < 4; ++k) { xoa[k] = xo0 ^ (k << 5); xta[k] = xt0 ^ (k << 5); asm volatile("" : "+v"(xoa[k]), "+v"(xta[k])); }
    const unsigned long long tag = epoch_wait(epoch_raw);

    bf16x8 xr[32];
    f32x16 kO = {}, kT = {}, vO = {}, vT = {};
    {
      BAR();
      bf16x8 xt = ldsr128(L, xta[0]), wk = ldstr(L, wkv0, wkv0 + 512), wv = ldstr(L, wkv0 + 8192, wkv0 + 8192 + 512);
      xr[0] = ldsr128(L, xoa[0]);
#pragma unroll
      for (int t = 0; t < 32; ++t) {
        bf16x8 nxt, nwk, nwv;
        const int s1 = (t + 1) >> 2, k1 = (t + 1) & 3;
        const unsigned slot = (s1 % 3) * 16384;
        if (t + 1 < 32 && k1 == 0) {
          WAITL0();
          if (s1 == CLR_STAGE) {
            WAITV(0);
            asm volatile("" :: "v"(ctouch));
            if (lane == 0) st_flag(ws, WS_FLAGC + (g * 4 + wave) * 128 + ti * 8, tag);
          }
          BAR();
        }
        SB();
        kO = mfma(xr[t], wk, kO);
        SB();
        if (t + 1 < 32) {
          xr[t + 1] = ldsr128(L, xoa[k1] + slot);
          nwk = ldstr(L, wkv0 + slot + k1 * 2048, wkv0 + slot + k1 * 2048 + 512);
        }
        SB();
        kT = mfma(xt, wk, kT);
        SB();
        if (t + 1 < 32) {
          nxt = ldsr128(L, xta[k1] + slot);
          nwv = ldstr(L, wkv0 + slot + 8192 + k1 * 2048, wkv0 + slot + 8192 + k1 * 2048 + 512);
        }
        SB();
        vO = mfma(xr[t], wv, vO);
        vT = mfma(xt, wv, vT);
        SB();
        if (t + 1 < 32) { xt = nxt; wk = nwk; wv = nwv; }
      }
    }
    {
      const bf16x8 pa0 = phi8(kO, 0), pa1 = phi8(kO, 1), pb0 = phi8(kT, 0), pb1 = phi8(kT, 1);
      const bf16x8 va0 = cvt8(vO, 0), va1 = cvt8(vO, 1), vb0 = cvt8(vT, 0), vb1 = cvt8(vT, 1);
      float zs = sum8(pb1, sum8(pb0, sum8(pa1, sum8(pa0, 0.f))));
      SB();
      f32x16 kv = {};
      kv = mfma(pa0, va0, kv); kv = mfma(pb0, vb0, kv); kv = mfma(pa1, va1, kv); kv = mfma(pb1, vb1, kv);
      SB();
      {
        const unsigned zb = __builtin_bit_cast(unsigned, zs);
        const auto sw2 = __builtin_amdgcn_permlane32_swap(zb, zb, false, false);
        zs = __builtin_bit_cast(float, (unsigned)sw2[0]) + __builtin_bit_cast(float, (unsigned)sw2[1]);
      }
      const unsigned gm = g1 ? 0xFFFFFFFFu : 0u;
      f32x4 lo, hi;
#pragma unroll
      for (int j = 0; j < 4; ++j) { lo[j] = bsel(gm, kv[j], kv[8 + j]); hi[j] = bsel(gm, kv[4 + j], kv[12 + j]); }
      const int hh = 2 * wc + g1;
      const unsigned a = SCR + (4 * wr + hh) * 1024 + (h * 16 + (lane & 15)) * 16;
      *(LAS f32x4*)(L + a) = lo;
      *(LAS f32x4*)(L + a + 512) = hi;
      if (h == 0) *(LAS float*)(L + SCR + (8 + hh) * 1024 + wr * 64 + (lane & 15) * 4) = zs;
    }
    WAITL0();
    BAR();
    f32x16 qa = {}, qb = {};
    {
      unsigned wq0 = WQ + wn0, wq1 = WQ + (wn0 ^ 64);
      asm volatile("" : "+v"(wq0), "+v"(wq1));
      bf16x8 a0[3], a1[3];
      a0[0] = ldstr(L, wq0, wq0 + 512); a1[0] = ldstr(L, wq1, wq1 + 512);
      a0[1] = ldstr(L, wq0 + 2048, wq0 + 2048 + 512); a1[1] = ldstr(L, wq1 + 2048, wq1 + 2048 + 512);
#pragma unroll
      for (int t = 0; t < 32; ++t) {
        const unsigned o = (t + 2) * 2048;
        SB();
        qa = mfma(a0[t % 3], xr[t], qa);
        SB();
        if (t + 2 < 32) a0[(t + 2) % 3] = ldstr(L, wq0 + o, wq0 + o + 512);
        SB();
        qb = mfma(a1[t % 3], xr[t], qb);
        SB();
        if (t + 2 < 32) a1[(t + 2) % 3] = ldstr(L, wq1 + o, wq1 + o + 512);
      }
      SB();
    }
    bf16x8 pq[4];
    pq[0] = phi8(qa, 0); pq[1] = phi8(qa, 1); pq[2] = phi8(qb, 0); pq[3] = phi8(qb, 1);
    asm volatile("" : "+v"(pq[0]), "+v"(pq[1]), "+v"(pq[2]), "+v"(pq[3]));
    WAITL0();
    BAR();
    {
      const unsigned st = 4 * ti + own;
      bf16x8 af[4];
#pragma unroll
      for (int hh = 0; hh < 4; ++hh) af[hh] = ldsr128(L, AFR + hh * 1024 + lane * 16);
      SB();
      f32x16 d[4];
#pragma unroll
      for (int hh = 0; hh < 4; ++hh) { d[hh] = f32x16{}; d[hh] = mfma(af[hh], pq[hh], d[hh]); }
      SB();
#pragma unroll
      for (int hh = 0; hh < 4; ++hh) {
        const float inv = __builtin_amdgcn_rcpf(d[hh][8] + 1e-6f);
        const u32x4 o = __builtin_bit_cast(u32x4, pack8(d[hh][0] * inv, d[hh][1] * inv, d[hh][2] * inv, d[hh][3] * inv, d[hh][4] * inv, d[hh][5] * inv, d[hh][6] * inv, d[hh][7] * inv));
        st128<16>(o, rsWS, lane * 16 + WS_O + (st * 64 + 4 * g + hh) * 1024);
      }
    }
    WAITV(0);
    if (lane == 0) st_flag(ws, WS_FLAGB + (4 * ti + own) * 128 + g * 8, tag);
    BAR();
    return;
  }

  const int hw = wave - 4;
  const unsigned long long epoch_raw = epoch_issue(wsp);
  __builtin_amdgcn_s_setprio(HPRIO);
  const int ro = 2 * ((ti + g) & 3);
  const u16* w = (g >= 8) ? wB : wA;
  const rsrc_t rsX = __builtin_amdgcn_make_buffer_rsrc((void*)x, 0, S_ * D_ * 2, 0x00020000);
  const rsrc_t rsW = __builtin_amdgcn_make_buffer_rsrc((void*)w, 0, D_ * 3 * D_ * 2, 0x00020000);
  const int rin = lane >> 3, ch = lane & 7;
  const unsigned vx = rin * 1024 + ((ch ^ (4 * (hw & 1) + (rin >> 1))) << 4);
  const unsigned csw = (ch ^ (((rin >> 1) & 1) << 2)) << 4;
  const unsigned vw = rin * 3072 + csw;
  const unsigned sx0 = (ti * 128 + 8 * hw) * 1024;
  const unsigned sw0 = 8 * hw * 3072 + gg * 128;
  auto stage = [&](int s) {
    const unsigned ks = (s + ro) & 7, slot = (s % 3) * 16384;
#pragma unroll
    for (int i = 0; i < 4; ++i) glds(vx, rsX, sx0 + ks * 128 + i * 32768, XR + slot + (hw + 4 * i) * 1024);
#pragma unroll
    for (int i = 0; i < 2; ++i) glds(vw, rsW, sw0 + ks * 196608 + i * 98304 + 1024, WR + slot + (hw + 4 * i) * 1024);
#pragma unroll
    for (int i = 0; i < 2; ++i) glds(vw, rsW, sw0 + ks * 196608 + i * 98304 + 2048, WR + slot + 8192 + (hw + 4 * i) * 1024);
  };
  auto stageq = [&](int s) {
    const unsigned ks = (s + ro) & 7;
#pragma unroll
    for (int i = 0; i < 2; ++i) glds(vw, rsW, sw0 + ks * 196608 + i * 98304, WQ + s * 8192 + (hw + 4 * i) * 1024);
  };
  unsigned wtouch = 0;
  auto wo_touch = [&]() {
    const unsigned row = r * 32 + hw * 8 + (lane >> 3);
    const char* a = (const char*)((row & 512) ? woB : woA) + (row & 511) * 1024 + (lane & 7) * 128;
    asm volatile("global_load_dword %0, %1, off" : "=v"(wtouch) : "v"(a) : "memory");
  };
  stage(0);
  stage(1);
  WAITV(8);
  const unsigned long long tag = epoch_wait(epoch_raw);
  BAR();
  const rsrc_t rsG = __builtin_amdgcn_make_buffer_rsrc((void*)gw, 0, D_ * 2, 0x00020000);
  const unsigned grow = ti * 128 + 8 * g + 2 * hw;
  u32x4 x0, x1, gv;
  const unsigned foff = WS_FLAGC + (g * 4 + hw) * 128 + (lane & 15) * 8;
  u32x2 fcv;
#pragma unroll
  for (int s = 1; s < 8; ++s) {
    if (s < 6) { stage(s + 1); WAITV(8); }
    else if (s == 6) {
      stage(7);
      asm volatile("buffer_load_dwordx4 %0, %4, %5, %7 offen\n\tbuffer_load_dwordx4 %1, %4, %5, %7 offen offset:1024\n\tbuffer_load_dwordx4 %2, %4, %6, 0 offen\n\t"
                   "global_load_dwordx2 %3, %8, %9 sc1"
                   : "=&v"(x0), "=&v"(x1), "=&v"(gv), "=&v"(fcv) : "v"(lane * 16), "s"(rsX), "s"(rsG), "s"(grow * 1024), "v"(foff), "s"(wsp) : "memory");
      WAITV(12);
    } else {
      stageq(0); stageq(1); stageq(2); stageq(3);
      WAITV(12);
    }
    BAR();
  }
  stageq(4); stageq(5); stageq(6); stageq(7);
  const rsrc_t rsWS = __builtin_amdgcn_make_buffer_rsrc((void*)wsp, 0, WS_END, 0x00020000);
  const rsrc_t rsOA = __builtin_amdgcn_make_buffer_rsrc((void*)woA, 0, D_ * D_ * 2, 0x00020000);
  const rsrc_t rsOB = __builtin_amdgcn_make_buffer_rsrc((void*)woB, 0, D_ * D_ * 2, 0x00020000);
  const unsigned vo = rin * 1024 + csw;
  const unsigned hid = g * 4 + hw;
  wo_touch();
  asm volatile("s_waitcnt vmcnt(17)" : "+v"(x0), "+v"(x1), "+v"(gv), "+v"(fcv) :: "memory");
  if (!__all((((unsigned long long)fcv[1] << 32) | fcv[0]) == tag)) wait_flags(ws, foff, tag);
  float gval;
  {
    float a0 = 0.f, a1 = 0.f;
#pragma unroll
    for (int i = 0; i < 4; ++i) {
      const unsigned ug = gv[i], u0 = x0[i], u1 = x1[i];
      const float gl = __builtin_bit_cast(float, ug << 16), gh = __builtin_bit_cast(float, ug & 0xffff0000u);
      a0 = ffma(__builtin_bit_cast(float, u0 << 16), gl, a0); a0 = ffma(__builtin_bit_cast(float, u0 & 0xffff0000u), gh, a0);
      a1 = ffma(__builtin_bit_cast(float, u1 << 16), gl, a1); a1 = ffma(__builtin_bit_cast(float, u1 & 0xffff0000u), gh, a1);
    }
    a0 = wave_sum63(a0); a1 = wave_sum63(a1);
    const float s0 = __builtin_bit_cast(float, __builtin_amdgcn_readlane(__builtin_bit_cast(int, a0), 63));
    const float s1 = __builtin_bit_cast(float, __builtin_amdgcn_readlane(__builtin_bit_cast(int, a1), 63));
    const float lg = (lane & 1) ? s1 : s0;
    gval = __builtin_amdgcn_rcpf(1.0f + __builtin_amdgcn_exp2f(-1.44269504088896340736f * lg));
  }
  asm volatile("s_waitcnt vmcnt(1)" : "+v"(gval) :: "memory");
  BAR();
  {
    const f32x4 p0 = *(LAS f32x4*)(L + SCR + hw * 1024 + lane * 16), p1 = *(LAS f32x4*)(L + SCR + (4 + hw) * 1024 + lane * 16);
    const f32x4 z0 = *(LAS f32x4*)(L + SCR + (8 + hw) * 1024 + (lane & 3) * 16), z1 = *(LAS f32x4*)(L + SCR + (8 + hw) * 1024 + 64 + (lane & 3) * 16);
    const unsigned po = WS_PART + (hid * 16 + ti) * PART_STRIDE;
    const f32x4 ps = fadd4(p0, p1);
    const unsigned d0 = __builtin_bit_cast(unsigned, cvt2(ps[0], ps[1])), d1 = __builtin_bit_cast(unsigned, cvt2(ps[2], ps[3]));
    const auto e0 = __builtin_amdgcn_permlane32_swap(d0, d0, false, false), e1 = __builtin_amdgcn_permlane32_swap(d1, d1, false, false);
    const u32x4 kvp = {e0[0], e1[0], e0[1], e1[1]};
    const u32x4 zp = __builtin_bit_cast(u32x4, fadd4(z0, z1));
    u32x4 pay;
#pragma unroll
    for (int i = 0; i < 4; ++i) pay[i] = nosent(lane < 32 ? kvp[i] : zp[i]);
    if (lane < 36) st128<16>(pay, rsWS, lane * 16 + po);
    if (lane < 2) __hip_atomic_store((GAS unsigned*)(ws + WS_GATE) + grow + lane, __builtin_bit_cast(unsigned, gval), __ATOMIC_RELAXED, __HIP_MEMORY_SCOPE_AGENT);
  }
#pragma unroll
  for (int i = 0; i < 16; ++i) glds(vo, rsOA, (hw + 4 * i) * 8192 + ct * 128, WOA + (hw + 4 * i) * 1024);
  const unsigned pb = WS_PART + hid * 16 * PART_STRIDE;
  for (int i = 0; i < PDELAY1; ++i) __builtin_amdgcn_s_sleep(4);
  {
    float a[8];
    f32x4 z;
    u32x4 pv[8], zv;
    auto gather = [&]() {
#pragma unroll
      for (int i = 0; i < 8; ++i) pv[i] = __builtin_amdgcn_raw_buffer_load_b128(rsWS, (lane >> 5) * PART_STRIDE + (lane & 31) * 16, pb + 2 * i * PART_STRIDE, 16);
      zv = __builtin_amdgcn_raw_buffer_load_b128(rsWS, (lane >> 2) * PART_STRIDE + 512 + (lane & 3) * 16, pb, 16);
    };
    auto reduce = [&]() {
      unsigned clo = 0x00003F80u, chi = 0x3F800000u;
      asm volatile("" : "+s"(clo), "+s"(chi));
      const bf16x2 lo1 = __builtin_bit_cast(bf16x2, clo), hi1 = __builtin_bit_cast(bf16x2, chi);
      unsigned mx = 0;
#pragma unroll
      for (int k = 0; k < 8; ++k) a[k] = 0.f;
#pragma unroll
      for (int i = 0; i < 8; ++i) {
#pragma unroll
        for (int k = 0; k < 4; ++k) {
          const unsigned u = pv[i][k];
          a[2 * k] = __builtin_amdgcn_fdot2_f32_bf16(__builtin_bit_cast(bf16x2, u), lo1, a[2 * k], false);
          a[2 * k + 1] = __builtin_amdgcn_fdot2_f32_bf16(__builtin_bit_cast(bf16x2, u), hi1, a[2 * k + 1], false);
        }
        mx = umax3(pv[i][0], pv[i][1], umax3(pv[i][2], pv[i][3], mx));
      }
      mx = umax3(zv[0], zv[1], umax3(zv[2], zv[3], mx));
      z = __builtin_bit_cast(f32x4, zv);
      return __all(mx != SENT);
    };
    gather();
    asm volatile("" ::: "memory");
    if (!reduce()) {
      for (unsigned spins = 0; spins < SPIN_CAP; ++spins) { gather(); if (reduce()) break; }
    }
#pragma unroll
    for (int k = 0; k < 8; ++k) {
      const unsigned u = __builtin_bit_cast(unsigned, a[k]);
      const auto t = __builtin_amdgcn_permlane32_swap(u, u, false, false);
      a[k] = __builtin_bit_cast(float, (unsigned)t[0]) + __builtin_bit_cast(float, (unsigned)t[1]);
    }
    z = f32x4{sum_stride4(z[0]), sum_stride4(z[1]), sum_stride4(z[2]), sum_stride4(z[3])};
    const int slot = lane & 31;
    if (lane < 32) *(LAS bf16x8*)(L + AFR + hw * 1024 + ((slot >> 4) * 32 + (slot & 15)) * 16) = pack8(a[0], a[1], a[2], a[3], a[4], a[5], a[6], a[7]);
    const int q4 = lane & 3;
    u32x2 kz = {__builtin_bit_cast(unsigned, cvt2(z[0], z[1])), __builtin_bit_cast(unsigned, cvt2(z[2], z[3]))};
    *(LAS u32x2*)(L + AFR + hw * 1024 + ((q4 & 1) * 32 + 16 + (lane >> 2)) * 16 + (q4 >> 1) * 8) = kz;
  }
  WAITV(0);
  asm volatile("" :: "v"(wtouch));
  WAITL0();
  BAR();
#pragma unroll
  for (int i = 0; i < 16; ++i) glds(vo, rsOB, (hw + 4 * i) * 8192 + ct * 128, WOB + (hw + 4 * i) * 1024);
  WAITV(0);
  BAR();
  {
    const int r32 = lane & 31, h = lane >> 5, g1 = (lane >> 4) & 1, q = (lane & 15) >> 2, p = lane & 3;
    const unsigned sw = ((2 * g1 + (p >> 1)) ^ (((q >> 1) & 1) << 2)) << 4;
    const int br2 = hw >> 1, sh = hw & 1;
    const unsigned st2 = 2 * rt + sh;
    const unsigned ob = WS_O + (st2 * 64 + 32 * br2) * 1024;
    for (int i = 0; i < PDELAY; ++i) __builtin_amdgcn_s_sleep(8);
    {
      const unsigned foffb = WS_FLAGB + st2 * 128 + (lane & 15) * 8;
      for (unsigned spins = 0; spins < SPIN_CAP; ++spins) {
        if (__all(ld_flag(ws, foffb) == tag)) break;
        __builtin_amdgcn_s_sleep(PBACK);
      }
      asm volatile("" ::: "memory");
    }
    unsigned wp0 = ((br2 ? WOB : WOA) + (4 * h + q) * 128 + sw + (p & 1) * 8) ^ (br2 << 6), wp1 = wp0 ^ 64;
    asm volatile("" : "+v"(wp0), "+v"(wp1));
    f32x16 c0 = {}, c1 = {};
    const unsigned gvb = ld_u32(ws, WS_GATE + (st2 * 32 + r32) * 4);
    {
      u32x4 of[32];
#pragma unroll
      for (int j = 0; j < OD; ++j) {
        of[j] = __builtin_amdgcn_raw_buffer_load_b128(rsWS, lane * 16, ob + j * 1024, 16);
        SB();
      }
      bf16x8 a0[3], a1[3];
      a0[0] = ldstr(L, wp0, wp0 + 1024); a1[0] = ldstr(L, wp1, wp1 + 1024);
      a0[1] = ldstr(L, wp0 + 2048, wp0 + 2048 + 1024); a1[1] = ldstr(L, wp1 + 2048, wp1 + 2048 + 1024);
#pragma unroll
      for (int j = 0; j < 32; ++j) {
        const unsigned o = (j + 2) * 2048;
        const bf16x8 bf = __builtin_bit_cast(bf16x8, of[j]);
        SB();
        c0 = mfma(a0[j % 3], bf, c0);
        SB();
        if (j + OD < 32) of[j + OD] = __builtin_amdgcn_raw_buffer_load_b128(rsWS, lane * 16, ob + (j + OD) * 1024, 16);
        if (j + 2 < 32) a0[(j + 2) % 3] = ldstr(L, wp0 + o, wp0 + o + 1024);
        SB();
        c1 = mfma(a1[j % 3], bf, c1);
        SB();
        if (j + 2 < 32) a1[(j + 2) % 3] = ldstr(L, wp1 + o, wp1 + o + 1024);
      }
      SB();
    }
    __builtin_amdgcn_s_setprio(0);
    const float gv = __builtin_bit_cast(float, gvb);
    const float gs = br2 ? 1.0f - gv : gv;
#pragma unroll
    for (int qq = 0; qq < 4; ++qq) {
      const f32x4 v = {c1[4 * qq] * gs, c1[4 * qq + 1] * gs, c1[4 * qq + 2] * gs, c1[4 * qq + 3] * gs};
      *(LAS f32x4*)(L + EXC + hw * 4096 + qq * 1024 + lane * 16) = v;
    }
    WAITL0();
    BAR();
    if (b == 0 && hw == 0 && lane == 0) st_flag(ws, WS_EPOCH, tag);
    unsigned pk[8];
#pragma unroll
    for (int qq = 0; qq < 4; ++qq) {
      const f32x4 v = *(LAS f32x4*)(L + EXC + (hw ^ 2) * 4096 + qq * 1024 + lane * 16);
      pk[2 * qq] = __builtin_bit_cast(unsigned, cvt2(c0[4 * qq] * gs + v[0], c0[4 * qq + 1] * gs + v[1]));
      pk[2 * qq + 1] = __builtin_bit_cast(unsigned, cvt2(c0[4 * qq + 2] * gs + v[2], c0[4 * qq + 3] * gs + v[3]));
    }
    const rsrc_t rsO = __builtin_amdgcn_make_buffer_rsrc((void*)out, 0, S_ * D_ * 2, 0x00020000);
    const unsigned orow = (st2 * 32 + r32) * 1024 + (64 * ct + 32 * br2) * 2 + h * 16;
#pragma unroll
    for (int qq = 0; qq < 4; qq += 2) {
      const auto s0 = __builtin_amdgcn_permlane32_swap(pk[2 * qq], pk[2 * qq + 2], false, false);
      const auto s1 = __builtin_amdgcn_permlane32_swap(pk[2 * qq + 1], pk[2 * qq + 3], false, false);
      const u32x4 v = {s0[0], s1[0], s0[1], s1[1]};
      st128<0>(v, rsO, orow + qq * 16);
    }
  }
}

extern "C" void kernel_launch(void* const* d_in, const int* in_sizes, int n_in, void* d_out, int out_size, void* d_ws, size_t ws_size,
                              hipStream_t stream) {
  (void)in_sizes; (void)n_in; (void)out_size;
  const u16* x = (const u16*)d_in[0];
  const u16* wA = (const u16*)d_in[1];
  const u16* woA = (const u16*)d_in[2];
  const u16* wB = (const u16*)d_in[4];
  const u16* woB = (const u16*)d_in[5];
  const u16* gw = (const u16*)d_in[6];
  (void)hipFuncSetAttribute((const void*)gated_mix_fused, hipFuncAttributeMaxDynamicSharedMemorySize, LDS_BYTES);
  gated_mix_fused<<<256, 512, LDS_BYTES, stream>>>(x, wA, wB, woA, woB, gw, (u16*)d_out, (char*)d_ws + ((ws_size / 2) & ~(size_t)0x1FFFFF));
}
```
